```python
import torch

def _softmax(x, axis=-1):
    return torch.softmax(x, dim=axis)
def _gelu(x):
    return torch.nn.functional.gelu(x, approximate="tanh")
def _silu(x):
    return torch.nn.functional.silu(x)
def _sigmoid(x):
    return torch.sigmoid(x)
def _layernorm(x, axis=-1, eps=1e-5):
    m = x.mean(dim=axis, keepdim=True)
    v = x.var(dim=axis, keepdim=True, unbiased=False)
    return (x - m) / torch.sqrt(v + eps)
def _scatter_add(tgt, idx, src, axis=0):
    return tgt.clone().index_add_(axis, idx, src)
def _segment_sum(src, seg, n):
    out = torch.zeros((n,) + src.shape[1:], dtype=src.dtype)
    return out.index_add_(0, seg, src)
def _pad(x, pw):
    flat = tuple(v for pair in reversed(pw) for v in pair)
    return torch.nn.functional.pad(x, flat)

kernel_name = 'concat_heads__grouped_kv_mqa__ssm_conv_s4__prompt'

def setup_inputs():

    _w = (lambda a: (lambda t: t.to(torch.bfloat16) if t.is_floating_point() else t)(torch.as_tensor(a)))
    def _A_setup():
        B, S, D, H = 1, 2048, 576, 32
        Dh = D // H
        return {
            "x": _w((lambda *s: torch.randn(*s, dtype=torch.bfloat16) * 0.05)(B, S, D)),
            "wq": _w((lambda *s: torch.randn(*s, dtype=torch.bfloat16) * 0.8)(D, H * Dh)),
            "wk": _w((lambda *s: torch.randn(*s, dtype=torch.bfloat16) * 0.8)(D, Dh)),
            "wv": _w((lambda *s: torch.randn(*s, dtype=torch.bfloat16) * 0.05)(D, Dh)),
            "wo": _w((lambda *s: torch.randn(*s, dtype=torch.bfloat16) * 0.05)(H * Dh, D)),
        }
    _A_in = _A_setup()

    def _B_setup():
        B, L, D, N = 1, 2048, 576, 128
        A_re = -(lambda _x: torch.exp(torch.as_tensor(_x)))((lambda *s: torch.randn(*s, dtype=torch.bfloat16) * 6.4)(D, N))
        A_im = torch.pi * (lambda *s: torch.randn(*s, dtype=torch.bfloat16) * 0.05)(D, N)
        B_re = (lambda *s: torch.randn(*s, dtype=torch.bfloat16) * 0.05)(D, N)
        B_im = (lambda *s: torch.randn(*s, dtype=torch.bfloat16) * 0.05)(D, N)
        C_re = (lambda *s: torch.randn(*s, dtype=torch.bfloat16) * 0.05)(D, N)
        C_im = (lambda *s: torch.randn(*s, dtype=torch.bfloat16) * 0.05)(D, N)
        log_dt = (lambda *s: torch.randn(*s, dtype=torch.bfloat16) * 0.05)(D) * 0.5 - 4.0
        Dskip = (lambda *s: torch.randn(*s, dtype=torch.bfloat16) * 0.05)(D)
        return {
            "u": _w((lambda *s: torch.randn(*s, dtype=torch.bfloat16) * 0.05)(B, L, D)),
            "A_re": _w(A_re),
            "A_im": _w(A_im),
            "B_re": _w(B_re),
            "B_im": _w(B_im),
            "C_re": _w(C_re),
            "C_im": _w(C_im),
            "log_dt": _w(log_dt),
            "Dskip": _w(Dskip),
        }
    _B_in = _B_setup()

    return {
        "A_x": _A_in["x"],
        "A_wq": _A_in["wq"],
        "A_wk": _A_in["wk"],
        "A_wv": _A_in["wv"],
        "A_wo": _A_in["wo"],
        "B_u": _B_in["u"],
        "B_A_re": _B_in["A_re"],
        "B_A_im": _B_in["A_im"],
        "B_B_re": _B_in["B_re"],
        "B_B_im": _B_in["B_im"],
        "B_C_re": _B_in["C_re"],
        "B_C_im": _B_in["C_im"],
        "B_log_dt": _B_in["log_dt"],
        "B_Dskip": _B_in["Dskip"],
    }

def reference(A_x, A_wq, A_wk, A_wv, A_wo, B_u, B_A_re, B_A_im, B_B_re, B_B_im, B_C_re, B_C_im, B_log_dt, B_Dskip):
    def _A_ref(x, wq, wk, wv, wo):
        B, S, D = x.shape
        H = 32
        Dh = D // H
        q = (lambda x, *d: x.permute(*d))((x @ wq).reshape(B, S, H, Dh), 0, 2, 1, 3)
        k = x @ wk
        v = x @ wv
        scores = torch.einsum("bhqd,bkd->bhqk", q, k) * (Dh ** -0.5)
        p = _softmax(scores, axis=-1)
        o = torch.einsum("bhqk,bkd->bhqd", p, v)
        return (lambda x, *d: x.permute(*d))(o, 0, 2, 1, 3).reshape(B, S, H * Dh) @ wo

    def _B_ref(u, A_re, A_im, B_re, B_im, C_re, C_im, log_dt, Dskip):
        Bsz, L, D = u.shape
        dt = torch.exp(log_dt)[:, None]
        A = A_re + 1j * A_im
        Bc = B_re + 1j * B_im
        Cc = C_re + 1j * C_im
        logdA = dt * A
        dB = dt * Bc
        l = (lambda x, dt: x.to(dt))(torch.arange(L), A_re.dtype)
        P = torch.exp(logdA[:, :, None] * l[None, None, :])
        K = torch.einsum("dn,dnl,dn->dl", Cc, P, dB).real
        L2 = 2 * L
        Kp = _pad(K, ((0, 0), (0, L)))
        up = (lambda x, *d: x.permute(*d))(u, 0, 2, 1)
        up = _pad(up, ((0, 0), (0, 0), (0, L)))
        Kf = (lambda x, axes=(-2,-1): torch.fft.rfft2(x.float(), dim=axes))(Kp[:, None, :], axes=(-2, -1))
        Uf = (lambda x, axes=(-2,-1): torch.fft.rfft2(x.float(), dim=axes))(up[:, :, None, :], axes=(-2, -1))
        Yf = Uf * Kf[None, :, :, :]
        y = (lambda x, s, axes=(-2,-1): torch.fft.irfft2(x, s=s, dim=axes).to(torch.bfloat16))(Yf, (1, L2), axes=(-2, -1))[:, :, 0, :L]
        y = (lambda x, *d: x.permute(*d))(y, 0, 2, 1) + u * Dskip[None, None, :]
        return y

    a = _A_ref(A_x, A_wq, A_wk, A_wv, A_wo)
    b = _B_ref(B_u, B_A_re, B_A_im, B_B_re, B_B_im, B_C_re, B_C_im, B_log_dt, B_Dskip)
    return torch.concatenate([a, b], axis=-1)
```

```cpp
#include <hip/hip_runtime.h>
#include <stdint.h>

typedef __bf16 bf16x2 __attribute__((ext_vector_type(2)));
typedef __bf16 bf16x8 __attribute__((ext_vector_type(8)));
typedef short s16x4 __attribute__((ext_vector_type(4)));
typedef float f32x2 __attribute__((ext_vector_type(2)));
typedef float f32x4 __attribute__((ext_vector_type(4)));
typedef float f32x16 __attribute__((ext_vector_type(16)));
typedef unsigned u32x2 __attribute__((ext_vector_type(2)));
typedef unsigned u32x4 __attribute__((ext_vector_type(4)));
typedef unsigned short u16;
#define DI __device__ __forceinline__
#define LDSP __attribute__((address_space(3)))
#define SBAR() __builtin_amdgcn_sched_barrier(0)
#define PIN(x) asm volatile("" : "+v"(x))
#define MFMA16(a, b, c) __builtin_amdgcn_mfma_f32_32x32x16_bf16(__builtin_bit_cast(bf16x8, a), __builtin_bit_cast(bf16x8, b), c, 0, 0, 0)
#define MFMA8(a, b, c) __builtin_amdgcn_mfma_f32_32x32x8bf16_1k(__builtin_bit_cast(s16x4, a), __builtin_bit_cast(s16x4, b), c, 0, 0, 0)

constexpr int SEQ = 2048, DM = 576, DH = 18, NST = 128;
constexpr int KV_TILE = 2368;
constexpr int KR_PAR = 4288, KR_CH = 2 * KR_PAR;
constexpr int UT_ROW = 2064;
constexpr size_t WS_Q = 0;
constexpr size_t WS_KV = WS_Q + (size_t)SEQ * DM * 2;
constexpr size_t WS_KMAX = WS_KV + 155648;
constexpr size_t WS_ZERO = WS_KMAX + 256;
constexpr size_t WS_O = WS_ZERO + 256;
constexpr size_t WS_KR = WS_O + (size_t)SEQ * DM * 2;
constexpr size_t WS_UT = WS_KR + (size_t)DM * KR_CH;
constexpr size_t WS_YT = WS_UT + (size_t)DM * UT_ROW * 2;

DI unsigned cvtpk(float a, float b) { f32x2 v = {a, b}; return __builtin_bit_cast(unsigned, __builtin_convertvector(v, bf16x2)); }
DI float bflo(unsigned u) { return __uint_as_float(u << 16); }
DI float bfhi(unsigned u) { return __uint_as_float(u & 0xffff0000u); }
DI float bf2f(u16 x) { return __uint_as_float((unsigned)x << 16); }
DI float rbf(float x) { return bflo(cvtpk(x, x)); }
DI int crow(int reg, int h) { return (reg & 3) + 8 * (reg >> 2) + 4 * h; }
DI void glds16(const void* g, unsigned lds_base) {
  unsigned sv;
  asm volatile("s_mov_b32 %0, m0\n\ts_mov_b32 m0, %2\n\ts_nop 0\n\tglobal_load_lds_dwordx4 %1, off\n\ts_mov_b32 m0, %0"
               : "=&s"(sv) : "v"(g), "s"(lds_base) : "memory");
}
#define WAITV(n) asm volatile("s_waitcnt vmcnt(" #n ")" ::: "memory")
#define WAITV_BAR(n) asm volatile("s_waitcnt vmcnt(" #n ") lgkmcnt(0)\n\ts_barrier" ::: "memory")
#define BAR() asm volatile("s_waitcnt lgkmcnt(0)\n\ts_barrier" ::: "memory")
DI unsigned rfl(unsigned x) { return __builtin_amdgcn_readfirstlane(x); }
template <typename T> DI T ldsr(const char* lds, unsigned off) { return *(const LDSP T*)((const LDSP char*)lds + off); }
template <typename T> DI void ldsw(char* lds, unsigned off, T v) { *(LDSP T*)((LDSP char*)lds + off) = v; }
DI s16x4 ldstr(const char* lds, unsigned off) {
  typedef short v4i16_t __attribute__((ext_vector_type(4)));
  return __builtin_bit_cast(s16x4, __builtin_amdgcn_ds_read_tr16_b64_v4i16((LDSP v4i16_t*)((LDSP char*)lds + off)));
}
DI float xhalf(float v) {
  return __shfl_xor(v, 32);
}

DI void gemm_stage(const u16* X, const u16* W, int s, unsigned lds0, int w, int lane) {
  const unsigned sb = lds0 + (s & 3) * 16384;
#pragma unroll
  for (int i = 0; i < 2; ++i) {
    const int row = 8 * (2 * w + i) + (lane >> 3), c = lane & 7;
    glds16(X + (size_t)row * DM + 64 * s + 8 * (c ^ ((row >> 1) & 7)), rfl(sb + (2 * w + i) * 1024));
  }
#pragma unroll
  for (int i = 0; i < 2; ++i) {
    const int row = 8 * (2 * w + i) + (lane >> 3), c = lane & 7;
    glds16(W + (size_t)(64 * s + row) * DM + 8 * (c ^ (((row >> 1) & 1) << 2)), rfl(sb + 8192 + (2 * w + i) * 1024));
  }
}
DI f32x16 gemm64(const u16* X, const u16* W, char* lds, int w, int lane) {
  const unsigned lds0 = (unsigned)(uintptr_t)lds;
  const int r = lane & 31, h = lane >> 5, wn = w & 1, wp = w >> 1;
  gemm_stage(X, W, 0, lds0, w, lane);
  gemm_stage(X, W, 1, lds0, w, lane);
  gemm_stage(X, W, 2, lds0, w, lane);
  f32x16 acc = {};
  const int prow = 32 * wp + r;
  const unsigned xoff = prow * 128;
  const int xs = (prow >> 1) & 7;
  const int blk = (lane >> 4) & 1, q = (lane & 15) >> 2, p = lane & 3;
#pragma unroll
  for (int s = 0; s < 9; ++s) {
    if (s <= 6) { WAITV_BAR(8); } else if (s == 7) { WAITV_BAR(4); } else { WAITV_BAR(0); }
    if (s + 3 < 9) gemm_stage(X, W, s + 3, lds0, w, lane);
    const unsigned sb = (s & 3) * 16384;
#pragma unroll
    for (int ks = 0; ks < 4; ++ks) {
      const u32x4 b = ldsr<u32x4>(lds, sb + xoff + 16 * ((2 * ks + h) ^ xs));
      s16x4 a0, a1;
      {
        const int R0 = 16 * ks + 8 * h + q, R1 = R0 + 4;
        const unsigned cb = wn * 64 + blk * 32 + p * 8;
        a0 = ldstr(lds, sb + 8192 + R0 * 128 + (cb ^ (((R0 >> 1) & 1) << 6)));
        a1 = ldstr(lds, sb + 8192 + R1 * 128 + (cb ^ (((R1 >> 1) & 1) << 6)));
      }
      const auto a = __builtin_shufflevector(a0, a1, 0, 1, 2, 3, 4, 5, 6, 7);
      acc = MFMA16(a, b, acc);
    }
  }
  return acc;
}
DI void gemm64_store(const f32x16& acc, u16* C, int ld, int w, int lane) {
  const int r = lane & 31, h = lane >> 5, wn = w & 1, wp = w >> 1;
  u16* row = C + (size_t)(32 * wp + r) * ld + 32 * wn + 4 * h;
#pragma unroll
  for (int g = 0; g < 4; ++g) {
    u32x2 v = {cvtpk(acc[4 * g], acc[4 * g + 1]), cvtpk(acc[4 * g + 2], acc[4 * g + 3])};
    *(u32x2*)(row + 8 * g) = v;
  }
}

struct cpx { float r, i; };
DI cpx cmul(cpx a, cpx b) { return cpx{a.r * b.r - a.i * b.i, a.r * b.i + a.i * b.r}; }
DI cpx cexp_l(float zr, float zi, float l) {
  const float mag = __builtin_amdgcn_exp2f((zr * l) * 1.4426950408889634f);
  const float rev = __builtin_amdgcn_fractf((zi * l) * 0.15915494309189535f);
  return cpx{mag * __builtin_amdgcn_cosf(rev), mag * __builtin_amdgcn_sinf(rev)};
}
constexpr int GEN_PITCH = 272, GEN_X = 0, GEN_Y = 32 * GEN_PITCH, GEN_WAVE = 14592;

DI void gen_block(int blk, const u16* Are, const u16* Aim, const u16* Bre, const u16* Bim, const u16* Cre, const u16* Cim,
                  const u16* logdt, char* ws, char* lds_all, int w, int lane) {
  const int d = 4 * blk + w, r = lane & 31, h = lane >> 5;
  char* lds = lds_all + w * GEN_WAVE;
  const float dt = rbf(expf(bf2f(logdt[d])));
  f32x16 acc = {};
#pragma unroll 1
  for (int pass = 0; pass < 2; ++pass) {
    const int idx = d * NST + 64 * pass + lane;
    const float zr = dt * bf2f(Are[idx]), zi = dt * bf2f(Aim[idx]);
    const float dBr = dt * bf2f(Bre[idx]), dBi = dt * bf2f(Bim[idx]);
    const float cr = bf2f(Cre[idx]), ci = bf2f(Cim[idx]);
    const cpx c = cpx{cr * dBr - ci * dBi, cr * dBi + ci * dBr};
    const cpx w1 = cexp_l(zr, zi, 1.f), w8 = cexp_l(zr, zi, 8.f), w256 = cexp_l(zr, zi, 256.f);
    cpx x = cpx{1.f, 0.f};
#pragma unroll
    for (int m = 0; m < 32; ++m) {
      ldsw<unsigned>(lds, GEN_X + m * GEN_PITCH + 4 * lane, cvtpk(x.r, x.i));
      if (m < 31) x = (m == 0) ? w8 : cmul(x, w8);
    }
#define PUTY(col, v) ldsw<unsigned>(lds, GEN_Y + (col) * GEN_PITCH + 4 * lane, cvtpk((v).r, -(v).i))
    cpx y[8];
    y[0] = c;
#pragma unroll
    for (int i = 1; i < 8; ++i) y[i] = cmul(y[i - 1], w1);
#pragma unroll
    for (int i = 0; i < 8; ++i) PUTY(i, y[i]);
#pragma unroll
    for (int i = 0; i < 4; ++i) { const cpx t = cmul(y[2 * i], w256); PUTY(8 + i, t); }
    const cpx p2 = cmul(w256, w256), p3 = cmul(p2, w256), p4 = cmul(p2, p2);
    { cpx t = cmul(y[0], p2); PUTY(12, t); t = cmul(y[4], p2); PUTY(13, t); }
    { cpx t = cmul(y[0], p3); PUTY(14, t); t = cmul(y[4], p3); PUTY(15, t); }
    { const cpx t4 = cmul(y[0], p4); PUTY(16, t4);
      cpx t = cmul(t4, w256); PUTY(17, t); t = cmul(t4, p2); PUTY(18, t); t = cmul(t4, p3); PUTY(19, t); t = cmul(t4, p4); PUTY(20, t); }
#undef PUTY
    asm volatile("s_waitcnt lgkmcnt(0)" ::: "memory");
    const int yr = r <= 20 ? r : r - 16;
#pragma unroll
    for (int ks = 0; ks < 8; ++ks) {
      const u32x4 a = ldsr<u32x4>(lds, GEN_X + r * GEN_PITCH + 32 * ks + 16 * h);
      const u32x4 b = ldsr<u32x4>(lds, GEN_Y + yr * GEN_PITCH + 32 * ks + 16 * h);
      acc = MFMA16(a, b, acc);
    }
    asm volatile("s_waitcnt lgkmcnt(0)" ::: "memory");
  }
  {
    const int c = r;
    const int hi = c < 8 ? c : c < 12 ? 256 + 2 * (c - 8) : c < 16 ? 512 + 256 * ((c - 12) >> 1) + 4 * ((c - 12) & 1) : 1024 + 256 * (c - 16);
    if (c < 20) {
#pragma unroll
      for (int i = 0; i < 16; ++i) ldsw<u16>(lds, 2 * (8 * crow(i, h) + hi), (u16)cvtpk(acc[i], acc[i]));
    } else if (c == 20 && h == 0) {
      ldsw<u16>(lds, 2 * 2048, (u16)cvtpk(acc[0], acc[0]));
    }
  }
  asm volatile("s_waitcnt lgkmcnt(0)" ::: "memory");
  unsigned* E = (unsigned*)(ws + WS_KR + (size_t)d * KR_CH);
  unsigned* Od = E + KR_PAR / 4;
  auto rd = [&](int l) -> unsigned { return ldsr<u16>(lds, 2 * l); };
  auto pk = [](unsigned lo, unsigned hi) -> unsigned { return lo | (hi << 16); };
  const int L = lane;
  {
    const unsigned e0 = rd(4 * L), e1 = rd(4 * L + 1), e2 = rd(4 * L + 2), e3 = rd(4 * L + 3);
    const unsigned pv = L ? rd(4 * L - 1) : 0u;
    const int p0 = 2 * L;
    *(u32x2*)(E + 1028 - p0) = u32x2{pk(e3, e2), pk(e1, e0)};
    *(u32x2*)(Od + 1028 - p0) = u32x2{pk(e2, e1), pk(e0, pv)};
  }
  {
    const int j0 = 256 + 4 * L;
    const unsigned a = rd(j0), b = rd(j0 + 2), c = rd(j0 + 4), pv = L ? a : rd(255);
    const int p0 = j0 / 2;
    *(u32x2*)(E + 1028 - p0) = u32x2{pk(c, b), pk(a, a)};
    *(u32x2*)(Od + 1028 - p0) = u32x2{pk(b, a), pk(a, pv)};
  }
  {
    const int j0 = 512 + 8 * L;
    const unsigned a = rd(j0), b = rd(j0 + 4), c = rd(j0 + 8);
    const unsigned aa = pk(a, a), bb = pk(b, b), cc = pk(c, c);
    unsigned* e = E + 1026 - j0 / 2;
    unsigned* o = Od + 1026 - j0 / 2;
    *(u32x2*)(e) = u32x2{cc, bb};          *(u32x2*)(e + 2) = u32x2{pk(b, a), aa};
    *(u32x2*)(o) = u32x2{pk(c, b), bb};    *(u32x2*)(o + 2) = u32x2{aa, aa};
  }
  {
    const int j0 = 1024 + 16 * L;
    const unsigned a = rd(j0), b = rd(j0 + 8), c = rd(j0 + 16);
    const unsigned aa = pk(a, a), bb = pk(b, b), cc = pk(c, c);
    unsigned* e = E + 1022 - j0 / 2;
    unsigned* o = Od + 1022 - j0 / 2;
    *(u32x2*)(e) = u32x2{cc, cc};          *(u32x2*)(e + 2) = u32x2{bb, bb};
    *(u32x2*)(e + 4) = u32x2{bb, pk(b, a)}; *(u32x2*)(e + 6) = u32x2{aa, aa};
    *(u32x2*)(o) = u32x2{cc, pk(c, b)};    *(u32x2*)(o + 2) = u32x2{bb, bb};
    *(u32x2*)(o + 4) = u32x2{bb, aa};      *(u32x2*)(o + 6) = u32x2{aa, aa};
    if (L == 63) Od[5] = pk(0u, c);
  }
  if (L < 6) E[L] = 0u;
  if (L < 5) Od[L] = 0u;
  if (L < 42) { E[1030 + L] = 0u; Od[1030 + L] = 0u; }
}

DI void kv_block(int kt, const u16* x, const u16* wk, const u16* wv, char* ws, char* lds, int w, int lane) {
  const unsigned lds0 = (unsigned)(uintptr_t)lds;
  const int r = lane & 31, h = lane >> 5, isv = w >> 1, kh = w & 1;
  for (int i = w; i < 42; i += 4) {
    const int m = i >= 21, part = m ? i - 21 : i;
    const int ch = part * 64 + lane;
    if (ch < 1296) glds16((const char*)(m ? wv : wk) + 16 * ch, rfl(lds0 + m * 20736 + part * 1024));
  }
  const u16* xr = x + (size_t)(32 * kt + r) * DM + 288 * kh + 8 * h;
  u32x4 xf[18];
#pragma unroll
  for (int ks = 0; ks < 18; ++ks) xf[ks] = *(const u32x4*)(xr + 16 * ks);
  WAITV_BAR(0);
  const int n = r < DH ? r : DH - 1;
  const unsigned wb = isv * 20736 + 2 * ((288 * kh + 8 * h) * DH + n);
  f32x16 acc = {};
#pragma unroll
  for (int ks = 0; ks < 18; ++ks) {
    u32x4 wf;
#pragma unroll
    for (int j = 0; j < 4; ++j) {
      const unsigned lo = ldsr<u16>(lds, wb + 2 * DH * (16 * ks + 2 * j)), hi = ldsr<u16>(lds, wb + 2 * DH * (16 * ks + 2 * j + 1));
      wf[j] = lo | (hi << 16);
    }
    if (isv) acc = MFMA16(xf[ks], wf, acc); else acc = MFMA16(wf, xf[ks], acc);
  }
  BAR();
  if (kh) {
#pragma unroll
    for (int g = 0; g < 4; ++g) ldsw<f32x4>(lds, isv * 4096 + g * 1024 + lane * 16, f32x4{acc[4 * g], acc[4 * g + 1], acc[4 * g + 2], acc[4 * g + 3]});
  }
  BAR();
  if (kh) return;
#pragma unroll
  for (int g = 0; g < 4; ++g) {
    const f32x4 t = ldsr<f32x4>(lds, isv * 4096 + g * 1024 + lane * 16);
#pragma unroll
    for (int e = 0; e < 4; ++e) acc[4 * g + e] += t[e];
  }
  char* img = ws + WS_KV + (size_t)kt * KV_TILE;
  if (!isv) {
    const float sc = 0.23570226039551584f * 1.4426950408889634f;
    const int e = (r & 0x13) | ((r & 4) << 1) | ((r & 8) >> 1);
    const unsigned d0 = cvtpk(acc[0] * sc, acc[1] * sc), d1 = cvtpk(acc[2] * sc, acc[3] * sc);
    const unsigned d2 = cvtpk(acc[4] * sc, acc[5] * sc), d3 = cvtpk(acc[6] * sc, acc[7] * sc);
    const unsigned d4 = h ? 0u : cvtpk(acc[8] * sc, acc[9] * sc);
    *(u32x2*)(img + e * 16 + 8 * h) = u32x2{d0, d1};
    *(u32x2*)(img + 512 + e * 16 + 8 * h) = u32x2{d2, d3};
    if (!h) *(unsigned*)(img + 1024 + e * 4) = d4;
    float n2 = bflo(d0) * bflo(d0) + bfhi(d0) * bfhi(d0) + bflo(d1) * bflo(d1) + bfhi(d1) * bfhi(d1) + bflo(d2) * bflo(d2) + bfhi(d2) * bfhi(d2) +
               bflo(d3) * bflo(d3) + bfhi(d3) * bfhi(d3) + bflo(d4) * bflo(d4) + bfhi(d4) * bfhi(d4);
    n2 += xhalf(n2);
#pragma unroll
    for (int o = 16; o; o >>= 1) n2 = fmaxf(n2, __shfl_xor(n2, o));
    if (lane == 0) ((float*)(ws + WS_KMAX))[kt] = n2;
  } else if (r <= DH) {
#pragma unroll
    for (int g = 0; g < 4; ++g) {
      u32x2 v = {cvtpk(acc[4 * g], acc[4 * g + 1]), cvtpk(acc[4 * g + 2], acc[4 * g + 3])};
      if (r == DH) v = u32x2{0x3f803f80u, 0x3f803f80u};
      *(u32x2*)(img + 1152 + g * 304 + r * 16 + 8 * h) = v;
    }
  }
}

DI void q_block(int b, const u16* x, const u16* wq, const u16* u, char* ws, char* lds, int w, int lane) {
  const int i = b >> 3, rb = (b & 7) + 8 * (i / 9), cb = i % 9;
  const f32x16 acc = gemm64(x + (size_t)64 * rb * DM, wq + 64 * cb, lds, w, lane);
  gemm64_store(acc, (u16*)(ws + WS_Q) + (size_t)64 * rb * DM + 64 * cb, DM, w, lane);
  const unsigned lds0 = (unsigned)(uintptr_t)lds;
  BAR();
#pragma unroll
  for (int k = 0; k < 2; ++k) {
    const int row = 8 * (2 * w + k) + (lane >> 3), c = lane & 7;
    glds16(u + (size_t)(64 * rb + row) * DM + 64 * cb + 8 * (c ^ (((row >> 1) & 1) << 2)), rfl(lds0 + (2 * w + k) * 1024));
  }
  WAITV_BAR(0);
  u16* uT = (u16*)(ws + WS_UT);
  const int g = lane >> 4, li = lane & 15, q = li >> 2, p = li & 3;
#pragma unroll
  for (int e = 0; e < 2; ++e) {
    const int oct = 4 * e + g, R0 = 8 * oct + q, R1 = R0 + 4;
    const unsigned cbyte = 32 * w + 8 * p;
    const s16x4 a0 = ldstr(lds, R0 * 128 + (cbyte ^ (((R0 >> 1) & 1) << 6)));
    const s16x4 a1 = ldstr(lds, R1 * 128 + (cbyte ^ (((R1 >> 1) & 1) << 6)));
    const auto v = __builtin_shufflevector(a0, a1, 0, 1, 2, 3, 4, 5, 6, 7);
    *(u32x4*)(uT + (size_t)(64 * cb + 16 * w + li) * UT_ROW + 8 + 64 * rb + 8 * oct) = __builtin_bit_cast(u32x4, v);
  }
  if (w == 0 && (rb == 0 || rb == 31)) *(u32x4*)(uT + (size_t)(64 * cb + lane) * UT_ROW + (rb ? 8 + SEQ : 0)) = u32x4{0, 0, 0, 0};
}

__global__ void __launch_bounds__(256, 2)
k1_prep(const u16* x, const u16* wq, const u16* wk, const u16* wv, const u16* u, const u16* Are, const u16* Aim, const u16* Bre,
        const u16* Bim, const u16* Cre, const u16* Cim, const u16* logdt, char* ws) {
  extern __shared__ __attribute__((aligned(16))) char lds[];
  const int b = blockIdx.x, lane = threadIdx.x & 63, w = rfl(threadIdx.x >> 6);
  if (b < 144) {
    if (b == 0 && w == 0) ((unsigned*)(ws + WS_ZERO))[lane] = 0u;
    gen_block(b, Are, Aim, Bre, Bim, Cre, Cim, logdt, ws, lds, w, lane);
  } else if (b < 208) {
    kv_block(b - 144, x, wk, wv, ws, lds, w, lane);
  } else {
    q_block(b - 208, x, wq, u, ws, lds, w, lane);
  }
}

constexpr int CV_CH = 31744, CV_K = 4416, CV_U = 17664, CV_UP = 7040;
constexpr int LDS_CMB = 3 * CV_CH;
constexpr int LDS_RED = LDS_CMB + 8 * 3328;
constexpr int K2_LDS = 151552 + 2048;
constexpr float FAST_BOUND = 30.f;

struct KVAddr { unsigned k, kx, v; };
#define LDK(dst, dstx, t_off) do { dst = ldsr<u32x4>(lds, ad.k + (t_off)); dstx[0] = ldsr<unsigned>(lds, ad.kx + (t_off)); } while (0)
#define LDV(d0, d1, t_off) do { d0 = ldsr<u32x4>(lds, ad.v + (t_off)); d1 = ldsr<u32x4>(lds, ad.v + 608 + (t_off)); } while (0)
#define EX2(x) __builtin_amdgcn_exp2f(x)
#define GRP(MF, S, P, i) do { MF; \
    { const float e0 = EX2(S[4 * (i)]), e1 = EX2(S[4 * (i) + 1]), e2 = EX2(S[4 * (i) + 2]), e3 = EX2(S[4 * (i) + 3]); \
      P[2 * (i)] = cvtpk(e0, e1); P[2 * (i) + 1] = cvtpk(e2, e3); } \
    SBAR(); } while (0)
typedef unsigned u32x8 __attribute__((ext_vector_type(8)));
DI u32x4 lo4(const u32x8& p) { return __builtin_shufflevector(p, p, 0, 1, 2, 3); }
DI u32x4 hi4(const u32x8& p) { return __builtin_shufflevector(p, p, 4, 5, 6, 7); }

#define STEP(Kc, Kcx, Kn, Knx, Vp0, Vp1, Vc0, Vc1, off) do { \
    LDK(Kn, Knx, (off) + KV_TILE); LDV(Vc0, Vc1, (off)); SBAR(); \
    GRP(sb = MFMA16(Kc, qbm, zero16), sa, pa, 0); \
    GRP(sb = MFMA8(Kcx, qbx, sb), sa, pa, 1); \
    GRP(ob = MFMA16(Vp0, lo4(pb), ob), sa, pa, 2); \
    GRP(ob = MFMA16(Vp1, hi4(pb), ob), sa, pa, 3); \
    GRP(sa = MFMA16(Kn, qam, zero16), sb, pb, 0); \
    GRP(sa = MFMA8(Knx, qax, sa), sb, pb, 1); \
    GRP(oa = MFMA16(Vc0, lo4(pa), oa), sb, pb, 2); \
    GRP(oa = MFMA16(Vc1, hi4(pa), oa), sb, pb, 3); \
  } while (0)

DI void slow_tile(const u32x4& kf, const u32x2& kx, const u32x4& v0, const u32x4& v1, const u32x4& qm, const u32x2& qx, f32x16& o, float& m) {
  f32x16 s = {};
  s = MFMA16(kf, qm, s);
  s = MFMA8(kx, qx, s);
  float mx = s[0];
#pragma unroll
  for (int i = 1; i < 16; ++i) mx = fmaxf(mx, s[i]);
  mx = fmaxf(mx, xhalf(mx));
  const float mn = fmaxf(m, mx);
  const float alpha = EX2(m - mn);
  m = mn;
#pragma unroll
  for (int i = 0; i < 16; ++i) o[i] *= alpha;
  u32x8 p;
#pragma unroll
  for (int i = 0; i < 8; ++i) p[i] = cvtpk(EX2(s[2 * i] - mn), EX2(s[2 * i + 1] - mn));
  o = MFMA16(v0, lo4(p), o);
  o = MFMA16(v1, hi4(p), o);
}

__global__ void __launch_bounds__(512, 2)
k2_main(char* ws) {
  extern __shared__ __attribute__((aligned(16))) char lds[];
  const unsigned lds0 = (unsigned)(uintptr_t)lds;
  const int b = blockIdx.x, lane = threadIdx.x & 63, w = rfl(threadIdx.x >> 6);
  const int r = lane & 31, h = lane >> 5;
  const int rb = (b & 7) + 8 * (b >> 6), hg = (b >> 3) & 7;
  const int pt = w & 1, hp = (w >> 1) & 1, kh = w >> 2;
  const int pos = 64 * rb + 32 * pt + r, head = 4 * hg + 2 * hp;

  const char* qrow = ws + WS_Q + (size_t)pos * (DM * 2) + head * (DH * 2);
  const u32x4 qam = *(const u32x4*)(qrow + 16 * h), qbm = *(const u32x4*)(qrow + 36 + 16 * h);
  const unsigned qa16 = *(const unsigned*)(qrow + 32), qb16 = *(const unsigned*)(qrow + 36 + 32);
  float kmax2 = ((const float*)(ws + WS_KMAX))[lane];
  const char* kvg = ws + WS_KV + 16 * lane;
#pragma unroll
  for (int j = 0; j < 3; ++j) {
    const int q = w + 8 * j, ins = q < 12 ? q : 62 + q;
    glds16(kvg + 1024 * ins, rfl(lds0 + 1024 * ins));
  }
#pragma unroll
  for (int o = 32; o; o >>= 1) kmax2 = fmaxf(kmax2, __shfl_xor(kmax2, o));
  auto sq = [](unsigned u) { return bflo(u) * bflo(u) + bfhi(u) * bfhi(u); };
  float na = sq(qam[0]) + sq(qam[1]) + sq(qam[2]) + sq(qam[3]) + (h ? 0.f : sq(qa16));
  float nb = sq(qbm[0]) + sq(qbm[1]) + sq(qbm[2]) + sq(qbm[3]) + (h ? 0.f : sq(qb16));
  na += xhalf(na); nb += xhalf(nb);
  const float ba = rbf(sqrtf(na * kmax2) * 1.01f + 1e-6f), bb = rbf(sqrtf(nb * kmax2) * 1.01f + 1e-6f);
  const bool fast = __all(ba <= FAST_BOUND && bb <= FAST_BOUND);
  WAITV(0);
#pragma unroll
  for (int j = 0; j < 16; ++j) {
    const int q = w + 8 * j, ins = q < 62 ? 12 + q : 24 + q;
    if (q < 124) glds16(kvg + 1024 * ins, rfl(lds0 + 1024 * ins));
  }
  asm volatile("s_barrier" ::: "memory");

  KVAddr ad;
  {
    const int vr = r <= 18 ? r : r - 16;
    const unsigned tb = kh * 32 * KV_TILE;
    ad.k = tb + h * 512 + r * 16; ad.kx = tb + 1024 + r * 4; ad.v = tb + 1152 + h * 304 + vr * 16;
  }
  f32x16 oa = {}, ob = {};
  float ma, mb;
  if (fast) {
    ma = ba; mb = bb;
    u32x2 qax = {h ? 0u : qa16, h ? 0u : (cvtpk(-ba, 0.f))}, qbx = {h ? 0u : qb16, h ? 0u : (cvtpk(-bb, 0.f))};
    u32x4 kA, kB, vA0, vA1, vB0, vB1;
    u32x2 kAx = {0u, 0x3f80u}, kBx = {0u, 0x3f80u};
    f32x16 zero16 = {}, sa, sb;
    u32x8 pa, pb = {};
    LDK(kA, kAx, 0); LDV(vB0, vB1, 0);
    sa = MFMA16(kA, qam, zero16); sa = MFMA8(kAx, qax, sa);
    STEP(kA, kAx, kB, kBx, vB0, vB1, vA0, vA1, 0);
    STEP(kB, kBx, kA, kAx, vA0, vA1, vB0, vB1, KV_TILE);
    STEP(kA, kAx, kB, kBx, vB0, vB1, vA0, vA1, 2 * KV_TILE);
    STEP(kB, kBx, kA, kAx, vA0, vA1, vB0, vB1, 3 * KV_TILE);
    asm volatile("s_waitcnt vmcnt(0)\n\ts_barrier" ::: "memory");
    unsigned off = 4 * KV_TILE;
#pragma unroll 1
    for (int t = 4; t < 32; t += 2) {
      STEP(kA, kAx, kB, kBx, vB0, vB1, vA0, vA1, off);
      STEP(kB, kBx, kA, kAx, vA0, vA1, vB0, vB1, off + KV_TILE);
      off += 2 * KV_TILE;
    }
    ob = MFMA16(vB0, lo4(pb), ob);
    ob = MFMA16(vB1, hi4(pb), ob);
  } else {
    ma = -INFINITY; mb = -INFINITY;
    const u32x2 qax = {h ? 0u : qa16, 0u}, qbx = {h ? 0u : qb16, 0u};
#pragma unroll 1
    for (int t = 0; t < 32; ++t) {
      if (t == 4) asm volatile("s_waitcnt vmcnt(0)\n\ts_barrier" ::: "memory");
      u32x4 kf, v0, v1; u32x2 kx = {0u, 0u};
      LDK(kf, kx, t * KV_TILE); LDV(v0, v1, t * KV_TILE);
      slow_tile(kf, kx, v0, v1, qam, qax, oa, ma);
      slow_tile(kf, kx, v0, v1, qbm, qbx, ob, mb);
    }
  }
  BAR();

  const int nch = (b & 3) == 0 ? 3 : 2;
  {
    const char* kr = ws + WS_KR;
    const char* ut = ws + WS_UT;
    for (int q = w; q < 34 * nch; q += 8) {
      const int ci = q / 34, item = q % 34;
      const int d = ci < 2 ? 2 * b + ci : 512 + (b >> 2);
      if (item < 20) {
        const int m = item / 5, part = item % 5, chunk = part * 64 + lane;
        if (chunk < 267) glds16(kr + (size_t)d * KR_CH + (m & 1) * KR_PAR + (m >> 1) * 4 + chunk * 16, rfl(lds0 + ci * CV_CH + m * CV_K + part * 1024));
      } else {
        const int bb_ = (item - 20) / 7, part = (item - 20) % 7, sg = part * 64 + lane;
        const int qc = sg - sg / 9, s0 = 8 * qc - 976 + 4 * bb_;
        const char* src = (s0 < -8 || sg % 9 == 8) ? ws + WS_ZERO : ut + ((size_t)d * UT_ROW + 8 + s0) * 2;
        if (sg <= 424) glds16(src, rfl(lds0 + ci * CV_CH + CV_U + bb_ * CV_UP + part * 1024));
      }
    }
  }

  {
    const f32x16& give = kh ? oa : ob;
    const float gm = kh ? ma : mb;
    char* own = lds + LDS_CMB + w * 3328;
#pragma unroll
    for (int g = 0; g < 3; ++g) ldsw<f32x4>(own, g * 1024 + lane * 16, f32x4{give[4 * g], give[4 * g + 1], give[4 * g + 2], give[4 * g + 3]});
    ldsw<float>(own, 3072 + lane * 4, gm);
    BAR();
    const char* oth = lds + LDS_CMB + (w ^ 4) * 3328;
    f32x16 keep = kh ? ob : oa;
    const float m1 = kh ? mb : ma, m2 = ldsr<float>(oth, 3072 + lane * 4);
    const float mm = fmaxf(m1, m2), f1 = EX2(m1 - mm), f2 = EX2(m2 - mm);
    float o[12];
#pragma unroll
    for (int g = 0; g < 3; ++g) {
      const f32x4 t = ldsr<f32x4>(oth, g * 1024 + lane * 16);
#pragma unroll
      for (int e = 0; e < 4; ++e) o[4 * g + e] = keep[4 * g + e] * f1 + t[e] * f2;
    }
    const float l = __shfl(o[10], r);
    const float inv = 1.0f / l;
    char* orow = ws + WS_O + (size_t)pos * (DM * 2) + (head + kh) * (DH * 2);
    *(u32x2*)(orow + 8 * h) = u32x2{cvtpk(o[0] * inv, o[1] * inv), cvtpk(o[2] * inv, o[3] * inv)};
    *(u32x2*)(orow + 16 + 8 * h) = u32x2{cvtpk(o[4] * inv, o[5] * inv), cvtpk(o[6] * inv, o[7] * inv)};
    if (!h) *(unsigned*)(orow + 32) = cvtpk(o[8] * inv, o[9] * inv);
  }

  WAITV_BAR(0);
  const int ci = w >> 1, hf = w & 1;
  f32x16 c0 = {}, c1 = {};
  if (ci < nch) {
    const unsigned kb = ci * CV_CH + (3 - (r & 3)) * CV_K + 2 * (8 * h - 8 * (r >> 2));
    const unsigned ub = ci * CV_CH + CV_U + (r & 1) * CV_UP + 144 * (r >> 1) + 16 * h;
    const int n0 = hf ? 33 : 0, n1 = hf ? 65 : 33;
#pragma unroll 2
    for (int n = n0; n < n1; ++n) {
      const u32x4 a1 = ldsr<u32x4>(lds, kb + 2 * (1080 + 16 * n));
      const u32x4 a2 = ldsr<u32x4>(lds, kb + 2 * (56 + 16 * n));
      const unsigned uo = ub + 32 * n + 16 * (n >> 2);
      const u32x4 b1 = ldsr<u32x4>(lds, uo);
      u32x4 b3 = ldsr<u32x4>(lds, uo + 2304);
      if (n == 0) b3 = u32x4{0, 0, 0, 0};
      c0 = MFMA16(a1, b1, c0);
      c1 = MFMA16(a2, b1, c1);
      c1 = MFMA16(a1, b3, c1);
    }
    const f32x16& give = hf ? c0 : c1;
    char* own = lds + LDS_RED + w * 4096;
#pragma unroll
    for (int g = 0; g < 4; ++g) ldsw<f32x4>(own, g * 1024 + lane * 16, f32x4{give[4 * g], give[4 * g + 1], give[4 * g + 2], give[4 * g + 3]});
  }
  BAR();
  if (ci < nch) {
    const int d = ci < 2 ? 2 * b + ci : 512 + (b >> 2);
    const char* oth = lds + LDS_RED + (w ^ 1) * 4096;
    const f32x16 keep = hf ? c1 : c0;
    u16* yrow = (u16*)(ws + WS_YT) + (size_t)d * SEQ + 1024 * hf + 8 * h + 4 * (r & 1) + 64 * (r >> 1);
#pragma unroll
    for (int g = 0; g < 4; ++g) {
      const f32x4 t = ldsr<f32x4>(oth, g * 1024 + lane * 16);
      *(u32x2*)(yrow + 16 * g) = u32x2{cvtpk(keep[4 * g] + t[0], keep[4 * g + 1] + t[1]), cvtpk(keep[4 * g + 2] + t[2], keep[4 * g + 3] + t[3])};
    }
  }
}

__global__ void __launch_bounds__(256, 2)
k3_fin(const u16* wo, const u16* u, const u16* dskip, const char* ws, u16* out) {
  extern __shared__ __attribute__((aligned(16))) char lds[];
  const int b = blockIdx.x, lane = threadIdx.x & 63, w = rfl(threadIdx.x >> 6);
  const int i = b >> 3, rb = (b & 7) + 8 * (i / 9), cb = i % 9;
  const f32x16 acc = gemm64((const u16*)(ws + WS_O) + (size_t)64 * rb * DM, wo + 64 * cb, lds, w, lane);
  gemm64_store(acc, out + (size_t)64 * rb * (2 * DM) + 64 * cb, 2 * DM, w, lane);
  const unsigned lds0 = (unsigned)(uintptr_t)lds;
  const u16* yT = (const u16*)(ws + WS_YT);
  BAR();
#pragma unroll
  for (int k = 0; k < 2; ++k) {
    const int row = 8 * (2 * w + k) + (lane >> 3), c = lane & 7;
    glds16(yT + (size_t)(64 * cb + row) * SEQ + 64 * rb + 8 * (c ^ (((row >> 1) & 1) << 2)), rfl(lds0 + (2 * w + k) * 1024));
  }
  WAITV_BAR(0);
  const int g = lane >> 4, li = lane & 15, q = li >> 2, p = li & 3;
#pragma unroll
  for (int e = 0; e < 2; ++e) {
    const int oct = 4 * e + g, R0 = 8 * oct + q, R1 = R0 + 4;
    const unsigned cbyte = 32 * w + 8 * p;
    const s16x4 a0 = ldstr(lds, R0 * 128 + (cbyte ^ (((R0 >> 1) & 1) << 6)));
    const s16x4 a1 = ldstr(lds, R1 * 128 + (cbyte ^ (((R1 >> 1) & 1) << 6)));
    const u32x4 y = __builtin_bit_cast(u32x4, __builtin_shufflevector(a0, a1, 0, 1, 2, 3, 4, 5, 6, 7));
    const int t = 64 * rb + 16 * w + li, d0 = 64 * cb + 8 * oct;
    const u32x4 uu = *(const u32x4*)(u + (size_t)t * DM + d0), dd = *(const u32x4*)(dskip + d0);
    u32x4 o;
#pragma unroll
    for (int k = 0; k < 4; ++k) {
      const unsigned sk = cvtpk(bflo(uu[k]) * bflo(dd[k]), bfhi(uu[k]) * bfhi(dd[k]));
      o[k] = cvtpk(bflo(y[k]) + bflo(sk), bfhi(y[k]) + bfhi(sk));
    }
    *(u32x4*)(out + (size_t)t * (2 * DM) + DM + d0) = o;
  }
}

extern "C" void kernel_launch(void* const* d_in, const int* in_sizes, int n_in, void* d_out, int out_size, void* d_ws, size_t ws_size,
                              hipStream_t stream) {
  const u16* x = (const u16*)d_in[0];
  const u16* wq = (const u16*)d_in[1];
  const u16* wk = (const u16*)d_in[2];
  const u16* wv = (const u16*)d_in[3];
  const u16* wo = (const u16*)d_in[4];
  const u16* u = (const u16*)d_in[5];
  const u16* Are = (const u16*)d_in[6];
  const u16* Aim = (const u16*)d_in[7];
  const u16* Bre = (const u16*)d_in[8];
  const u16* Bim = (const u16*)d_in[9];
  const u16* Cre = (const u16*)d_in[10];
  const u16* Cim = (const u16*)d_in[11];
  const u16* logdt = (const u16*)d_in[12];
  const u16* dskip = (const u16*)d_in[13];
  char* ws = (char*)d_ws;
  (void)hipFuncSetAttribute((const void*)k1_prep, hipFuncAttributeMaxDynamicSharedMemorySize, 65536);
  (void)hipFuncSetAttribute((const void*)k2_main, hipFuncAttributeMaxDynamicSharedMemorySize, K2_LDS);
  (void)hipFuncSetAttribute((const void*)k3_fin, hipFuncAttributeMaxDynamicSharedMemorySize, 65536);
  k1_prep<<<496, 256, 65536, stream>>>(x, wq, wk, wv, u, Are, Aim, Bre, Bim, Cre, Cim, logdt, ws);
  k2_main<<<256, 512, K2_LDS, stream>>>(ws);
  k3_fin<<<288, 256, 65536, stream>>>(wo, u, dskip, ws, (u16*)d_out);
}
```

```cpp
#if defined(__HIP_DEVICE_COMPILE__)
#pragma clang attribute push(__attribute__((target("no-packed-fp32-ops"))), apply_to = function)
#endif
#include <hip/hip_runtime.h>
#include <stdint.h>

typedef __bf16 bf16x2 __attribute__((ext_vector_type(2)));
typedef __bf16 bf16x8 __attribute__((ext_vector_type(8)));
typedef short s16x4 __attribute__((ext_vector_type(4)));
typedef float f32x2 __attribute__((ext_vector_type(2)));
typedef float f32x4 __attribute__((ext_vector_type(4)));
typedef float f32x16 __attribute__((ext_vector_type(16)));
typedef unsigned u32x2 __attribute__((ext_vector_type(2)));
typedef unsigned u32x4 __attribute__((ext_vector_type(4)));
typedef unsigned short u16;
#define DI __device__ __forceinline__
#define BID ((int)__builtin_amdgcn_workgroup_id_x())
#define TID ((int)__builtin_amdgcn_workitem_id_x())
#define LDSP __attribute__((address_space(3)))
#define SBAR() __builtin_amdgcn_sched_barrier(0)
#define PIN(x) asm volatile("" : "+v"(x))
#define MFMA16(a, b, c) __builtin_amdgcn_mfma_f32_32x32x16_bf16(__builtin_bit_cast(bf16x8, a), __builtin_bit_cast(bf16x8, b), c, 0, 0, 0)
#define MFMA8(a, b, c) __builtin_amdgcn_mfma_f32_32x32x8bf16_1k(__builtin_bit_cast(s16x4, a), __builtin_bit_cast(s16x4, b), c, 0, 0, 0)

constexpr int SEQ = 2048, DM = 576, DH = 18, NST = 128;
constexpr int KV_TILE = 2368;
constexpr int KV_HALF = 4 * KV_TILE, KV_SEG = 2 * KV_HALF;
constexpr int KV_HDR = 8 * KV_SEG;
constexpr int KR_PAR = 4288, KR_CH = 2 * KR_PAR;
constexpr int UT_ROW = 2064;
constexpr size_t WS_Q = 0;
constexpr size_t WS_KV = WS_Q + (size_t)SEQ * DM * 2;
constexpr size_t WS_KMAX = WS_KV + 155648;
constexpr size_t WS_ZERO = WS_KMAX + 256;
constexpr size_t WS_O = WS_ZERO + 256;
constexpr size_t WS_KR = WS_O + (size_t)SEQ * DM * 2;
constexpr size_t WS_UT = WS_KR + (size_t)DM * KR_CH;
constexpr size_t WS_YT = WS_UT + (size_t)DM * 2064 * 2;

DI unsigned cvtpk(float a, float b) { f32x2 v = {a, b}; return __builtin_bit_cast(unsigned, __builtin_convertvector(v, bf16x2)); }
DI float bflo(unsigned u) { return __uint_as_float(u << 16); }
DI float bfhi(unsigned u) { return __uint_as_float(u & 0xffff0000u); }
DI float bf2f(u16 x) { return __uint_as_float((unsigned)x << 16); }
DI float rbf(float x) { return bflo(cvtpk(x, x)); }
DI int crow(int reg, int h) { return (reg & 3) + 8 * (reg >> 2) + 4 * h; }
DI void glds16(const void* g, unsigned lds_base) {
  unsigned sv;
  asm volatile("s_mov_b32 %0, m0\n\ts_mov_b32 m0, %2\n\ts_nop 0\n\tglobal_load_lds_dwordx4 %1, off\n\ts_mov_b32 m0, %0"
               : "=&s"(sv) : "v"(g), "s"(lds_base) : "memory");
}
#define WAITV(n) asm volatile("s_waitcnt vmcnt(" #n ")" ::: "memory")
#define WAITV_BAR(n) asm volatile("s_waitcnt vmcnt(" #n ") lgkmcnt(0)\n\ts_barrier" ::: "memory")
#define BAR() asm volatile("s_waitcnt lgkmcnt(0)\n\ts_barrier" ::: "memory")
#define STAMP(kern, i)
DI unsigned rfl(unsigned x) { return __builtin_amdgcn_readfirstlane(x); }
template <typename T> DI T ldsr(const char* lds, unsigned off) { return *(const LDSP T*)((const LDSP char*)lds + off); }
template <typename T> DI void ldsw(char* lds, unsigned off, T v) { *(LDSP T*)((LDSP char*)lds + off) = v; }
DI s16x4 ldstr(const char* lds, unsigned off) {
  typedef short v4i16_t __attribute__((ext_vector_type(4)));
  return __builtin_bit_cast(s16x4, __builtin_amdgcn_ds_read_tr16_b64_v4i16((LDSP v4i16_t*)((LDSP char*)lds + off)));
}
DI float xhalf(float v) {
  return __shfl_xor(v, 32);
}

constexpr int GS_W = 10240, GS_BYTES = 10240 + 8192;
DI int wswz(int row) { return ((row >> 1) & 1) | (((row >> 3) & 1) << 1); }
template <int NSTG>
DI void gemm_stage(const u16* X, int p0, const u16* W, int s, unsigned lds0, int w, int lane) {
  const unsigned sb = lds0 + (s % NSTG) * GS_BYTES;
#pragma unroll
  for (int i = 0; i < 3; ++i) {
    if (i < 2 || w < 2) {
      const int ins = w + 4 * i, row = 8 * ins + (lane >> 3), c = lane & 7;
      const int gr = p0 + row < SEQ ? p0 + row : SEQ - 1;
      glds16(X + (size_t)gr * DM + 64 * s + 8 * (c ^ ((row >> 1) & 7)), sb + ins * 1024);
    }
  }
#pragma unroll
  for (int i = 0; i < 2; ++i) {
    const int ins = w + 4 * i, row = 8 * ins + (lane >> 3), c = lane & 7;
    glds16(W + (size_t)(64 * s + row) * DM + 8 * (c ^ (wswz(row) << 1)), sb + GS_W + ins * 1024);
  }
}
template <int N> DI void waitv_bar() { asm volatile("s_waitcnt vmcnt(%0) lgkmcnt(0)\n\ts_barrier" ::"i"(N) : "memory"); }
template <int NSTG, int S> DI void gemm_step(const u16* X, int p0, const u16* W, const char* lds, unsigned lds0, int w, int lane, f32x4 (&acc)[5], char* ws, int kern) {
  constexpr int inflight = (S + NSTG - 2 > 8 ? 8 : S + NSTG - 2) - S;
  if (w < 2) waitv_bar<5 * inflight>(); else waitv_bar<4 * inflight>();
  STAMP(kern, 1 + S);
  if (S + NSTG - 1 < 9) gemm_stage<NSTG>(X, p0, W, S + NSTG - 1, lds0, w, lane);
  const unsigned sb = (S % NSTG) * GS_BYTES;
  const int li = lane & 15, g = lane >> 4, q = li >> 2, p = lane & 3;
#pragma unroll
  for (int ks = 0; ks < 2; ++ks) {
    const int R0 = 32 * ks + 8 * g + q, R1 = R0 + 4;
    const s16x4 a0 = ldstr(lds, sb + GS_W + R0 * 128 + ((w ^ wswz(R0)) * 32) + 8 * p);
    const s16x4 a1 = ldstr(lds, sb + GS_W + R1 * 128 + ((w ^ wswz(R1)) * 32) + 8 * p);
    const auto a = __builtin_shufflevector(a0, a1, 0, 1, 2, 3, 4, 5, 6, 7);
#pragma unroll
    for (int pt = 0; pt < 5; ++pt) {
      const int prow = 16 * pt + li;
      const u32x4 b = ldsr<u32x4>(lds, sb + prow * 128 + 16 * ((4 * ks + g) ^ ((prow >> 1) & 7)));
      acc[pt] = __builtin_amdgcn_mfma_f32_16x16x32_bf16(__builtin_bit_cast(bf16x8, a), __builtin_bit_cast(bf16x8, b), acc[pt], 0, 0, 0);
    }
  }
  if constexpr (S + 1 < 9) gemm_step<NSTG, S + 1>(X, p0, W, lds, lds0, w, lane, acc, ws, kern);
}
template <int NSTG> DI void gemm80(const u16* X, int p0, const u16* W, u16* C, int ld, char* lds, int w, int lane, char* ws, int kern) {
  const unsigned lds0 = (unsigned)(uintptr_t)lds;
#pragma unroll
  for (int s = 0; s < NSTG - 1; ++s) if (s < 9) gemm_stage<NSTG>(X, p0, W, s, lds0, w, lane);
  f32x4 acc[5] = {};
  STAMP(kern, 11);
  gemm_step<NSTG, 0>(X, p0, W, lds, lds0, w, lane, acc, ws, kern);
  STAMP(kern, 10);
#pragma unroll
  for (int pt = 0; pt < 5; ++pt) {
    const int row = p0 + 16 * pt + (lane & 15);
    if (row < SEQ) *(u32x2*)(C + (size_t)row * ld + 16 * w + 4 * (lane >> 4)) = u32x2{cvtpk(acc[pt][0], acc[pt][1]), cvtpk(acc[pt][2], acc[pt][3])};
  }
}

struct cpx { float r, i; };
DI cpx cmul(cpx a, cpx b) { return cpx{a.r * b.r - a.i * b.i, a.r * b.i + a.i * b.r}; }
DI cpx cexp_l(float zr, float zi, float l) {
  const float mag = __builtin_amdgcn_exp2f((zr * l) * 1.4426950408889634f);
  const float rev = __builtin_amdgcn_fractf((zi * l) * 0.15915494309189535f);
  return cpx{mag * __builtin_amdgcn_cosf(rev), mag * __builtin_amdgcn_sinf(rev)};
}
constexpr int GEN_PITCH = 272, GEN_X = 0, GEN_Y = 32 * GEN_PITCH, GEN_WAVE = 14592;

DI void gen_wave(int d, const u16* Are, const u16* Aim, const u16* Bre, const u16* Bim, const u16* Cre, const u16* Cim,
                  const u16* logdt, char* ws, char* lds, int lane) {
  const int r = lane & 31, h = lane >> 5;
  const float dt = rbf(expf(bf2f(logdt[d])));
  f32x16 acc = {};
#pragma unroll 1
  for (int pass = 0; pass < 2; ++pass) {
    const int idx = d * NST + 64 * pass + lane;
    const float zr = dt * bf2f(Are[idx]), zi = dt * bf2f(Aim[idx]);
    const float dBr = dt * bf2f(Bre[idx]), dBi = dt * bf2f(Bim[idx]);
    const float cr = bf2f(Cre[idx]), ci = bf2f(Cim[idx]);
    const cpx c = cpx{cr * dBr - ci * dBi, cr * dBi + ci * dBr};
    const cpx w1 = cexp_l(zr, zi, 1.f), w8 = cexp_l(zr, zi, 8.f), w256 = cexp_l(zr, zi, 256.f);
    cpx x = cpx{1.f, 0.f};
#pragma unroll
    for (int m = 0; m < 32; ++m) {
      ldsw<unsigned>(lds, GEN_X + m * GEN_PITCH + 4 * lane, cvtpk(x.r, x.i));
      if (m < 31) x = (m == 0) ? w8 : cmul(x, w8);
    }
#define PUTY(col, v) ldsw<unsigned>(lds, GEN_Y + (col) * GEN_PITCH + 4 * lane, cvtpk((v).r, -(v).i))
    cpx y[8];
    y[0] = c;
#pragma unroll
    for (int i = 1; i < 8; ++i) y[i] = cmul(y[i - 1], w1);
#pragma unroll
    for (int i = 0; i < 8; ++i) PUTY(i, y[i]);
#pragma unroll
    for (int i = 0; i < 4; ++i) { const cpx t = cmul(y[2 * i], w256); PUTY(8 + i, t); }
    const cpx p2 = cmul(w256, w256), p3 = cmul(p2, w256), p4 = cmul(p2, p2);
    { cpx t = cmul(y[0], p2); PUTY(12, t); t = cmul(y[4], p2); PUTY(13, t); }
    { cpx t = cmul(y[0], p3); PUTY(14, t); t = cmul(y[4], p3); PUTY(15, t); }
    { const cpx t4 = cmul(y[0], p4); PUTY(16, t4);
      cpx t = cmul(t4, w256); PUTY(17, t); t = cmul(t4, p2); PUTY(18, t); t = cmul(t4, p3); PUTY(19, t); t = cmul(t4, p4); PUTY(20, t); }
#undef PUTY
    asm volatile("s_waitcnt lgkmcnt(0)" ::: "memory");
    const int yr = r <= 20 ? r : r - 16;
#pragma unroll
    for (int ks = 0; ks < 8; ++ks) {
      const u32x4 a = ldsr<u32x4>(lds, GEN_X + r * GEN_PITCH + 32 * ks + 16 * h);
      const u32x4 b = ldsr<u32x4>(lds, GEN_Y + yr * GEN_PITCH + 32 * ks + 16 * h);
      acc = MFMA16(a, b, acc);
    }
    asm volatile("s_waitcnt lgkmcnt(0)" ::: "memory");
  }
  {
    const int c = r;
    const int hi = c < 8 ? c : c < 12 ? 256 + 2 * (c - 8) : c < 16 ? 512 + 256 * ((c - 12) >> 1) + 4 * ((c - 12) & 1) : 1024 + 256 * (c - 16);
    if (c < 20) {
#pragma unroll
      for (int i = 0; i < 16; ++i) ldsw<u16>(lds, 2 * (8 * crow(i, h) + hi), (u16)cvtpk(acc[i], acc[i]));
    } else if (c == 20 && h == 0) {
      ldsw<u16>(lds, 2 * 2048, (u16)cvtpk(acc[0], acc[0]));
    }
  }
  asm volatile("s_waitcnt lgkmcnt(0)" ::: "memory");
  unsigned* E = (unsigned*)(ws + WS_KR + (size_t)d * KR_CH);
  unsigned* Od = E + KR_PAR / 4;
  auto rd = [&](int l) -> unsigned { return ldsr<u16>(lds, 2 * l); };
  auto pk = [](unsigned lo, unsigned hi) -> unsigned { return lo | (hi << 16); };
  const int L = lane;
  {
    const unsigned e0 = rd(4 * L), e1 = rd(4 * L + 1), e2 = rd(4 * L + 2), e3 = rd(4 * L + 3);
    const unsigned pv = L ? rd(4 * L - 1) : 0u;
    const int p0 = 2 * L;
    *(u32x2*)(E + 1028 - p0) = u32x2{pk(e3, e2), pk(e1, e0)};
    *(u32x2*)(Od + 1028 - p0) = u32x2{pk(e2, e1), pk(e0, pv)};
  }
  {
    const int j0 = 256 + 4 * L;
    const unsigned a = rd(j0), b = rd(j0 + 2), c = rd(j0 + 4), pv = L ? a : rd(255);
    const int p0 = j0 / 2;
    *(u32x2*)(E + 1028 - p0) = u32x2{pk(c, b), pk(a, a)};
    *(u32x2*)(Od + 1028 - p0) = u32x2{pk(b, a), pk(a, pv)};
  }
  {
    const int j0 = 512 + 8 * L;
    const unsigned a = rd(j0), b = rd(j0 + 4), c = rd(j0 + 8);
    const unsigned aa = pk(a, a), bb = pk(b, b), cc = pk(c, c);
    unsigned* e = E + 1026 - j0 / 2;
    unsigned* o = Od + 1026 - j0 / 2;
    *(u32x2*)(e) = u32x2{cc, bb};          *(u32x2*)(e + 2) = u32x2{pk(b, a), aa};
    *(u32x2*)(o) = u32x2{pk(c, b), bb};    *(u32x2*)(o + 2) = u32x2{aa, aa};
  }
  {
    const int j0 = 1024 + 16 * L;
    const unsigned a = rd(j0), b = rd(j0 + 8), c = rd(j0 + 16);
    const unsigned aa = pk(a, a), bb = pk(b, b), cc = pk(c, c);
    unsigned* e = E + 1022 - j0 / 2;
    unsigned* o = Od + 1022 - j0 / 2;
    *(u32x2*)(e) = u32x2{cc, cc};          *(u32x2*)(e + 2) = u32x2{bb, bb};
    *(u32x2*)(e + 4) = u32x2{bb, pk(b, a)}; *(u32x2*)(e + 6) = u32x2{aa, aa};
    *(u32x2*)(o) = u32x2{cc, pk(c, b)};    *(u32x2*)(o + 2) = u32x2{bb, bb};
    *(u32x2*)(o + 4) = u32x2{bb, aa};      *(u32x2*)(o + 6) = u32x2{aa, aa};
    if (L == 63) Od[5] = pk(0u, c);
  }
  if (L < 6) E[L] = 0u;
  if (L < 5) Od[L] = 0u;
  if (L < 42) { E[1030 + L] = 0u; Od[1030 + L] = 0u; }
}

DI void kv_block(int kt, const u16* x, const u16* wk, const u16* wv, char* ws, char* lds, int w, int lane) {
  const unsigned lds0 = (unsigned)(uintptr_t)lds;
  const int r = lane & 31, h = lane >> 5;
  const u16* xr = x + (size_t)(32 * kt + r) * DM + 144 * w + 8 * h;
  u32x4 xf[9];
#pragma unroll
  for (int ks = 0; ks < 9; ++ks) xf[ks] = *(const u32x4*)(xr + 16 * ks);
#pragma unroll
  for (int i = 0; i < 11; ++i) {
    const int id = w + 4 * i, m = id >= 21, part = m ? id - 21 : id, ch = part * 64 + lane;
    if (id < 42 && ch < 1296) glds16((const char*)(m ? wv : wk) + 16 * ch, lds0 + m * 20736 + part * 1024);
  }
  WAITV_BAR(0);
  const int n = r < DH ? r : DH - 1;
  const unsigned wb = 2 * ((144 * w + 8 * h) * DH + n);
  f32x16 ak = {}, av = {};
#pragma unroll
  for (int ks = 0; ks < 9; ++ks) {
    u32x4 fk, fv;
#pragma unroll
    for (int j = 0; j < 4; ++j) {
      const unsigned o = wb + 2 * DH * (16 * ks + 2 * j);
      fk[j] = (unsigned)ldsr<u16>(lds, o) | ((unsigned)ldsr<u16>(lds, o + 2 * DH) << 16);
      fv[j] = (unsigned)ldsr<u16>(lds, 20736 + o) | ((unsigned)ldsr<u16>(lds, 20736 + o + 2 * DH) << 16);
    }
    ak = MFMA16(fk, xf[ks], ak);
    av = MFMA16(xf[ks], fv, av);
  }
  BAR();
#pragma unroll
  for (int g = 0; g < 4; ++g) {
    ldsw<f32x4>(lds, (2 * w) * 4096 + g * 1024 + lane * 16, f32x4{ak[4 * g], ak[4 * g + 1], ak[4 * g + 2], ak[4 * g + 3]});
    ldsw<f32x4>(lds, (2 * w + 1) * 4096 + g * 1024 + lane * 16, f32x4{av[4 * g], av[4 * g + 1], av[4 * g + 2], av[4 * g + 3]});
  }
  BAR();
  if (w >= 2) return;
  f32x16 acc;
#pragma unroll
  for (int g = 0; g < 4; ++g) {
    f32x4 t = ldsr<f32x4>(lds, w * 4096 + g * 1024 + lane * 16);
#pragma unroll
    for (int v = 1; v < 4; ++v) t += ldsr<f32x4>(lds, (2 * v + w) * 4096 + g * 1024 + lane * 16);
#pragma unroll
    for (int e = 0; e < 4; ++e) acc[4 * g + e] = t[e];
  }
  const int kh = kt >> 5, t = kt & 31;
  char* kv = ws + WS_KV;
  if (w == 0) {
    char* img = t ? kv + ((t - 1) >> 2) * KV_SEG + kh * KV_HALF + ((t - 1) & 3) * KV_TILE : kv + KV_HDR + kh * 1152;
    const float sc = 0.23570226039551584f * 1.4426950408889634f;
    const int e = (r & 0x13) | ((r & 4) << 1) | ((r & 8) >> 1);
    const unsigned d0 = cvtpk(acc[0] * sc, acc[1] * sc), d1 = cvtpk(acc[2] * sc, acc[3] * sc);
    const unsigned d2 = cvtpk(acc[4] * sc, acc[5] * sc), d3 = cvtpk(acc[6] * sc, acc[7] * sc);
    const unsigned d4 = h ? 0u : cvtpk(acc[8] * sc, acc[9] * sc);
    *(u32x2*)(img + e * 16 + 8 * h) = u32x2{d0, d1};
    *(u32x2*)(img + 512 + e * 16 + 8 * h) = u32x2{d2, d3};
    if (!h) *(unsigned*)(img + 1024 + e * 4) = d4;
    float n2 = bflo(d0) * bflo(d0) + bfhi(d0) * bfhi(d0) + bflo(d1) * bflo(d1) + bfhi(d1) * bfhi(d1) + bflo(d2) * bflo(d2) + bfhi(d2) * bfhi(d2) +
               bflo(d3) * bflo(d3) + bfhi(d3) * bfhi(d3) + bflo(d4) * bflo(d4) + bfhi(d4) * bfhi(d4);
    n2 += xhalf(n2);
#pragma unroll
    for (int o = 16; o; o >>= 1) n2 = fmaxf(n2, __shfl_xor(n2, o));
    if (lane == 0) ((float*)(ws + WS_KMAX))[kt] = n2;
  } else if (r <= DH) {
    char* img = kv + (t >> 2) * KV_SEG + kh * KV_HALF + (t & 3) * KV_TILE + 1152;
#pragma unroll
    for (int g = 0; g < 4; ++g) {
      u32x2 v = {cvtpk(acc[4 * g], acc[4 * g + 1]), cvtpk(acc[4 * g + 2], acc[4 * g + 3])};
      if (r == DH) v = u32x2{0x3f803f80u, 0x3f803f80u};
      *(u32x2*)(img + g * 304 + r * 16 + 8 * h) = v;
    }
  }
}

DI void ut_wave(int tile, const u16* u, char* ws, char* lds, int lane) {
  const unsigned lds0 = (unsigned)(uintptr_t)lds;
  const int tb = tile / 9, cb = tile - 9 * tb;
#pragma unroll
  for (int i = 0; i < 16; ++i) {
    const int row = 8 * i + (lane >> 3), c = lane & 7;
    glds16(u + (size_t)(128 * tb + row) * DM + 64 * cb + 8 * (c ^ (wswz(row) << 1)), lds0 + i * 1024);
  }
  u16* uT = (u16*)(ws + WS_UT);
  if (tb == 0 || tb == 15) *(u32x4*)(uT + (size_t)(64 * cb + lane) * UT_ROW + (tb ? 8 + SEQ : 0)) = u32x4{0, 0, 0, 0};
  WAITV(0);
  const int g = lane >> 4, li = lane & 15, q = li >> 2, p = li & 3;
#pragma unroll
  for (int db = 0; db < 4; ++db) {
#pragma unroll
    for (int e = 0; e < 4; ++e) {
      const int oct = 4 * e + g, R0 = 8 * oct + q, R1 = R0 + 4;
      const s16x4 a0 = ldstr(lds, R0 * 128 + ((db ^ wswz(R0)) * 32) + 8 * p), a1 = ldstr(lds, R1 * 128 + ((db ^ wswz(R1)) * 32) + 8 * p);
      const auto v = __builtin_shufflevector(a0, a1, 0, 1, 2, 3, 4, 5, 6, 7);
      *(u32x4*)(uT + (size_t)(64 * cb + 16 * db + li) * UT_ROW + 8 + 128 * tb + 8 * oct) = __builtin_bit_cast(u32x4, v);
    }
  }
}

constexpr int K1_LDS = 4 * GS_BYTES;
__global__ void __launch_bounds__(256, 2)
k1_prep(const u16* x, const u16* wq, const u16* wk, const u16* wv, const u16* u, const u16* Are, const u16* Aim, const u16* Bre,
        const u16* Bim, const u16* Cre, const u16* Cim, const u16* logdt, char* ws) {
  extern __shared__ __attribute__((aligned(16))) char lds[];
  const int b = BID, lane = TID & 63, w = rfl(TID >> 6);
  STAMP(0, 0);
  if (b < 256) {
    const int x8 = b & 7, i = b >> 3, id = x8 * 32 + i;
    const int tile = ((id >> 5) * 234 >> 3) + (id & 31);
    const int tend = (((id >> 5) + 1) * 234 >> 3);
    if (tile < tend) {
      const int rb = tile / 9, cb = tile - 9 * rb;
      gemm80<4>(x, 80 * rb, wq + 64 * cb, (u16*)(ws + WS_Q) + 64 * cb, DM, lds, w, lane, ws, 0);
    }
  } else {
    const int j = b - 256;
    if (j == 255 && w == 3) ((unsigned*)(ws + WS_ZERO))[lane] = 0u;
    if (j < 192) {
      if (w < 3) gen_wave(3 * j + w, Are, Aim, Bre, Bim, Cre, Cim, logdt, ws, lds + w * GEN_WAVE, lane);
      else if (j < 144) ut_wave(j, u, ws, lds + 3 * GEN_WAVE, lane);
    } else {
      kv_block(j - 192, x, wk, wv, ws, lds, w, lane);
    }
  }
  STAMP(0, 15);
}

constexpr int LDS_CMB = 0;
constexpr int LDS_CV = 3 * KV_SEG;
constexpr int CV_CH = 31744, CV_K = 4416, CV_U = 17664, CV_UP = 7040;
constexpr int K2_LDS = LDS_CV + 3 * CV_CH;
constexpr float FAST_BOUND = 30.f;

struct KVAddr { unsigned k, kx, v; };
#define LDK(dst, dstx, t_off) do { dst = ldsr<u32x4>(lds, ad.k + (t_off)); dstx[0] = ldsr<unsigned>(lds, ad.kx + (t_off)); } while (0)
#define LDV(d0, d1, t_off) do { d0 = ldsr<u32x4>(lds, ad.v + (t_off)); d1 = ldsr<u32x4>(lds, ad.v + 608 + (t_off)); } while (0)
#define EX2(x) __builtin_amdgcn_exp2f(x)
#define GRP(MF, S, P, i) do { MF; SBAR(); \
    { const float e0 = EX2(S[4 * (i)]), e1 = EX2(S[4 * (i) + 1]), e2 = EX2(S[4 * (i) + 2]), e3 = EX2(S[4 * (i) + 3]); \
      P[2 * (i)] = cvtpk(e0, e1); P[2 * (i) + 1] = cvtpk(e2, e3); } \
    SBAR(); } while (0)
typedef unsigned u32x8 __attribute__((ext_vector_type(8)));
DI u32x4 lo4(const u32x8& p) { return __builtin_shufflevector(p, p, 0, 1, 2, 3); }
DI u32x4 hi4(const u32x8& p) { return __builtin_shufflevector(p, p, 4, 5, 6, 7); }

#define STEP(Kc, Kcx, Kn, Knx, Vp0, Vp1, Vc0, Vc1, off) do { \
    LDK(Kn, Knx, (off)); LDV(Vc0, Vc1, (off)); SBAR(); \
    GRP(sb = MFMA16(Kc, qbm, zero16), sa, pa, 0); \
    GRP(ob = MFMA16(Vp0, lo4(pb), ob), sa, pa, 1); \
    GRP(sb = MFMA8(Kcx, qbx, sb), sa, pa, 2); \
    GRP(ob = MFMA16(Vp1, hi4(pb), ob), sa, pa, 3); \
    GRP(sa = MFMA16(Kn, qam, zero16), sb, pb, 0); \
    GRP(oa = MFMA16(Vc0, lo4(pa), oa), sb, pb, 1); \
    GRP(sa = MFMA8(Knx, qax, sa), sb, pb, 2); \
    GRP(oa = MFMA16(Vc1, hi4(pa), oa), sb, pb, 3); \
  } while (0)

DI void slow_tile(const u32x4& kf, const u32x2& kx, const u32x4& v0, const u32x4& v1, const u32x4& qm, const u32x2& qx, f32x16& o, float& m) {
  f32x16 s = {};
  s = MFMA16(kf, qm, s);
  s = MFMA8(kx, qx, s);
  float mx = s[0];
#pragma unroll
  for (int i = 1; i < 16; ++i) mx = fmaxf(mx, s[i]);
  mx = fmaxf(mx, xhalf(mx));
  const float mn = fmaxf(m, mx);
  const float alpha = EX2(m - mn);
  m = mn;
#pragma unroll
  for (int i = 0; i < 16; ++i) o[i] *= alpha;
  u32x8 p;
#pragma unroll
  for (int i = 0; i < 8; ++i) p[i] = cvtpk(EX2(s[2 * i] - mn), EX2(s[2 * i + 1] - mn));
  o = MFMA16(v0, lo4(p), o);
  o = MFMA16(v1, hi4(p), o);
}

#define SB() asm volatile("s_barrier" ::: "memory")
DI void conv_wave(int d, char* ws, char* lds, unsigned lds0, int lane) {
  const int r = lane & 31, h = lane >> 5;
  SB();
  {
    const char* kr = ws + WS_KR + (size_t)d * KR_CH + 16 * lane;
#pragma unroll
    for (int m = 0; m < 4; ++m)
#pragma unroll
      for (int part = 0; part < 5; ++part)
        if (part < 4 || lane < 11) glds16(kr + (m & 1) * KR_PAR + (m >> 1) * 4 + part * 1024, lds0 + m * CV_K + part * 1024);
    const char* ur = ws + WS_UT + (size_t)d * (UT_ROW * 2);
#pragma unroll
    for (int part = 0; part < 7; ++part) {
      const int sg = part * 64 + lane, qc = sg - sg / 9;
#pragma unroll
      for (int bb = 0; bb < 2; ++bb) {
        const int s0 = 8 * qc - 976 + 4 * bb;
        const char* src = (s0 < -8 || sg % 9 == 8) ? ws + WS_ZERO : ur + 2 * (8 + s0);
        if (part < 6 || lane <= 40) glds16(src, lds0 + CV_U + bb * CV_UP + part * 1024);
      }
    }
  }
  SB();
  WAITV(0);
  STAMP(1, 6);
  const unsigned kb = (3 - (r & 3)) * CV_K + 2 * (8 * h - 8 * (r >> 2));
  const unsigned ub = CV_U + (r & 1) * CV_UP + 144 * (r >> 1) + 16 * h;
  f32x16 c0 = {}, c1 = {};
  u32x4 a1[2], a2[2], b1[2], b3[2];
#define CLD(i, n) do { a1[i] = ldsr<u32x4>(lds, kb + 2 * (1080 + 16 * (n))); a2[i] = ldsr<u32x4>(lds, kb + 2 * (56 + 16 * (n))); \
    b1[i] = ldsr<u32x4>(lds, ub + 32 * (n) + 16 * ((n) >> 2)); b3[i] = ldsr<u32x4>(lds, ub + 32 * (n) + 16 * ((n) >> 2) + 2304); } while (0)
#define CMM(i) do { c0 = MFMA16(a1[i], b1[i], c0); c1 = MFMA16(a2[i], b1[i], c1); c1 = MFMA16(a1[i], b3[i], c1); } while (0)
  CLD(0, 0); CLD(1, 1);
  c0 = MFMA16(a1[0], b1[0], c0); c1 = MFMA16(a2[0], b1[0], c1);
  SBAR();
#pragma unroll
  for (int seg = 0; seg < 4; ++seg) {
#pragma unroll
    for (int n = 16 * seg + 1; n <= 16 * seg + 16; n += 2) {
      CLD(0, n + 1); SBAR(); CMM(1); SBAR();
      if (n + 2 <= 64) CLD(1, n + 2);
      SBAR(); CMM(0); SBAR();
    }
    SB();
  }
#undef CLD
#undef CMM
  STAMP(1, 7);
  u16* yrow = (u16*)(ws + WS_YT) + (size_t)d * SEQ + 8 * h + 4 * (r & 1) + 64 * (r >> 1);
#pragma unroll
  for (int g = 0; g < 4; ++g) {
    *(u32x2*)(yrow + 16 * g) = u32x2{cvtpk(c0[4 * g], c0[4 * g + 1]), cvtpk(c0[4 * g + 2], c0[4 * g + 3])};
    *(u32x2*)(yrow + 1024 + 16 * g) = u32x2{cvtpk(c1[4 * g], c1[4 * g + 1]), cvtpk(c1[4 * g + 2], c1[4 * g + 3])};
  }
  SB(); SB();
}

__global__ void __launch_bounds__(768, 3)
k2_main(char* ws) {
  extern __shared__ __attribute__((aligned(16))) char lds[];
  const unsigned lds0 = (unsigned)(uintptr_t)lds;
  const int b = BID, lane = TID & 63, w = rfl(TID >> 6);
  STAMP(1, 0);
  if (w == 11) {
    const char* kvg = ws + WS_KV + 16 * lane;
    auto issue = [&](int s) {
#pragma unroll
      for (int j = 0; j < 19; ++j)
        if (j < 18 || lane < 32) glds16(kvg + s * KV_SEG + 1024 * j, lds0 + (s % 3) * KV_SEG + 1024 * j);
    };
    issue(0);
    WAITV_BAR(0);
    issue(1); issue(2);
#pragma unroll
    for (int s = 1; s < 8; ++s) {
      if (s < 7) { WAITV_BAR(19); } else { WAITV_BAR(0); }
      if (s + 2 < 8) issue(s + 2);
    }
    STAMP(1, 15);
    return;
  }
  if (w >= 8) {
    const int sw = w - 8, i = b >> 3, xc = b & 7;
    if (sw < 2 || (i & 3) == 0) {
      conv_wave(sw < 2 ? 72 * xc + 2 * i + sw : 72 * xc + 64 + (i >> 2), ws, lds + LDS_CV + sw * CV_CH, lds0 + LDS_CV + sw * CV_CH, lane);
    } else {
#pragma unroll
      for (int s = 0; s < 8; ++s) SB();
    }
    STAMP(1, 15);
    return;
  }
  const int r = lane & 31, h = lane >> 5;
  const int rb = (b & 7) + 8 * (b >> 6), hg = (b >> 3) & 7;
  const int pt = w & 1, hp = (w >> 1) & 1, kh = w >> 2;
  const int pos = 64 * rb + 32 * pt + r, head = 4 * hg + 2 * hp;

  const char* qrow = ws + WS_Q + (size_t)pos * (DM * 2) + head * (DH * 2);
  const u32x4 qam = *(const u32x4*)(qrow + 16 * h), qbm = *(const u32x4*)(qrow + 36 + 16 * h);
  const unsigned qa16 = *(const unsigned*)(qrow + 32), qb16 = *(const unsigned*)(qrow + 36 + 32);
  const char* k0p = ws + WS_KV + KV_HDR + kh * 1152;
  u32x4 kA = *(const u32x4*)(k0p + h * 512 + r * 16);
  u32x2 kAx = {*(const unsigned*)(k0p + 1024 + r * 4), 0x3f80u};
  float kmax2 = ((const float*)(ws + WS_KMAX))[lane];
#pragma unroll
  for (int o = 32; o; o >>= 1) kmax2 = fmaxf(kmax2, __shfl_xor(kmax2, o));
  auto sq = [](unsigned u) { return bflo(u) * bflo(u) + bfhi(u) * bfhi(u); };
  float na = sq(qam[0]) + sq(qam[1]) + sq(qam[2]) + sq(qam[3]) + (h ? 0.f : sq(qa16));
  float nb = sq(qbm[0]) + sq(qbm[1]) + sq(qbm[2]) + sq(qbm[3]) + (h ? 0.f : sq(qb16));
  na += xhalf(na); nb += xhalf(nb);
  const float ba = rbf(sqrtf(na * kmax2) * 1.01f + 1e-6f), bb = rbf(sqrtf(nb * kmax2) * 1.01f + 1e-6f);
  const bool fast = __all(ba <= FAST_BOUND && bb <= FAST_BOUND);

  KVAddr ad;
  {
    const int vr = r <= 18 ? r : r - 16;
    const unsigned tb = kh * KV_HALF;
    ad.k = tb + h * 512 + r * 16; ad.kx = tb + 1024 + r * 4; ad.v = tb + 1152 + h * 304 + vr * 16;
  }
  f32x16 oa = {}, ob = {};
  float ma, mb;
  if (fast) {
    ma = ba; mb = bb;
    u32x2 qax = {h ? 0u : qa16, h ? 0u : (cvtpk(-ba, 0.f))}, qbx = {h ? 0u : qb16, h ? 0u : (cvtpk(-bb, 0.f))};
    u32x4 kB, vA0, vA1, vB0, vB1;
    u32x2 kBx = {0u, 0x3f80u};
    f32x16 zero16 = {}, sa, sb;
    u32x8 pa, pb = {};
    sa = MFMA16(kA, qam, zero16); sa = MFMA8(kAx, qax, sa);
    STAMP(1, 1);
    SB();
    STAMP(1, 2);
    LDV(vB0, vB1, 0);
    unsigned off = 0;
#pragma unroll 1
    for (int s = 0; s < 8; ++s) {
      STEP(kA, kAx, kB, kBx, vB0, vB1, vA0, vA1, off);
      STEP(kB, kBx, kA, kAx, vA0, vA1, vB0, vB1, off + KV_TILE);
      STEP(kA, kAx, kB, kBx, vB0, vB1, vA0, vA1, off + 2 * KV_TILE);
      STEP(kB, kBx, kA, kAx, vA0, vA1, vB0, vB1, off + 3 * KV_TILE);
      off = off == 2 * KV_SEG ? 0 : off + KV_SEG;
      if (s < 7) BAR();
    }
    ob = MFMA16(vB0, lo4(pb), ob);
    ob = MFMA16(vB1, hi4(pb), ob);
  } else {
    ma = -INFINITY; mb = -INFINITY;
    const u32x2 qax = {h ? 0u : qa16, 0u}, qbx = {h ? 0u : qb16, 0u};
    kAx[1] = 0u;
#pragma unroll 1
    for (int t = 0; t < 32; ++t) {
      if ((t & 3) == 0) BAR();
      const unsigned off = ((t >> 2) % 3) * KV_SEG + (t & 3) * KV_TILE;
      u32x4 kn, v0, v1; u32x2 knx = {0u, 0u};
      LDK(kn, knx, off); LDV(v0, v1, off);
      slow_tile(kA, kAx, v0, v1, qam, qax, oa, ma);
      slow_tile(kA, kAx, v0, v1, qbm, qbx, ob, mb);
      kA = kn; kAx = knx;
    }
  }
  STAMP(1, 5);
  BAR();
  STAMP(1, 6);

  {
    const f32x16& give = kh ? oa : ob;
    const float gm = kh ? ma : mb;
    char* own = lds + LDS_CMB + w * 3328;
#pragma unroll
    for (int g = 0; g < 3; ++g) ldsw<f32x4>(own, g * 1024 + lane * 16, f32x4{give[4 * g], give[4 * g + 1], give[4 * g + 2], give[4 * g + 3]});
    ldsw<float>(own, 3072 + lane * 4, gm);
    BAR();
    const char* oth = lds + LDS_CMB + (w ^ 4) * 3328;
    f32x16 keep = kh ? ob : oa;
    const float m1 = kh ? mb : ma, m2 = ldsr<float>(oth, 3072 + lane * 4);
    const float mm = fmaxf(m1, m2), f1 = EX2(m1 - mm), f2 = EX2(m2 - mm);
    float o[12];
#pragma unroll
    for (int g = 0; g < 3; ++g) {
      const f32x4 t = ldsr<f32x4>(oth, g * 1024 + lane * 16);
#pragma unroll
      for (int e = 0; e < 4; ++e) o[4 * g + e] = keep[4 * g + e] * f1 + t[e] * f2;
    }
    const float l = __shfl(o[10], r);
    const float inv = 1.0f / l;
    char* orow = ws + WS_O + (size_t)pos * (DM * 2) + (head + kh) * (DH * 2);
    *(u32x2*)(orow + 8 * h) = u32x2{cvtpk(o[0] * inv, o[1] * inv), cvtpk(o[2] * inv, o[3] * inv)};
    *(u32x2*)(orow + 16 + 8 * h) = u32x2{cvtpk(o[4] * inv, o[5] * inv), cvtpk(o[6] * inv, o[7] * inv)};
    if (!h) *(unsigned*)(orow + 32) = cvtpk(o[8] * inv, o[9] * inv);
  }
  STAMP(1, 15);
}

#define K3_NSTG 4
constexpr int K3_LDS = K3_NSTG * GS_BYTES;
__global__ void __launch_bounds__(256, 1)
k3_fin(const u16* wo, const u16* u, const u16* dskip, char* ws, u16* out) {
  extern __shared__ __attribute__((aligned(16))) char lds[];
  const int b = BID, lane = TID & 63, w = rfl(TID >> 6);
  STAMP(2, 0);
  if (b < 256) {
    const int x8 = b & 7, i = b >> 3, id = x8 * 32 + i;
    const int tile = ((id >> 5) * 234 >> 3) + (id & 31);
    const int tend = (((id >> 5) + 1) * 234 >> 3);
    if (tile < tend) {
      const int rb = tile / 9, cb = tile - 9 * rb;
      gemm80<K3_NSTG>((const u16*)(ws + WS_O), 80 * rb, wo + 64 * cb, out + 64 * cb, 2 * DM, lds, w, lane, ws, 2);
    }
  } else {
    const int tile = b - 256, tb = tile / 9, cb = tile - 9 * tb;
    const unsigned lds0 = (unsigned)(uintptr_t)lds + w * 4096;
    const char* ldw = lds + w * 4096;
    const u16* yT = (const u16*)(ws + WS_YT);
#pragma unroll
    for (int i = 0; i < 4; ++i) {
      const int row = 16 * i + (lane >> 2), c = lane & 3;
      glds16(yT + (size_t)(64 * cb + row) * SEQ + 128 * tb + 32 * w + 8 * (c ^ (((row >> 1) & 1) << 1)), lds0 + i * 1024);
    }
    const int g = lane >> 4, li = lane & 15, q = li >> 2, p = li & 3;
    u32x4 uu[4], dd[2];
#pragma unroll
    for (int e = 0; e < 2; ++e) dd[e] = *(const u32x4*)(dskip + 64 * cb + 8 * (4 * e + g));
#pragma unroll
    for (int tq = 0; tq < 2; ++tq)
#pragma unroll
      for (int e = 0; e < 2; ++e) uu[2 * tq + e] = *(const u32x4*)(u + (size_t)(128 * tb + 32 * w + 16 * tq + li) * DM + 64 * cb + 8 * (4 * e + g));
    WAITV(0);
#pragma unroll
    for (int tq = 0; tq < 2; ++tq) {
#pragma unroll
      for (int e = 0; e < 2; ++e) {
        const int oct = 4 * e + g, R0 = 8 * oct + q, R1 = R0 + 4;
        const s16x4 a0 = ldstr(ldw, R0 * 64 + ((tq ^ ((R0 >> 1) & 1)) * 32) + 8 * p);
        const s16x4 a1 = ldstr(ldw, R1 * 64 + ((tq ^ ((R1 >> 1) & 1)) * 32) + 8 * p);
        const u32x4 y = __builtin_bit_cast(u32x4, __builtin_shufflevector(a0, a1, 0, 1, 2, 3, 4, 5, 6, 7));
        u32x4 o;
#pragma unroll
        for (int k = 0; k < 4; ++k) {
          const unsigned sk = cvtpk(bflo(uu[2 * tq + e][k]) * bflo(dd[e][k]), bfhi(uu[2 * tq + e][k]) * bfhi(dd[e][k]));
          o[k] = cvtpk(bflo(y[k]) + bflo(sk), bfhi(y[k]) + bfhi(sk));
        }
        *(u32x4*)(out + (size_t)(128 * tb + 32 * w + 16 * tq + li) * (2 * DM) + DM + 64 * cb + 8 * oct) = o;
      }
    }
  }
  STAMP(2, 15);
}

#if defined(__HIP_DEVICE_COMPILE__)
#pragma clang attribute pop
#endif
extern "C" void kernel_launch(void* const* d_in, const int* in_sizes, int n_in, void* d_out, int out_size, void* d_ws, size_t ws_size,
                              hipStream_t stream) {
  const u16* x = (const u16*)d_in[0];
  const u16* wq = (const u16*)d_in[1];
  const u16* wk = (const u16*)d_in[2];
  const u16* wv = (const u16*)d_in[3];
  const u16* wo = (const u16*)d_in[4];
  const u16* u = (const u16*)d_in[5];
  const u16* Are = (const u16*)d_in[6];
  const u16* Aim = (const u16*)d_in[7];
  const u16* Bre = (const u16*)d_in[8];
  const u16* Bim = (const u16*)d_in[9];
  const u16* Cre = (const u16*)d_in[10];
  const u16* Cim = (const u16*)d_in[11];
  const u16* logdt = (const u16*)d_in[12];
  const u16* dskip = (const u16*)d_in[13];
  char* ws = (char*)d_ws;
  (void)hipFuncSetAttribute((const void*)k1_prep, hipFuncAttributeMaxDynamicSharedMemorySize, K1_LDS);
  (void)hipFuncSetAttribute((const void*)k2_main, hipFuncAttributeMaxDynamicSharedMemorySize, K2_LDS);
  (void)hipFuncSetAttribute((const void*)k3_fin, hipFuncAttributeMaxDynamicSharedMemorySize, K3_LDS);
  k1_prep<<<512, 256, K1_LDS, stream>>>(x, wq, wk, wv, u, Are, Aim, Bre, Bim, Cre, Cim, logdt, ws);
  k2_main<<<256, 768, K2_LDS, stream>>>(ws);
  k3_fin<<<400, 256, K3_LDS, stream>>>(wo, u, dskip, ws, (u16*)d_out);
}
```

```cpp
#if defined(__HIP_DEVICE_COMPILE__)
#pragma clang attribute push(__attribute__((target("no-packed-fp32-ops"))), apply_to = function)
#endif
#include <hip/hip_runtime.h>
#include <stdint.h>

typedef __bf16 bf16x2 __attribute__((ext_vector_type(2)));
typedef __bf16 bf16x8 __attribute__((ext_vector_type(8)));
typedef short s16x4 __attribute__((ext_vector_type(4)));
typedef float f32x2 __attribute__((ext_vector_type(2)));
typedef float f32x4 __attribute__((ext_vector_type(4)));
typedef float f32x16 __attribute__((ext_vector_type(16)));
typedef unsigned u32x2 __attribute__((ext_vector_type(2)));
typedef unsigned u32x4 __attribute__((ext_vector_type(4)));
typedef unsigned short u16;
#define DI __device__ __forceinline__
#define BID ((int)__builtin_amdgcn_workgroup_id_x())
#define TID ((int)__builtin_amdgcn_workitem_id_x())
#define LDSP __attribute__((address_space(3)))
#define SBAR() __builtin_amdgcn_sched_barrier(0)
#define PIN(x) asm volatile("" : "+v"(x))
#define MFMA16(a, b, c) __builtin_amdgcn_mfma_f32_32x32x16_bf16(__builtin_bit_cast(bf16x8, a), __builtin_bit_cast(bf16x8, b), c, 0, 0, 0)
#define MFMA8(a, b, c) __builtin_amdgcn_mfma_f32_32x32x8bf16_1k(__builtin_bit_cast(s16x4, a), __builtin_bit_cast(s16x4, b), c, 0, 0, 0)

constexpr int SEQ = 2048, DM = 576, DH = 18, NST = 128;
constexpr int KV_VP = 320;
constexpr int KV_TILE = 1152 + 4 * KV_VP;
constexpr int KV_HALF = 4 * KV_TILE, KV_SEG = 2 * KV_HALF;
constexpr int KV_HDR = 8 * KV_SEG;
constexpr int KR_PAR = 4288, KR_CH = 2 * KR_PAR;
constexpr int UT_ROW = 2064;
constexpr size_t WS_Q = 0;
constexpr size_t WS_KV = WS_Q + (size_t)SEQ * DM * 2;
constexpr size_t WS_KMAX = WS_KV + 163840;
constexpr size_t WS_ZERO = WS_KMAX + 256;
constexpr size_t WS_CNT = WS_ZERO + 256;
constexpr size_t WS_O = WS_CNT + 8192;
constexpr size_t WS_KR = WS_O + (size_t)SEQ * DM * 2;
constexpr size_t WS_UT = WS_KR + (size_t)DM * KR_CH;
constexpr size_t WS_YT = WS_UT + (size_t)DM * UT_ROW * 2;

DI unsigned cvtpk(float a, float b) { f32x2 v = {a, b}; return __builtin_bit_cast(unsigned, __builtin_convertvector(v, bf16x2)); }
DI float bflo(unsigned u) { return __uint_as_float(u << 16); }
DI float bfhi(unsigned u) { return __uint_as_float(u & 0xffff0000u); }
DI float bf2f(u16 x) { return __uint_as_float((unsigned)x << 16); }
DI float rbf(float x) { return bflo(cvtpk(x, x)); }
DI int crow(int reg, int h) { return (reg & 3) + 8 * (reg >> 2) + 4 * h; }
DI void glds16(const void* g, unsigned lds_base) {
  unsigned sv;
  asm volatile("s_mov_b32 %0, m0\n\ts_mov_b32 m0, %2\n\ts_nop 0\n\tglobal_load_lds_dwordx4 %1, off\n\ts_mov_b32 m0, %0"
               : "=&s"(sv) : "v"(g), "s"(lds_base) : "memory");
}
DI void glds16s(const void* sbase, unsigned voff, unsigned lds_base) {
  unsigned sv;
  asm volatile("s_mov_b32 %0, m0\n\ts_mov_b32 m0, %3\n\ts_nop 0\n\tglobal_load_lds_dwordx4 %1, %2\n\ts_mov_b32 m0, %0"
               : "=&s"(sv) : "v"(voff), "s"(sbase), "s"(lds_base) : "memory");
}
#define WAITV(n) asm volatile("s_waitcnt vmcnt(" #n ")" ::: "memory")
#define WAITV_BAR(n) asm volatile("s_waitcnt vmcnt(" #n ") lgkmcnt(0)\n\ts_barrier" ::: "memory")
#define SB() asm volatile("s_barrier" ::: "memory")
#define BAR() asm volatile("s_waitcnt lgkmcnt(0)\n\ts_barrier" ::: "memory")
#define STAMP(kern, i)
DI unsigned rfl(unsigned x) { return __builtin_amdgcn_readfirstlane(x); }
template <typename T> DI T ldsr(const char* lds, unsigned off) { return *(const LDSP T*)((const LDSP char*)lds + off); }
template <typename T> DI void ldsw(char* lds, unsigned off, T v) { *(LDSP T*)((LDSP char*)lds + off) = v; }
DI s16x4 ldstr(const char* lds, unsigned off) {
  typedef short v4i16_t __attribute__((ext_vector_type(4)));
  return __builtin_bit_cast(s16x4, __builtin_amdgcn_ds_read_tr16_b64_v4i16((LDSP v4i16_t*)((LDSP char*)lds + off)));
}
DI float half_sum(float v) {
  const auto t = __builtin_amdgcn_permlane32_swap(__float_as_uint(v), __float_as_uint(v), false, false);
  return __uint_as_float(t[0]) + __uint_as_float(t[1]);
}
DI float half_max(float v) {
  const auto t = __builtin_amdgcn_permlane32_swap(__float_as_uint(v), __float_as_uint(v), false, false);
  return fmaxf(__uint_as_float(t[0]), __uint_as_float(t[1]));
}
DI float half_lo(float v) {
  const auto t = __builtin_amdgcn_permlane32_swap(__float_as_uint(v), __float_as_uint(v), false, false);
  return __uint_as_float(t[0]);
}
template <int CTRL> DI float dpp_max(float v) {
  return fmaxf(v, __uint_as_float(__builtin_amdgcn_update_dpp(__float_as_uint(v), __float_as_uint(v), CTRL, 0xf, 0xf, false)));
}
DI float wave_max(float v) {
  v = dpp_max<0xb1>(v);
  v = dpp_max<0x4e>(v);
  v = dpp_max<0x141>(v);
  v = dpp_max<0x140>(v);
  const unsigned u = __float_as_uint(v);
  const float a = __uint_as_float(__builtin_amdgcn_readlane(u, 0)), b = __uint_as_float(__builtin_amdgcn_readlane(u, 16));
  const float c = __uint_as_float(__builtin_amdgcn_readlane(u, 32)), d = __uint_as_float(__builtin_amdgcn_readlane(u, 48));
  return fmaxf(fmaxf(a, b), fmaxf(c, d));
}

constexpr int QS = 18432;
DI int wswz(int row) { return ((row >> 1) & 1) | (((row >> 3) & 1) << 1); }
template <int NP, int S> DI void q_issue(const char* wb, const char* xb, int xrows, const unsigned (&vw)[5], const unsigned (&vx)[5], int lw, unsigned lds0) {
#pragma unroll
  for (int j = 0; j < NP; ++j) {
    const int p = lw + 4 * j;
    if constexpr (S < 4) glds16s(wb + (size_t)144 * S * (DM * 2), vw[j], lds0 + (S & 3) * QS + 1024 * p);
    else glds16s(xb + (size_t)(16 * (S - 4) < xrows ? 16 * (S - 4) : xrows) * (DM * 2), vx[j], lds0 + (S & 3) * QS + 1024 * p);
  }
}
template <int NP, int S> DI void q_load_steps(const char* wb, const char* xb, int xrows, const unsigned (&vw)[5], const unsigned (&vx)[5], int lw, unsigned lds0) {
  constexpr int inflight = (S + 2 > 9 ? 9 : S + 2) - S;
  asm volatile("s_waitcnt vmcnt(%0)\n\ts_barrier" ::"i"(NP * inflight) : "memory");
  if constexpr (S + 3 < 10) q_issue<NP, S + 3>(wb, xb, xrows, vw, vx, lw, lds0);
  if constexpr (S + 1 < 10) q_load_steps<NP, S + 1>(wb, xb, xrows, vw, vx, lw, lds0);
}
template <int NP> DI void q_loader(const u16* x, int p0, const u16* W, int lw, int lane, unsigned lds0) {
  unsigned vw[5], vx[5];
#pragma unroll
  for (int j = 0; j < NP; ++j) {
    const int p = lw + 4 * j;
    { const int row = 8 * p + (lane >> 3), c = lane & 7; vw[j] = row * (DM * 2) + 16 * (c ^ (wswz(row) << 1)); }
    { const int n = 64 * p + lane, row = n / 72, c = n - 72 * row; vx[j] = row * (DM * 2) + 16 * ((c & ~7) | ((c ^ (row >> 1)) & 7)); }
  }
  const int xrows = SEQ - 16 - p0;
  const char* wb = (const char*)W;
  const char* xb = (const char*)(x + (size_t)p0 * DM);
  q_issue<NP, 0>(wb, xb, xrows, vw, vx, lw, lds0);
  q_issue<NP, 1>(wb, xb, xrows, vw, vx, lw, lds0);
  q_issue<NP, 2>(wb, xb, xrows, vw, vx, lw, lds0);
  q_load_steps<NP, 0>(wb, xb, xrows, vw, vx, lw, lds0);
}
DI void q_block(const u16* x, int p0, const u16* W, u16* C, char* lds, int w, int lane) {
  if (w >= 4) {
    const unsigned lds0 = (unsigned)(uintptr_t)lds;
    if (w == 4) q_loader<5>(x, p0, W, 0, lane, lds0);
    else if (w == 5) q_loader<5>(x, p0, W, 1, lane, lds0);
    else if (w == 6) q_loader<4>(x, p0, W, 2, lane, lds0);
    else q_loader<4>(x, p0, W, 3, lane, lds0);
    return;
  }
  const int li = lane & 15, g = lane >> 4, q = li >> 2, p = lane & 3;
  u32x4 wf[18];
#pragma unroll
  for (int s = 0; s < 4; ++s) {
    BAR();
#pragma unroll
    for (int ks = 0; ks < 18; ++ks) {
#pragma unroll
      for (int t = 0; t < 2; ++t) {
        const int half = 2 * ks;
        const bool lo = half / 9 == s, hi = (half + 1) / 9 == s;
        if (lo || hi) {
          const int R = 8 * (g & 1) + 4 * t + q;
          const int grp = (g >> 1) ? (hi ? half + 1 - 9 * s : 0) : (lo ? half - 9 * s : 0);
          const s16x4 v = ldstr(lds, s * QS + (16 * grp + R) * 128 + ((w ^ wswz(R)) * 32) + 8 * p);
          const bool mine = (g >> 1) ? hi : lo;
          const u32x2 vv = __builtin_bit_cast(u32x2, v);
          if (lo && hi) { wf[ks][2 * t] = vv[0]; wf[ks][2 * t + 1] = vv[1]; }
          else if (lo) { wf[ks][2 * t] = vv[0]; wf[ks][2 * t + 1] = vv[1]; }
          else { wf[ks][2 * t] = mine ? vv[0] : wf[ks][2 * t]; wf[ks][2 * t + 1] = mine ? vv[1] : wf[ks][2 * t + 1]; }
        }
      }
    }
  }
  const unsigned xa = li * (DM * 2);
  const int xs = (li >> 1) & 7;
#pragma unroll 1
  for (int pt = 0; pt < 6; ++pt) {
    BAR();
    const unsigned sb = (pt & 3) * QS + xa;
    f32x4 acc = {};
    u32x4 bf[9];
#define XRD(ks) ldsr<u32x4>(lds, sb + 16 * (((4 * (ks) + g) & ~7) | (((4 * (ks) + g) ^ xs) & 7)))
#pragma unroll
    for (int ks = 0; ks < 9; ++ks) bf[ks] = XRD(ks);
    SBAR();
#pragma unroll
    for (int ks = 0; ks < 18; ++ks) {
      acc = __builtin_amdgcn_mfma_f32_16x16x32_bf16(__builtin_bit_cast(bf16x8, wf[ks]), __builtin_bit_cast(bf16x8, bf[ks % 9]), acc, 0, 0, 0);
      SBAR();
      if (ks < 9) bf[ks] = XRD(ks + 9);
      SBAR();
    }
#undef XRD
    const int row = p0 + 16 * pt + li;
    if (row < SEQ) *(u32x2*)(C + (size_t)row * DM + 16 * w + 4 * g) = u32x2{cvtpk(acc[0], acc[1]), cvtpk(acc[2], acc[3])};
  }
}

DI f32x2 cmul(f32x2 a, f32x2 b) { return f32x2{__builtin_fmaf(a[0], b[0], -(a[1] * b[1])), __builtin_fmaf(a[0], b[1], a[1] * b[0])}; }
DI f32x2 cexp_l(float zr, float zi, float l) {
  const float mag = __builtin_amdgcn_exp2f((zr * l) * 1.4426950408889634f);
  const float rev = __builtin_amdgcn_fractf((zi * l) * 0.15915494309189535f);
  return f32x2{mag * __builtin_amdgcn_cosf(rev), mag * __builtin_amdgcn_sinf(rev)};
}
constexpr int GEN_PITCH = 272, GEN_X = 0, GEN_Y = 32 * GEN_PITCH, GEN_WAVE = 14592;

DI void gen_wave(int d, const u16* Are, const u16* Aim, const u16* Bre, const u16* Bim, const u16* Cre, const u16* Cim,
                  const u16* logdt, char* ws, char* lds, int lane) {
  const int r = lane & 31, h = lane >> 5;
  u16 pr[2][6];
#pragma unroll
  for (int pass = 0; pass < 2; ++pass) {
    const int idx = d * NST + 64 * pass + lane;
    pr[pass][0] = Are[idx]; pr[pass][1] = Aim[idx]; pr[pass][2] = Bre[idx]; pr[pass][3] = Bim[idx]; pr[pass][4] = Cre[idx]; pr[pass][5] = Cim[idx];
  }
  const float dt = rbf(expf(bf2f(logdt[d])));
  f32x16 acc = {};
#pragma unroll
  for (int pass = 0; pass < 2; ++pass) {
    const float zr = dt * bf2f(pr[pass][0]), zi = dt * bf2f(pr[pass][1]);
    const float dBr = dt * bf2f(pr[pass][2]), dBi = dt * bf2f(pr[pass][3]);
    const float cr = bf2f(pr[pass][4]), ci = bf2f(pr[pass][5]);
    const f32x2 c = {cr * dBr - ci * dBi, cr * dBi + ci * dBr};
    const f32x2 w1 = cexp_l(zr, zi, 1.f), w256 = cexp_l(zr, zi, 256.f);
    const f32x2 v8 = cexp_l(zr, -zi, 8.f), v16 = cmul(v8, v8);
#define PUT(off, v) ldsw<unsigned>(lds, (off) + 4 * lane, cvtpk((v)[0], (v)[1]))
    PUT(GEN_X, (f32x2{1.f, 0.f})); PUT(GEN_X + GEN_PITCH, v8); PUT(GEN_X + 2 * GEN_PITCH, v16);
    f32x2 xe = v16, xo = v8;
#pragma unroll
    for (int m = 3; m < 32; m += 2) {
      xo = cmul(xo, v16); PUT(GEN_X + m * GEN_PITCH, xo);
      if (m < 31) { xe = cmul(xe, v16); PUT(GEN_X + (m + 1) * GEN_PITCH, xe); }
    }
#define PUTY(col, v) PUT(GEN_Y + (col) * GEN_PITCH, v)
    f32x2 y[8];
    y[0] = c;
#pragma unroll
    for (int i = 1; i < 8; ++i) y[i] = cmul(y[i - 1], w1);
#pragma unroll
    for (int i = 0; i < 8; ++i) PUTY(i, y[i]);
#pragma unroll
    for (int i = 0; i < 4; ++i) { const f32x2 t = cmul(y[2 * i], w256); PUTY(8 + i, t); }
    const f32x2 p2 = cmul(w256, w256), p3 = cmul(p2, w256), p4 = cmul(p2, p2);
    { f32x2 t = cmul(y[0], p2); PUTY(12, t); t = cmul(y[4], p2); PUTY(13, t); }
    { f32x2 t = cmul(y[0], p3); PUTY(14, t); t = cmul(y[4], p3); PUTY(15, t); }
    { const f32x2 t4 = cmul(y[0], p4); PUTY(16, t4);
      f32x2 t = cmul(t4, w256); PUTY(17, t); t = cmul(t4, p2); PUTY(18, t); t = cmul(t4, p3); PUTY(19, t); t = cmul(t4, p4); PUTY(20, t); }
#undef PUTY
#undef PUT
    asm volatile("" ::: "memory");
    const int yr = r <= 20 ? r : r - 16;
    u32x4 fa[8], fb[8];
#pragma unroll
    for (int ks = 0; ks < 8; ++ks) {
      fa[ks] = ldsr<u32x4>(lds, GEN_X + r * GEN_PITCH + 32 * ks + 16 * h);
      fb[ks] = ldsr<u32x4>(lds, GEN_Y + yr * GEN_PITCH + 32 * ks + 16 * h);
    }
#pragma unroll
    for (int ks = 0; ks < 8; ++ks) acc = MFMA16(fa[ks], fb[ks], acc);
    asm volatile("" ::: "memory");
  }
  {
    const int c = r;
    const int hi = c < 8 ? c : c < 12 ? 256 + 2 * (c - 8) : c < 16 ? 512 + 256 * ((c - 12) >> 1) + 4 * ((c - 12) & 1) : 1024 + 256 * (c - 16);
    if (c < 20) {
#pragma unroll
      for (int i = 0; i < 16; ++i) ldsw<u16>(lds, 2 * (8 * crow(i, h) + hi), (u16)cvtpk(acc[i], acc[i]));
    } else if (c == 20 && h == 0) {
      ldsw<u16>(lds, 2 * 2048, (u16)cvtpk(acc[0], acc[0]));
    }
  }
  asm volatile("s_waitcnt lgkmcnt(0)" ::: "memory");
  unsigned* E = (unsigned*)(ws + WS_KR + (size_t)d * KR_CH);
  unsigned* Od = E + KR_PAR / 4;
  auto rd = [&](int l) -> unsigned { return ldsr<u16>(lds, 2 * l); };
  auto pk = [](unsigned lo, unsigned hi) -> unsigned { return lo | (hi << 16); };
  const int L = lane;
  {
    const unsigned e0 = rd(4 * L), e1 = rd(4 * L + 1), e2 = rd(4 * L + 2), e3 = rd(4 * L + 3);
    const unsigned pv = L ? rd(4 * L - 1) : 0u;
    const int p0 = 2 * L;
    *(u32x2*)(E + 1028 - p0) = u32x2{pk(e3, e2), pk(e1, e0)};
    *(u32x2*)(Od + 1028 - p0) = u32x2{pk(e2, e1), pk(e0, pv)};
  }
  {
    const int j0 = 256 + 4 * L;
    const unsigned a = rd(j0), b = rd(j0 + 2), c = rd(j0 + 4), pv = L ? a : rd(255);
    const int p0 = j0 / 2;
    *(u32x2*)(E + 1028 - p0) = u32x2{pk(c, b), pk(a, a)};
    *(u32x2*)(Od + 1028 - p0) = u32x2{pk(b, a), pk(a, pv)};
  }
  {
    const int j0 = 512 + 8 * L;
    const unsigned a = rd(j0), b = rd(j0 + 4), c = rd(j0 + 8);
    const unsigned aa = pk(a, a), bb = pk(b, b), cc = pk(c, c);
    unsigned* e = E + 1026 - j0 / 2;
    unsigned* o = Od + 1026 - j0 / 2;
    *(u32x2*)(e) = u32x2{cc, bb};          *(u32x2*)(e + 2) = u32x2{pk(b, a), aa};
    *(u32x2*)(o) = u32x2{pk(c, b), bb};    *(u32x2*)(o + 2) = u32x2{aa, aa};
  }
  {
    const int j0 = 1024 + 16 * L;
    const unsigned a = rd(j0), b = rd(j0 + 8), c = rd(j0 + 16);
    const unsigned aa = pk(a, a), bb = pk(b, b), cc = pk(c, c);
    unsigned* e = E + 1022 - j0 / 2;
    unsigned* o = Od + 1022 - j0 / 2;
    *(u32x2*)(e) = u32x2{cc, cc};          *(u32x2*)(e + 2) = u32x2{bb, bb};
    *(u32x2*)(e + 4) = u32x2{bb, pk(b, a)}; *(u32x2*)(e + 6) = u32x2{aa, aa};
    *(u32x2*)(o) = u32x2{cc, pk(c, b)};    *(u32x2*)(o + 2) = u32x2{bb, bb};
    *(u32x2*)(o + 4) = u32x2{bb, aa};      *(u32x2*)(o + 6) = u32x2{aa, aa};
    if (L == 63) Od[5] = pk(0u, c);
  }
  if (L < 6) E[L] = 0u;
  if (L < 5) Od[L] = 0u;
  if (L < 42) { E[1030 + L] = 0u; Od[1030 + L] = 0u; }
}

DI void kv_block(int kb, const u16* x, const u16* wk, const u16* wv, char* ws, char* lds, int w, int lane) {
  const int r = lane & 31, h = lane >> 5, tl = w >> 2, kq = w & 3, tid = 64 * w + lane;
  const u16* xr = x + (size_t)(64 * kb + 32 * tl + r) * DM + 144 * kq + 8 * h;
  u32x4 xf[9];
#pragma unroll
  for (int ks = 0; ks < 9; ++ks) xf[ks] = *(const u32x4*)(xr + 16 * ks);
  typedef unsigned u32x3 __attribute__((ext_vector_type(3)));
  u32x3 wl[7];
#pragma unroll
  for (int j = 0; j < 7; ++j) {
    const int e = tid + 512 * j, m = e >= 1728, e1 = e < 3456 ? e - 1728 * m : 0;
    __builtin_memcpy(&wl[j], (const char*)(m ? wv : wk) + 12 * e1, 12);
  }
#pragma unroll
  for (int j = 0; j < 7; ++j) {
    const int e = tid + 512 * j, m = e >= 1728, e1 = e - 1728 * m, k = e1 / 3, c = e1 - 3 * k;
    if (e < 3456) {
      const unsigned o = k * 128 + ((64 * m) ^ (((k >> 1) & 1) << 6)) + 12 * c;
      ldsw<unsigned>(lds, o, wl[j][0]); ldsw<unsigned>(lds, o + 4, wl[j][1]); ldsw<unsigned>(lds, o + 8, wl[j][2]);
    }
  }
  BAR();
  const int blk = (lane >> 4) & 1, q = (lane & 15) >> 2, p = lane & 3;
  f32x16 ak = {}, av = {};
#pragma unroll
  for (int ks = 0; ks < 9; ++ks) {
    const int R0 = 144 * kq + 16 * ks + 8 * h + q, R1 = R0 + 4;
    const unsigned c0 = blk * 32 + p * 8, s0 = ((R0 >> 1) & 1) << 6, s1 = ((R1 >> 1) & 1) << 6;
    const auto fk = __builtin_shufflevector(ldstr(lds, R0 * 128 + (c0 ^ s0)), ldstr(lds, R1 * 128 + (c0 ^ s1)), 0, 1, 2, 3, 4, 5, 6, 7);
    const auto fv = __builtin_shufflevector(ldstr(lds, R0 * 128 + ((64 + c0) ^ s0)), ldstr(lds, R1 * 128 + ((64 + c0) ^ s1)), 0, 1, 2, 3, 4, 5, 6, 7);
    ak = MFMA16(fk, xf[ks], ak);
    av = MFMA16(xf[ks], fv, av);
  }
  BAR();
#pragma unroll
  for (int g = 0; g < 4; ++g) {
    ldsw<f32x4>(lds, (2 * w) * 4096 + g * 1024 + lane * 16, f32x4{ak[4 * g], ak[4 * g + 1], ak[4 * g + 2], ak[4 * g + 3]});
    ldsw<f32x4>(lds, (2 * w + 1) * 4096 + g * 1024 + lane * 16, f32x4{av[4 * g], av[4 * g + 1], av[4 * g + 2], av[4 * g + 3]});
  }
  BAR();
  if (kq >= 2) return;
  f32x16 acc;
#pragma unroll
  for (int g = 0; g < 4; ++g) {
    f32x4 t = ldsr<f32x4>(lds, (8 * tl + kq) * 4096 + g * 1024 + lane * 16);
#pragma unroll
    for (int v = 1; v < 4; ++v) t += ldsr<f32x4>(lds, (8 * tl + 2 * v + kq) * 4096 + g * 1024 + lane * 16);
#pragma unroll
    for (int e = 0; e < 4; ++e) acc[4 * g + e] = t[e];
  }
  const int kt = 2 * kb + tl, kh = kt >> 5, t = kt & 31;
  char* kv = ws + WS_KV;
  if (kq == 0) {
    char* img = t ? kv + ((t - 1) >> 2) * KV_SEG + kh * KV_HALF + ((t - 1) & 3) * KV_TILE : kv + KV_HDR + kh * 1152;
    const float sc = 0.23570226039551584f * 1.4426950408889634f;
    const int e = (r & 0x13) | ((r & 4) << 1) | ((r & 8) >> 1);
    const unsigned d0 = cvtpk(acc[0] * sc, acc[1] * sc), d1 = cvtpk(acc[2] * sc, acc[3] * sc);
    const unsigned d2 = cvtpk(acc[4] * sc, acc[5] * sc), d3 = cvtpk(acc[6] * sc, acc[7] * sc);
    const unsigned d4 = h ? 0u : cvtpk(acc[8] * sc, acc[9] * sc);
    *(u32x2*)(img + e * 16 + 8 * h) = u32x2{d0, d1};
    *(u32x2*)(img + 512 + e * 16 + 8 * h) = u32x2{d2, d3};
    if (!h) *(unsigned*)(img + 1024 + e * 4) = d4;
    float n2 = bflo(d0) * bflo(d0) + bfhi(d0) * bfhi(d0) + bflo(d1) * bflo(d1) + bfhi(d1) * bfhi(d1) + bflo(d2) * bflo(d2) + bfhi(d2) * bfhi(d2) +
               bflo(d3) * bflo(d3) + bfhi(d3) * bfhi(d3) + bflo(d4) * bflo(d4) + bfhi(d4) * bfhi(d4);
    n2 = wave_max(half_sum(n2));
    if (lane == 0) ((float*)(ws + WS_KMAX))[kt] = n2;
  } else if (r <= DH + 1) {
    char* img = kv + (t >> 2) * KV_SEG + kh * KV_HALF + (t & 3) * KV_TILE + 1152;
#pragma unroll
    for (int g = 0; g < 4; ++g) {
      u32x2 v = {cvtpk(acc[4 * g], acc[4 * g + 1]), cvtpk(acc[4 * g + 2], acc[4 * g + 3])};
      if (r == DH) v = u32x2{0x3f803f80u, 0x3f803f80u};
      if (r == DH + 1) v = u32x2{0u, 0u};
      *(u32x2*)(img + g * KV_VP + r * 16 + 8 * h) = v;
    }
  }
}

DI void ut_wave(int tile, const u16* u, char* ws, char* lds, int lane) {
  const unsigned lds0 = (unsigned)(uintptr_t)lds;
  const int tb = tile / 9, cb = tile - 9 * tb;
#pragma unroll
  for (int i = 0; i < 16; ++i) {
    const int row = 8 * i + (lane >> 3), c = lane & 7;
    glds16(u + (size_t)(128 * tb + row) * DM + 64 * cb + 8 * (c ^ (wswz(row) << 1)), lds0 + i * 1024);
  }
  u16* uT = (u16*)(ws + WS_UT);
  if (tb == 0 || tb == 15) *(u32x4*)(uT + (size_t)(64 * cb + lane) * UT_ROW + (tb ? 8 + SEQ : 0)) = u32x4{0, 0, 0, 0};
  WAITV(0);
  const int g = lane >> 4, li = lane & 15, q = li >> 2, p = li & 3;
#pragma unroll
  for (int db = 0; db < 4; ++db) {
#pragma unroll
    for (int e = 0; e < 4; ++e) {
      const int oct = 4 * e + g, R0 = 8 * oct + q, R1 = R0 + 4;
      const s16x4 a0 = ldstr(lds, R0 * 128 + ((db ^ wswz(R0)) * 32) + 8 * p), a1 = ldstr(lds, R1 * 128 + ((db ^ wswz(R1)) * 32) + 8 * p);
      const auto v = __builtin_shufflevector(a0, a1, 0, 1, 2, 3, 4, 5, 6, 7);
      *(u32x4*)(uT + (size_t)(64 * cb + 16 * db + li) * UT_ROW + 8 + 128 * tb + 8 * oct) = __builtin_bit_cast(u32x4, v);
    }
  }
}

constexpr int K1_LDS = 77824;
__global__ void __launch_bounds__(512, 4)
k1_prep(const u16* x, const u16* wq, const u16* wk, const u16* wv, const u16* u, const u16* Are, const u16* Aim, const u16* Bre,
        const u16* Bim, const u16* Cre, const u16* Cim, const u16* logdt, char* ws) {
  extern __shared__ __attribute__((aligned(16))) char lds[];
  const int b = BID, lane = TID & 63, w = rfl(TID >> 6);
  STAMP(0, 0);
  if (b < 200) {
    const int tile = (b & 7) * 25 + (b >> 3), rb = tile / 9, cb = tile - 9 * rb;
    if (tile < 198) q_block(x, 96 * rb, wq + 64 * cb, (u16*)(ws + WS_Q) + 64 * cb, lds, w, lane);
  } else if (b < 232) {
    kv_block(4 * (b & 7) + ((b - 200) >> 3), x, wk, wv, ws, lds, w, lane);
  } else if (b >= 256) {
    const int j = b - 256;
    if (w < 4) gen_wave(4 * j + w, Are, Aim, Bre, Bim, Cre, Cim, logdt, ws, lds + w * GEN_WAVE, lane);
    else if (w == 4) ut_wave(j, u, ws, lds + 4 * GEN_WAVE, lane);
  } else if (b == 255 && w == 0) {
    ((unsigned*)(ws + WS_ZERO))[lane] = 0u;
    if (lane < 40) ((unsigned*)(ws + WS_CNT))[32 * lane] = 0u;
  }
  STAMP(0, 15);
}

constexpr int LDS_CV = 3 * KV_SEG;
constexpr int CV_CH = 31744, CV_K = 4416, CV_U = 17664, CV_UP = 7040;
constexpr int LDS_CMB = 0;
constexpr int LDS_OT = 8 * 3328;
constexpr int LDS_FA = LDS_CV + 82944, LDS_FB = LDS_FA + 9216;
constexpr int LDS_SCR = LDS_CV + 3 * CV_CH;
constexpr int LDS_FLAG = LDS_SCR + 4 * 2048;
constexpr int K2_LDS = LDS_FLAG + 128;
constexpr float FAST_BOUND = 30.f;
#define CONV_PRIO 0
constexpr int kRsrc3 = 0x00020000;
typedef __attribute__((address_space(1))) unsigned gu32;

struct KVAddr { unsigned k, kx, v; };
#define LDK(dst, dstx, t_off) do { dst = ldsr<u32x4>(lds, ad.k + (t_off)); dstx[0] = ldsr<unsigned>(lds, ad.kx + (t_off)); } while (0)
#define LDV(d0, d1, t_off) do { d0 = ldsr<u32x4>(lds, ad.v + (t_off)); d1 = ldsr<u32x4>(lds, ad.v + 2 * KV_VP + (t_off)); } while (0)
#define EX2(x) __builtin_amdgcn_exp2f(x)
#define GRP(MF, S, P, i) do { MF; SBAR(); \
    { const float e0 = EX2(S[4 * (i)]), e1 = EX2(S[4 * (i) + 1]), e2 = EX2(S[4 * (i) + 2]), e3 = EX2(S[4 * (i) + 3]); \
      unsigned t0_ = cvtpk(e0, e1), t1_ = cvtpk(e2, e3); PIN(t0_); PIN(t1_); P[2 * (i)] = t0_; P[2 * (i) + 1] = t1_; } \
    SBAR(); } while (0)
typedef unsigned u32x8 __attribute__((ext_vector_type(8)));
DI u32x4 lo4(const u32x8& p) { return __builtin_shufflevector(p, p, 0, 1, 2, 3); }
DI u32x4 hi4(const u32x8& p) { return __builtin_shufflevector(p, p, 4, 5, 6, 7); }

#define STEP(Kc, Kcx, Kn, Knx, Vp0, Vp1, Vc0, Vc1, off) do { \
    LDK(Kn, Knx, (off)); LDV(Vc0, Vc1, (off)); SBAR(); \
    GRP(sb = MFMA16(Kc, qbm, zero16), sa, pa, 0); \
    GRP(ob = MFMA16(Vp0, lo4(pb), ob), sa, pa, 1); \
    GRP(sb = MFMA8(Kcx, qbx, sb), sa, pa, 2); \
    GRP(ob = MFMA16(Vp1, hi4(pb), ob), sa, pa, 3); \
    GRP(sa = MFMA16(Kn, qam, zero16), sb, pb, 0); \
    GRP(oa = MFMA16(Vc0, lo4(pa), oa), sb, pb, 1); \
    GRP(sa = MFMA8(Knx, qax, sa), sb, pb, 2); \
    GRP(oa = MFMA16(Vc1, hi4(pa), oa), sb, pb, 3); \
  } while (0)

DI void slow_tile(const u32x4& kf, const u32x2& kx, const u32x4& v0, const u32x4& v1, const u32x4& qm, const u32x2& qx, f32x16& o, float& m) {
  f32x16 s = {};
  s = MFMA16(kf, qm, s);
  s = MFMA8(kx, qx, s);
  float mx = s[0];
#pragma unroll
  for (int i = 1; i < 16; ++i) mx = fmaxf(mx, s[i]);
  mx = half_max(mx);
  const float mn = fmaxf(m, mx);
  const float alpha = EX2(m - mn);
  m = mn;
#pragma unroll
  for (int i = 0; i < 16; ++i) o[i] *= alpha;
  u32x8 p;
#pragma unroll
  for (int i = 0; i < 8; ++i) p[i] = cvtpk(EX2(s[2 * i] - mn), EX2(s[2 * i + 1] - mn));
  o = MFMA16(v0, lo4(p), o);
  o = MFMA16(v1, hi4(p), o);
}

constexpr int F_FULL = 0, F_SEG0 = 1, F_CFILL = 2, F_CDONE = 3, F_DONE = 8;
DI void flag_add(char* lds, int word) { __hip_atomic_fetch_add((LDSP unsigned*)((LDSP char*)lds + LDS_FLAG) + word, 1u, __ATOMIC_RELAXED, __HIP_MEMORY_SCOPE_WORKGROUP); }
DI void flag_set(char* lds, int word, unsigned v) {
  asm volatile("" ::: "memory");
  __hip_atomic_store((LDSP unsigned*)((LDSP char*)lds + LDS_FLAG) + word, v, __ATOMIC_RELAXED, __HIP_MEMORY_SCOPE_WORKGROUP);
  asm volatile("" ::: "memory");
}
DI unsigned flag_get(char* lds, int word) { return __hip_atomic_load((LDSP unsigned*)((LDSP char*)lds + LDS_FLAG) + word, __ATOMIC_RELAXED, __HIP_MEMORY_SCOPE_WORKGROUP); }
template <int SLEEP> DI void lds_wait(char* lds, int word, unsigned need) {
#pragma unroll 1
  for (unsigned spins = 0; spins < (1u << 20); ++spins) {
    if (rfl(flag_get(lds, word)) >= need) break;
    __builtin_amdgcn_s_sleep(SLEEP);
  }
  asm volatile("" ::: "memory");
}
template <int SLEEP> DI void done_wait(char* lds, unsigned need, int lane) {
#pragma unroll 1
  for (unsigned spins = 0; spins < (1u << 20); ++spins) {
    if (__builtin_amdgcn_ballot_w64(flag_get(lds, F_DONE + (lane & 7)) < need) == 0) break;
    __builtin_amdgcn_s_sleep(SLEEP);
  }
  asm volatile("" ::: "memory");
}
#define ONE_LANE(stmt) do { asm volatile("" ::: "memory"); if (lane == 0) { stmt; } asm volatile("" ::: "memory"); } while (0)

template <int SW> DI void conv_fill(int d, const char* ws, unsigned cl0, int lane) {
  const char* kr = ws + WS_KR + (size_t)d * KR_CH;
  const unsigned ur = (unsigned)(WS_UT - WS_ZERO) + d * (UT_ROW * 2);
#pragma unroll
  for (int item = SW; item < 34; item += 4) {
    if (item < 20) {
      const int m = item / 5, part = item - 5 * m;
      if (part < 4 || lane < 11) glds16s(kr + (m & 1) * KR_PAR + (m >> 1) * 4 + part * 1024, 16 * lane, cl0 + m * CV_K + part * 1024);
    } else {
      const int bb = (item - 20) / 7, part = item - 20 - 7 * bb, sg = part * 64 + lane, qc = sg - sg / 9, s0 = 8 * qc - 976 + 4 * bb;
      const unsigned voff = (s0 < -8 || sg % 9 == 8) ? 0u : ur + 2 * (8 + s0);
      if (part < 6 || lane <= 40) glds16s(ws + WS_ZERO, voff, cl0 + CV_U + bb * CV_UP + part * 1024);
    }
  }
}
template <int SW> DI void conv_fill3(int d0, int dx, bool third, const char* ws, unsigned lds0, int lane) {
  conv_fill<SW>(d0, ws, lds0 + LDS_CV, lane);
  conv_fill<(SW + 2) & 3>(d0 + 1, ws, lds0 + LDS_CV + CV_CH, lane);
  if (third) conv_fill<SW>(dx, ws, lds0 + LDS_CV + 2 * CV_CH, lane);
}
DI void conv_fill_share(int d0, int dx, bool third, int sw, const char* ws, unsigned lds0, int lane) {
  if (sw == 0) conv_fill3<0>(d0, dx, third, ws, lds0, lane);
  else if (sw == 1) conv_fill3<1>(d0, dx, third, ws, lds0, lane);
  else if (sw == 2) conv_fill3<2>(d0, dx, third, ws, lds0, lane);
  else conv_fill3<3>(d0, dx, third, ws, lds0, lane);
}
template <bool TWO, bool SKIP0> DI void conv_steps(const char* lds, unsigned pa0, unsigned pb0, unsigned pa1, unsigned pb1, f32x16& c0, f32x16& c1) {
  __builtin_amdgcn_s_setprio(CONV_PRIO);
  u32x4 a0[2], b0[2], a1[2], b1[2];
#define CLD(i, n) do { a0[i] = ldsr<u32x4>(lds, pa0 + 32 * (n)); b0[i] = ldsr<u32x4>(lds, pb0 + 32 * (n) + 16 * ((n) >> 2)); \
    if (TWO) { a1[i] = ldsr<u32x4>(lds, pa1 + 32 * (n)); b1[i] = ldsr<u32x4>(lds, pb1 + 32 * (n) + 16 * ((n) >> 2)); } } while (0)
#define CMM(i) do { c0 = MFMA16(a0[i], b0[i], c0); if (TWO) c1 = MFMA16(a1[i], b1[i], c1); } while (0)
  CLD(0, 0); CLD(1, 1);
  c0 = MFMA16(a0[0], b0[0], c0);
  if (TWO && !SKIP0) c1 = MFMA16(a1[0], b1[0], c1);
  SBAR();
#pragma unroll 1
  for (int n4 = 0; n4 < 16; ++n4) {
    const unsigned oa = 128 * n4, ob = 144 * n4;
#define CLD4(i, k) do { a0[i] = ldsr<u32x4>(lds, pa0 + oa + 32 * (k)); b0[i] = ldsr<u32x4>(lds, pb0 + ob + 32 * (k) + 16 * ((k) >> 2)); \
    if (TWO) { a1[i] = ldsr<u32x4>(lds, pa1 + oa + 32 * (k)); b1[i] = ldsr<u32x4>(lds, pb1 + ob + 32 * (k) + 16 * ((k) >> 2)); } } while (0)
    CLD4(0, 2); SBAR(); CMM(1); SBAR();
    CLD4(1, 3); SBAR(); CMM(0); SBAR();
    CLD4(0, 4); SBAR(); CMM(1); SBAR();
    CLD4(1, 5); SBAR(); CMM(0); SBAR();
#undef CLD4
  }
#undef CLD
#undef CMM
  __builtin_amdgcn_s_setprio(3);
}
DI void conv_out(const f32x16& c, int d, int tl, char* ws, char* scratch, int lane) {
  const int r = lane & 31, h = lane >> 5;
#pragma unroll
  for (int g = 0; g < 4; ++g)
    ldsw<u32x2>(scratch, 2 * (16 * g + 8 * h + 4 * (r & 1) + 64 * (r >> 1)), u32x2{cvtpk(c[4 * g], c[4 * g + 1]), cvtpk(c[4 * g + 2], c[4 * g + 3])});
  const __amdgpu_buffer_rsrc_t rsY = __builtin_amdgcn_make_buffer_rsrc((void*)(ws + WS_YT + (size_t)d * (SEQ * 2) + 2048 * tl), 0, 2048, kRsrc3);
#pragma unroll
  for (int j = 0; j < 2; ++j) __builtin_amdgcn_raw_buffer_store_b128(ldsr<u32x4>(scratch, 1024 * j + 16 * lane), rsY, 1024 * j + 16 * lane, 0, 16);
}

DI void skip_tile(int rb, int hg, const u16* u, const u16* dskip, char* ws, u16* out, char* lds, int lane) {
  u32x4 y[9], uu[9];
#pragma unroll
  for (int j = 0; j < 9; ++j) {
    const int ch = 64 * j + lane, row = ch / 9, c = ch - 9 * row;
    uu[j] = *(const u32x4*)(u + (size_t)(64 * rb + row) * DM + 72 * hg + 8 * c);
  }
  gu32* cnt = (gu32*)(ws + WS_CNT) + 32 * (32 + hg);
#pragma unroll 1
  for (unsigned spins = 0; spins < (1u << 20); ++spins) {
    if (rfl(__hip_atomic_load(cnt, __ATOMIC_RELAXED, __HIP_MEMORY_SCOPE_AGENT)) == 32u) break;
    __builtin_amdgcn_s_sleep(4);
  }
  __builtin_amdgcn_fence(__ATOMIC_ACQUIRE, "wavefront");
  const __amdgpu_buffer_rsrc_t rsY = __builtin_amdgcn_make_buffer_rsrc((void*)(ws + WS_YT + (size_t)72 * hg * (SEQ * 2)), 0, 72 * SEQ * 2, kRsrc3);
#pragma unroll
  for (int j = 0; j < 9; ++j) {
    const int ch = 64 * j + lane, row = ch >> 3, c = ch & 7;
    y[j] = __builtin_amdgcn_raw_buffer_load_b128(rsY, row * (SEQ * 2) + 128 * rb + 16 * c, 0, 16);
  }
#pragma unroll
  for (int j = 0; j < 9; ++j) {
    const int ch = 64 * j + lane, row = ch >> 3, c = ch & 7;
    ldsw<u32x4>(lds, LDS_FA + row * 128 + 16 * (c ^ (wswz(row) << 1)), y[j]);
  }
  const int g = lane >> 4, li = lane & 15, q = li >> 2, p = lane & 3;
#pragma unroll
  for (int j = 0; j < 9; ++j) {
    const int R0 = 8 * j + q, R1 = R0 + 4;
    const s16x4 a0 = ldstr(lds, LDS_FA + R0 * 128 + ((g ^ wswz(R0)) * 32) + 8 * p), a1 = ldstr(lds, LDS_FA + R1 * 128 + ((g ^ wswz(R1)) * 32) + 8 * p);
    ldsw<u32x4>(lds, LDS_FB + (16 * g + li) * 144 + 16 * j, __builtin_bit_cast(u32x4, __builtin_shufflevector(a0, a1, 0, 1, 2, 3, 4, 5, 6, 7)));
  }
#pragma unroll
  for (int j = 0; j < 9; ++j) {
    const int ch = 64 * j + lane, row = ch / 9, c = ch - 9 * row;
    const u32x4 yy = ldsr<u32x4>(lds, LDS_FB + ch * 16), dd = *(const u32x4*)(dskip + 72 * hg + 8 * c);
    u32x4 o;
#pragma unroll
    for (int k = 0; k < 4; ++k) {
      const unsigned sk = cvtpk(bflo(uu[j][k]) * bflo(dd[k]), bfhi(uu[j][k]) * bfhi(dd[k]));
      o[k] = cvtpk(bflo(yy[k]) + bflo(sk), bfhi(yy[k]) + bfhi(sk));
    }
    *(u32x4*)(out + (size_t)(64 * rb + row) * (2 * DM) + DM + 72 * hg + 8 * c) = o;
  }
}

DI void tail_part(const f32x16& oa, const f32x16& ob, float ma, float mb, int w, int rb, int hg, const u16* u, const u16* dskip, char* ws, u16* out, char* lds) {
  int lane;
  asm volatile("v_mbcnt_lo_u32_b32 %0, -1, 0\n\tv_mbcnt_hi_u32_b32 %0, -1, %0" : "=v"(lane));
  const int r = lane & 31, h = lane >> 5, pt = w & 1, hp = (w >> 1) & 1, kh = w >> 2;

  {
    const f32x16& give = kh ? oa : ob;
    const float gm = kh ? ma : mb;
    char* own = lds + LDS_CMB + w * 3328;
#pragma unroll
    for (int g = 0; g < 3; ++g) ldsw<f32x4>(own, g * 1024 + lane * 16, f32x4{give[4 * g], give[4 * g + 1], give[4 * g + 2], give[4 * g + 3]});
    ldsw<float>(own, 3072 + lane * 4, gm);
    BAR();
    const char* oth = lds + LDS_CMB + (w ^ 4) * 3328;
    f32x16 keep = kh ? ob : oa;
    const float m1 = kh ? mb : ma, m2 = ldsr<float>(oth, 3072 + lane * 4);
    const float mm = fmaxf(m1, m2), f1 = EX2(m1 - mm), f2 = EX2(m2 - mm);
    float o[12];
#pragma unroll
    for (int g = 0; g < 3; ++g) {
      const f32x4 t = ldsr<f32x4>(oth, g * 1024 + lane * 16);
#pragma unroll
      for (int e = 0; e < 4; ++e) o[4 * g + e] = keep[4 * g + e] * f1 + t[e] * f2;
    }
    const float inv = 1.0f / half_lo(o[10]);
    const unsigned ot = LDS_OT + (32 * pt + r) * 144 + (2 * hp + kh) * 36;
    ldsw<unsigned>(lds, ot + 8 * h, cvtpk(o[0] * inv, o[1] * inv));
    ldsw<unsigned>(lds, ot + 8 * h + 4, cvtpk(o[2] * inv, o[3] * inv));
    ldsw<unsigned>(lds, ot + 16 + 8 * h, cvtpk(o[4] * inv, o[5] * inv));
    ldsw<unsigned>(lds, ot + 16 + 8 * h + 4, cvtpk(o[6] * inv, o[7] * inv));
    if (!h) ldsw<unsigned>(lds, ot + 32, cvtpk(o[8] * inv, o[9] * inv));
  }
  BAR();
  const __amdgpu_buffer_rsrc_t rsO = __builtin_amdgcn_make_buffer_rsrc((void*)(ws + WS_O + (size_t)64 * rb * (DM * 2)), 0, 64 * DM * 2, kRsrc3);
  {
    const int c0 = 64 * w + lane, row = c0 / 9, c = c0 - 9 * row;
    __builtin_amdgcn_raw_buffer_store_b128(ldsr<u32x4>(lds, LDS_OT + c0 * 16), rsO, row * (DM * 2) + hg * 144 + c * 16, 0, 16);
    if (w == 0) {
      const int c1 = 512 + lane, row1 = c1 / 9, cc = c1 - 9 * row1;
      __builtin_amdgcn_raw_buffer_store_b128(ldsr<u32x4>(lds, LDS_OT + c1 * 16), rsO, row1 * (DM * 2) + hg * 144 + cc * 16, 0, 16);
    }
  }
  asm volatile("s_waitcnt vmcnt(0)" ::: "memory");
  BAR();
  STAMP(1, 9);
  gu32* cnt = (gu32*)(ws + WS_CNT) + 32 * rb;
  if (w == 0) {
    if (lane == 0) __hip_atomic_fetch_add(cnt, 1u, __ATOMIC_RELAXED, __HIP_MEMORY_SCOPE_AGENT);
    for (unsigned spins = 0; spins < (1u << 20); ++spins) {
      if (rfl(__hip_atomic_load(cnt, __ATOMIC_RELAXED, __HIP_MEMORY_SCOPE_AGENT)) == 8u) break;
      __builtin_amdgcn_s_sleep(1);
    }
    __builtin_amdgcn_fence(__ATOMIC_ACQUIRE, "wavefront");
  }
  BAR();
  STAMP(1, 10);
  if (w == 4) skip_tile(rb, hg, u, dskip, ws, out, lds, lane);
  if (w >= 4) return;

  const int li = lane & 15, g = lane >> 4, q = li >> 2, p = lane & 3;
  u32x4 xf[12];
#define XLD(ks) __builtin_amdgcn_raw_buffer_load_b128(rsO, (16 * w + li) * (DM * 2) + 64 * (ks) + 16 * g, 0, 16)
#pragma unroll
  for (int ks = 0; ks < 12; ++ks) xf[ks] = XLD(ks);
  STAMP(1, 11);
  f32x4 acc[5] = {};
  const unsigned wb = LDS_CV + (8 * g + q) * 144 + 8 * p;
  s16x4 wa[2][10];
#define WRD(buf, ks) do { _Pragma("unroll") for (int nt = 0; nt < 5; ++nt) { \
    wa[buf][2 * nt] = ldstr(lds, wb + 32 * (ks) * 144 + 32 * nt); wa[buf][2 * nt + 1] = ldstr(lds, wb + (32 * (ks) + 4) * 144 + 32 * nt); } } while (0)
  WRD(0, 0);
#pragma unroll
  for (int ks = 0; ks < 18; ++ks) {
    if (ks + 1 < 18) WRD((ks + 1) & 1, ks + 1);
    SBAR();
#pragma unroll
    for (int nt = 0; nt < 5; ++nt) {
      const auto a = __builtin_shufflevector(wa[ks & 1][2 * nt], wa[ks & 1][2 * nt + 1], 0, 1, 2, 3, 4, 5, 6, 7);
      acc[nt] = __builtin_amdgcn_mfma_f32_16x16x32_bf16(__builtin_bit_cast(bf16x8, a), __builtin_bit_cast(bf16x8, xf[ks % 12]), acc[nt], 0, 0, 0);
    }
    SBAR();
    if (ks < 6) xf[ks] = XLD(ks + 12);
    SBAR();
  }
#undef WRD
#undef XLD
  STAMP(1, 12);
  u16* orow = out + (size_t)(64 * rb + 16 * w + li) * (2 * DM) + 72 * hg + 4 * g;
#pragma unroll
  for (int nt = 0; nt < 5; ++nt)
    if (nt < 4 || lane < 32) *(u32x2*)(orow + 16 * nt) = u32x2{cvtpk(acc[nt][0], acc[nt][1]), cvtpk(acc[nt][2], acc[nt][3])};
  STAMP(1, 15);
}

__global__ void __launch_bounds__(832)
k2_main(const u16* wo, const u16* u, const u16* dskip, char* ws, u16* out) {
  extern __shared__ __attribute__((aligned(16))) char lds[];
  const unsigned lds0 = (unsigned)(uintptr_t)lds;
  const int b = BID, lane = TID & 63, w = rfl(TID >> 6);
  const int rb = (b & 7) + 8 * (b >> 6), hg = (b >> 3) & 7;
  STAMP(1, 0);
  if (TID < 32) ((LDSP unsigned*)((LDSP char*)lds + LDS_FLAG))[TID] = 0u;
  BAR();
  if (w >= 8) {
    const int sw = w - 8, i = b >> 3, xc = b & 7;
    __builtin_amdgcn_s_setprio(3);
    const char* kvg = ws + WS_KV;
    if (sw < 4) {
#pragma unroll
      for (int j = 0; j < 5; ++j) {
        const int ins = sw + 4 * j;
        if (ins < 19) glds16s(kvg + 1024 * ins, 16 * lane, lds0 + 1024 * ins);
      }
      WAITV(0);
      ONE_LANE(flag_add(lds, F_SEG0));
    }
    if (sw == 4) {
      auto issue = [&](int s) {
#pragma unroll
        for (int j = 0; j < 19; ++j) glds16s(kvg + s * KV_SEG + 1024 * j, 16 * lane, lds0 + (s % 3) * KV_SEG + 1024 * j);
      };
      issue(1); issue(2);
      WAITV(19);
      flag_set(lds, F_FULL, 2);
      WAITV(0);
      flag_set(lds, F_FULL, 3);
#pragma unroll 1
      for (int s = 3; s < 8; ++s) {
        done_wait<4>(lds, s - 2, lane);
        issue(s);
        WAITV(0);
        flag_set(lds, F_FULL, s + 1);
      }
      STAMP(1, 15);
      return;
    }
    const int d0 = 72 * xc + 2 * i, dx = 72 * xc + 64 + (i >> 2), third = (i & 3) < 2;
    conv_fill_share(d0, dx, third, sw, ws, lds0, lane);
    WAITV(0);
    ONE_LANE(flag_add(lds, F_CFILL));
    STAMP(1, 6);
    {
      const int r = lane & 31, h = lane >> 5;
      const unsigned kb = LDS_CV + (3 - (r & 3)) * CV_K + 2 * (8 * h - 8 * (r >> 2));
      const unsigned ub = LDS_CV + CV_U + (r & 1) * CV_UP + 144 * (r >> 1) + 16 * h;
      const unsigned A1 = kb + 2 * 1080, A2 = kb + 2 * 56, B1 = ub, B3 = ub + 2304;
      char* scr = lds + LDS_SCR + sw * 2048;
      f32x16 c0 = {}, c1 = {};
      lds_wait<4>(lds, F_CFILL, 4);
      if (sw == 2) {
        conv_steps<true, false>(lds, A1, B1, A1 + CV_CH, B1 + CV_CH, c0, c1);
        conv_out(c0, d0, 0, ws, scr, lane);
        conv_out(c1, d0 + 1, 0, ws, scr, lane);
      } else if (sw < 2 || (i & 3) == 0) {
        const unsigned so = (sw < 2 ? sw : 2) * CV_CH;
        conv_steps<true, true>(lds, A2 + so, B1 + so, A1 + so, B3 + so, c0, c1);
#pragma unroll
        for (int k = 0; k < 16; ++k) c0[k] += c1[k];
        conv_out(c0, sw < 2 ? d0 + sw : dx, 1, ws, scr, lane);
      } else if (third) {
        conv_steps<false, false>(lds, A1 + 2 * CV_CH, B1 + 2 * CV_CH, 0, 0, c0, c1);
        conv_out(c0, dx, 0, ws, scr, lane);
      }
      STAMP(1, 7);
    }
    asm volatile("s_waitcnt vmcnt(0) lgkmcnt(0)" ::: "memory");
    unsigned arrived = 0;
    if (lane == 0) arrived = __hip_atomic_fetch_add((LDSP unsigned*)((LDSP char*)lds + LDS_FLAG) + F_CDONE, 1u, __ATOMIC_RELAXED, __HIP_MEMORY_SCOPE_WORKGROUP);
    if (rfl(arrived) == 3 && lane == 0) __hip_atomic_fetch_add((gu32*)(ws + WS_CNT) + 32 * (32 + xc), 1u, __ATOMIC_RELAXED, __HIP_MEMORY_SCOPE_AGENT);
    lds_wait<4>(lds, F_CDONE, 4);
    {
      unsigned vo[3];
#pragma unroll
      for (int k = 0; k < 3; ++k) { const int ch = 64 * (sw + 4 * k) + lane, row = ch / 9, c = ch - 9 * row; vo[k] = row * (DM * 2) + c * 16; }
      const char* wb = (const char*)(wo + 72 * hg);
#pragma unroll 1
      for (int a = 0; a < 9; ++a) {
#pragma unroll
        for (int k = 0; k < 3; ++k)
          if (k < 2 || sw == 0) glds16s(wb + (size_t)a * 64 * (DM * 2), vo[k], lds0 + LDS_CV + 1024 * (9 * a + sw + 4 * k));
      }
    }
    STAMP(1, 8);
    WAITV(0);
    STAMP(1, 15);
    return;
  }
  const int r = lane & 31, h = lane >> 5;
  const int pt = w & 1, hp = (w >> 1) & 1, kh = w >> 2;
  const int pos = 64 * rb + 32 * pt + r, head = 4 * hg + 2 * hp;

  const char* qrow = ws + WS_Q + (size_t)pos * (DM * 2) + head * (DH * 2);
  const u32x4 qam = *(const u32x4*)(qrow + 16 * h), qbm = *(const u32x4*)(qrow + 36 + 16 * h);
  const unsigned qa16 = *(const unsigned*)(qrow + 32), qb16 = *(const unsigned*)(qrow + 36 + 32);
  const char* k0p = ws + WS_KV + KV_HDR + kh * 1152;
  u32x4 kA = *(const u32x4*)(k0p + h * 512 + r * 16);
  u32x2 kAx = {*(const unsigned*)(k0p + 1024 + r * 4), 0x3f80u};
  const float kmax2 = wave_max(((const float*)(ws + WS_KMAX))[lane]);
  auto sq = [](unsigned u) { return bflo(u) * bflo(u) + bfhi(u) * bfhi(u); };
  const float na = half_sum(sq(qam[0]) + sq(qam[1]) + sq(qam[2]) + sq(qam[3]) + (h ? 0.f : sq(qa16)));
  const float nb = half_sum(sq(qbm[0]) + sq(qbm[1]) + sq(qbm[2]) + sq(qbm[3]) + (h ? 0.f : sq(qb16)));
  const float ba = rbf(__builtin_amdgcn_sqrtf(na * kmax2) * 1.01f + 1e-6f), bb = rbf(__builtin_amdgcn_sqrtf(nb * kmax2) * 1.01f + 1e-6f);
  const bool fast = __builtin_amdgcn_ballot_w64(!(ba <= FAST_BOUND && bb <= FAST_BOUND)) == 0;

  KVAddr ad;
  {
    const int vr = r <= 18 ? r : 19;
    const unsigned tb = kh * KV_HALF;
    ad.k = tb + h * 512 + r * 16; ad.kx = tb + 1024 + r * 4; ad.v = tb + 1152 + h * KV_VP + vr * 16;
  }
  f32x16 oa = {}, ob = {};
  float ma, mb;
  if (fast) {
    ma = 0.f; mb = 0.f;
    u32x2 qax = {h ? 0u : qa16, h ? 0u : (cvtpk(-ba, 0.f))}, qbx = {h ? 0u : qb16, h ? 0u : (cvtpk(-bb, 0.f))};
    u32x4 kB, vA0, vA1, vB0, vB1;
    u32x2 kBx = {0u, 0x3f80u};
    f32x16 zero16 = {}, sa, sb;
    u32x8 pa, pb = {};
    sa = MFMA16(kA, qam, zero16); sa = MFMA8(kAx, qax, sa);
    STAMP(1, 1);
    lds_wait<1>(lds, F_SEG0, 4);
    STAMP(1, 2);
    LDV(vB0, vB1, 0);
    unsigned off = 0;
#pragma unroll 1
    for (int s = 0; s < 8; ++s) {
      STEP(kA, kAx, kB, kBx, vB0, vB1, vA0, vA1, off);
      STEP(kB, kBx, kA, kAx, vA0, vA1, vB0, vB1, off + KV_TILE);
      STEP(kA, kAx, kB, kBx, vB0, vB1, vA0, vA1, off + 2 * KV_TILE);
      const unsigned full = flag_get(lds, F_FULL);
      STEP(kB, kBx, kA, kAx, vA0, vA1, vB0, vB1, off + 3 * KV_TILE);
      flag_set(lds, F_DONE + w, s + 1);
      off = off == 2 * KV_SEG ? 0 : off + KV_SEG;
      if (s < 7 && rfl(full) < s + 2) lds_wait<1>(lds, F_FULL, s + 2);
    }
    ob = MFMA16(vB0, lo4(pb), ob);
    ob = MFMA16(vB1, hi4(pb), ob);
  } else {
    ma = -INFINITY; mb = -INFINITY;
    const u32x2 qax = {h ? 0u : qa16, 0u}, qbx = {h ? 0u : qb16, 0u};
    kAx[1] = 0u;
#pragma unroll 1
    for (int t = 0; t < 32; ++t) {
      if (t == 0) lds_wait<1>(lds, F_SEG0, 4); else if ((t & 3) == 0) lds_wait<1>(lds, F_FULL, (t >> 2) + 1);
      const unsigned off = ((t >> 2) % 3) * KV_SEG + (t & 3) * KV_TILE;
      u32x4 kn, v0, v1; u32x2 knx = {0u, 0u};
      LDK(kn, knx, off); LDV(v0, v1, off);
      if ((t & 3) == 3) flag_set(lds, F_DONE + w, (t >> 2) + 1);
      slow_tile(kA, kAx, v0, v1, qam, qax, oa, ma);
      slow_tile(kA, kAx, v0, v1, qbm, qbx, ob, mb);
      kA = kn; kAx = knx;
    }
  }
  STAMP(1, 5);
  BAR();
  tail_part(oa, ob, ma, mb, w, rb, hg, u, dskip, ws, out, lds);
}

#if defined(__HIP_DEVICE_COMPILE__)
#pragma clang attribute pop
#endif
extern "C" void kernel_launch(void* const* d_in, const int* in_sizes, int n_in, void* d_out, int out_size, void* d_ws, size_t ws_size,
                              hipStream_t stream) {
  const u16* x = (const u16*)d_in[0];
  const u16* wq = (const u16*)d_in[1];
  const u16* wk = (const u16*)d_in[2];
  const u16* wv = (const u16*)d_in[3];
  const u16* wo = (const u16*)d_in[4];
  const u16* u = (const u16*)d_in[5];
  const u16* Are = (const u16*)d_in[6];
  const u16* Aim = (const u16*)d_in[7];
  const u16* Bre = (const u16*)d_in[8];
  const u16* Bim = (const u16*)d_in[9];
  const u16* Cre = (const u16*)d_in[10];
  const u16* Cim = (const u16*)d_in[11];
  const u16* logdt = (const u16*)d_in[12];
  const u16* dskip = (const u16*)d_in[13];
  char* ws = (char*)d_ws;
  (void)hipFuncSetAttribute((const void*)k1_prep, hipFuncAttributeMaxDynamicSharedMemorySize, K1_LDS);
  (void)hipFuncSetAttribute((const void*)k2_main, hipFuncAttributeMaxDynamicSharedMemorySize, K2_LDS);
  k1_prep<<<400, 512, K1_LDS, stream>>>(x, wq, wk, wv, u, Are, Aim, Bre, Bim, Cre, Cim, logdt, ws);
  k2_main<<<256, 832, K2_LDS, stream>>>(wo, u, dskip, ws, (u16*)d_out);
}
```

```cpp
#if defined(__HIP_DEVICE_COMPILE__)
#pragma clang attribute push(__attribute__((target("no-packed-fp32-ops"))), apply_to = function)
#endif
#include <hip/hip_runtime.h>
#include <stdint.h>

typedef __bf16 bf16x2 __attribute__((ext_vector_type(2)));
typedef __bf16 bf16x8 __attribute__((ext_vector_type(8)));
typedef short s16x4 __attribute__((ext_vector_type(4)));
typedef float f32x2 __attribute__((ext_vector_type(2)));
typedef float f32x4 __attribute__((ext_vector_type(4)));
typedef float f32x16 __attribute__((ext_vector_type(16)));
typedef unsigned u32x2 __attribute__((ext_vector_type(2)));
typedef unsigned u32x4 __attribute__((ext_vector_type(4)));
typedef unsigned short u16;
#define DI __device__ __forceinline__
#define BID ((int)__builtin_amdgcn_workgroup_id_x())
#define TID ((int)__builtin_amdgcn_workitem_id_x())
#define LDSP __attribute__((address_space(3)))
#define SBAR() __builtin_amdgcn_sched_barrier(0)
#define PIN(x) asm volatile("" : "+v"(x))
#define MFMA16(a, b, c) __builtin_amdgcn_mfma_f32_32x32x16_bf16(__builtin_bit_cast(bf16x8, a), __builtin_bit_cast(bf16x8, b), c, 0, 0, 0)
#define MFMA8(a, b, c) __builtin_amdgcn_mfma_f32_32x32x8bf16_1k(__builtin_bit_cast(s16x4, a), __builtin_bit_cast(s16x4, b), c, 0, 0, 0)

constexpr int SEQ = 2048, DM = 576, DH = 18, NST = 128;
constexpr int KV_VP = 320;
constexpr int KV_TILE = 1152 + 4 * KV_VP;
constexpr int KV_HALF = 4 * KV_TILE, KV_SEG = 2 * KV_HALF;
constexpr int KV_HDR = 8 * KV_SEG;
constexpr int KR_PAR = 4288, KR_CH = 2 * KR_PAR;
constexpr int UT_ROW = 2064;
constexpr size_t WS_Q = 0;
constexpr size_t WS_KV = WS_Q + (size_t)SEQ * DM * 2;
constexpr size_t WS_KMAX = WS_KV + 163840;
constexpr size_t WS_ZERO = WS_KMAX + 256;
constexpr size_t WS_CNT = WS_ZERO + 256;
constexpr size_t WS_O = WS_CNT + 8192;
constexpr size_t WS_KR = WS_O + (size_t)SEQ * DM * 2;
constexpr size_t WS_UT = WS_KR + (size_t)DM * KR_CH;
constexpr size_t WS_YT = WS_UT + (size_t)DM * UT_ROW * 2;

DI unsigned cvtpk(float a, float b) { f32x2 v = {a, b}; return __builtin_bit_cast(unsigned, __builtin_convertvector(v, bf16x2)); }
DI float bflo(unsigned u) { return __uint_as_float(u << 16); }
DI float bfhi(unsigned u) { return __uint_as_float(u & 0xffff0000u); }
DI float bf2f(u16 x) { return __uint_as_float((unsigned)x << 16); }
DI float rbf(float x) { return bflo(cvtpk(x, x)); }
DI int crow(int reg, int h) { return (reg & 3) + 8 * (reg >> 2) + 4 * h; }
DI void glds16(const void* g, unsigned lds_base) {
  unsigned sv;
  asm volatile("s_mov_b32 %0, m0\n\ts_mov_b32 m0, %2\n\ts_nop 0\n\tglobal_load_lds_dwordx4 %1, off\n\ts_mov_b32 m0, %0"
               : "=&s"(sv) : "v"(g), "s"(lds_base) : "memory");
}
DI void glds16s(const void* sbase, unsigned voff, unsigned lds_base) {
  unsigned sv;
  asm volatile("s_mov_b32 %0, m0\n\ts_mov_b32 m0, %3\n\ts_nop 0\n\tglobal_load_lds_dwordx4 %1, %2\n\ts_mov_b32 m0, %0"
               : "=&s"(sv) : "v"(voff), "s"(sbase), "s"(lds_base) : "memory");
}
#define WAITV(n) asm volatile("s_waitcnt vmcnt(" #n ")" ::: "memory")
#define WAITV_BAR(n) asm volatile("s_waitcnt vmcnt(" #n ") lgkmcnt(0)\n\ts_barrier" ::: "memory")
#define SB() asm volatile("s_barrier" ::: "memory")
#define BAR() asm volatile("s_waitcnt lgkmcnt(0)\n\ts_barrier" ::: "memory")
#define STAMP(kern, i)
DI void wait_until(unsigned long long t) { while (__builtin_amdgcn_s_memrealtime() < t) __builtin_amdgcn_s_sleep(2); }
constexpr size_t WS_T0 = (size_t)100 << 20;
DI unsigned rfl(unsigned x) { return __builtin_amdgcn_readfirstlane(x); }
template <typename T> DI T ldsr(const char* lds, unsigned off) { return *(const LDSP T*)((const LDSP char*)lds + off); }
template <typename T> DI void ldsw(char* lds, unsigned off, T v) { *(LDSP T*)((LDSP char*)lds + off) = v; }
DI s16x4 ldstr(const char* lds, unsigned off) {
  typedef short v4i16_t __attribute__((ext_vector_type(4)));
  return __builtin_bit_cast(s16x4, __builtin_amdgcn_ds_read_tr16_b64_v4i16((LDSP v4i16_t*)((LDSP char*)lds + off)));
}
DI float half_sum(float v) {
  const auto t = __builtin_amdgcn_permlane32_swap(__float_as_uint(v), __float_as_uint(v), false, false);
  return __uint_as_float(t[0]) + __uint_as_float(t[1]);
}
DI float half_max(float v) {
  const auto t = __builtin_amdgcn_permlane32_swap(__float_as_uint(v), __float_as_uint(v), false, false);
  return fmaxf(__uint_as_float(t[0]), __uint_as_float(t[1]));
}
DI float half_lo(float v) {
  const auto t = __builtin_amdgcn_permlane32_swap(__float_as_uint(v), __float_as_uint(v), false, false);
  return __uint_as_float(t[0]);
}
template <int CTRL> DI float dpp_max(float v) {
  return fmaxf(v, __uint_as_float(__builtin_amdgcn_update_dpp(__float_as_uint(v), __float_as_uint(v), CTRL, 0xf, 0xf, false)));
}
DI float wave_max(float v) {
  v = dpp_max<0xb1>(v);
  v = dpp_max<0x4e>(v);
  v = dpp_max<0x141>(v);
  v = dpp_max<0x140>(v);
  const unsigned u = __float_as_uint(v);
  const float a = __uint_as_float(__builtin_amdgcn_readlane(u, 0)), b = __uint_as_float(__builtin_amdgcn_readlane(u, 16));
  const float c = __uint_as_float(__builtin_amdgcn_readlane(u, 32)), d = __uint_as_float(__builtin_amdgcn_readlane(u, 48));
  return fmaxf(fmaxf(a, b), fmaxf(c, d));
}

constexpr int QS = 18432;
DI int wswz(int row) { return ((row >> 1) & 1) | (((row >> 3) & 1) << 1); }
template <int NP, int S> DI void q_issue(const char* wb, const char* xb, int xrows, const unsigned (&vw)[5], const unsigned (&vx)[5], int lw, unsigned lds0) {
#pragma unroll
  for (int j = 0; j < NP; ++j) {
    const int p = lw + 4 * j;
    if constexpr (S < 4) glds16s(wb + (size_t)144 * S * (DM * 2), vw[j], lds0 + (S & 3) * QS + 1024 * p);
    else glds16s(xb + (size_t)(16 * (S - 4) < xrows ? 16 * (S - 4) : xrows) * (DM * 2), vx[j], lds0 + (S & 3) * QS + 1024 * p);
  }
}
template <int NP, int S> DI void q_load_steps(const char* wb, const char* xb, int xrows, const unsigned (&vw)[5], const unsigned (&vx)[5], int lw, unsigned lds0) {
  constexpr int inflight = (S + 2 > 9 ? 9 : S + 2) - S;
  asm volatile("s_waitcnt vmcnt(%0)\n\ts_barrier" ::"i"(NP * inflight) : "memory");
  if constexpr (S + 3 < 10) q_issue<NP, S + 3>(wb, xb, xrows, vw, vx, lw, lds0);
  if constexpr (S + 1 < 10) q_load_steps<NP, S + 1>(wb, xb, xrows, vw, vx, lw, lds0);
}
template <int NP> DI void q_loader(const u16* x, int p0, const u16* W, int lw, int lane, unsigned lds0) {
  unsigned vw[5], vx[5];
#pragma unroll
  for (int j = 0; j < NP; ++j) {
    const int p = lw + 4 * j;
    { const int row = 8 * p + (lane >> 3), c = lane & 7; vw[j] = row * (DM * 2) + 16 * (c ^ (wswz(row) << 1)); }
    { const int n = 64 * p + lane, row = n / 72, c = n - 72 * row; vx[j] = row * (DM * 2) + 16 * ((c & ~7) | ((c ^ (row >> 1)) & 7)); }
  }
  const int xrows = SEQ - 16 - p0;
  const char* wb = (const char*)W;
  const char* xb = (const char*)(x + (size_t)p0 * DM);
  q_issue<NP, 0>(wb, xb, xrows, vw, vx, lw, lds0);
  q_issue<NP, 1>(wb, xb, xrows, vw, vx, lw, lds0);
  q_issue<NP, 2>(wb, xb, xrows, vw, vx, lw, lds0);
  q_load_steps<NP, 0>(wb, xb, xrows, vw, vx, lw, lds0);
}
DI void q_block(const u16* x, int p0, const u16* W, u16* C, char* lds, int w, int lane) {
  if (w >= 4) {
    const unsigned lds0 = (unsigned)(uintptr_t)lds;
    if (w == 4) q_loader<5>(x, p0, W, 0, lane, lds0);
    else if (w == 5) q_loader<5>(x, p0, W, 1, lane, lds0);
    else if (w == 6) q_loader<4>(x, p0, W, 2, lane, lds0);
    else q_loader<4>(x, p0, W, 3, lane, lds0);
    return;
  }
  const int li = lane & 15, g = lane >> 4, q = li >> 2, p = lane & 3;
  u32x4 wf[18];
#pragma unroll
  for (int s = 0; s < 4; ++s) {
    BAR();
#pragma unroll
    for (int ks = 0; ks < 18; ++ks) {
#pragma unroll
      for (int t = 0; t < 2; ++t) {
        const int half = 2 * ks;
        const bool lo = half / 9 == s, hi = (half + 1) / 9 == s;
        if (lo || hi) {
          const int R = 8 * (g & 1) + 4 * t + q;
          const int grp = (g >> 1) ? (hi ? half + 1 - 9 * s : 0) : (lo ? half - 9 * s : 0);
          const s16x4 v = ldstr(lds, s * QS + (16 * grp + R) * 128 + ((w ^ wswz(R)) * 32) + 8 * p);
          const bool mine = (g >> 1) ? hi : lo;
          const u32x2 vv = __builtin_bit_cast(u32x2, v);
          if (lo && hi) { wf[ks][2 * t] = vv[0]; wf[ks][2 * t + 1] = vv[1]; }
          else if (lo) { wf[ks][2 * t] = vv[0]; wf[ks][2 * t + 1] = vv[1]; }
          else { wf[ks][2 * t] = mine ? vv[0] : wf[ks][2 * t]; wf[ks][2 * t + 1] = mine ? vv[1] : wf[ks][2 * t + 1]; }
        }
      }
    }
  }
  const unsigned xa = li * (DM * 2);
  const int xs = (li >> 1) & 7;
#pragma unroll 1
  for (int pt = 0; pt < 6; ++pt) {
    BAR();
    const unsigned sb = (pt & 3) * QS + xa;
    f32x4 acc = {};
    u32x4 bf[9];
#define XRD(ks) ldsr<u32x4>(lds, sb + 16 * (((4 * (ks) + g) & ~7) | (((4 * (ks) + g) ^ xs) & 7)))
#pragma unroll
    for (int ks = 0; ks < 9; ++ks) bf[ks] = XRD(ks);
    SBAR();
#pragma unroll
    for (int ks = 0; ks < 18; ++ks) {
      acc = __builtin_amdgcn_mfma_f32_16x16x32_bf16(__builtin_bit_cast(bf16x8, wf[ks]), __builtin_bit_cast(bf16x8, bf[ks % 9]), acc, 0, 0, 0);
      SBAR();
      if (ks < 9) bf[ks] = XRD(ks + 9);
      SBAR();
    }
#undef XRD
    const int row = p0 + 16 * pt + li;
    if (row < SEQ) *(u32x2*)(C + (size_t)row * DM + 16 * w + 4 * g) = u32x2{cvtpk(acc[0], acc[1]), cvtpk(acc[2], acc[3])};
  }
}

DI f32x2 cmul(f32x2 a, f32x2 b) { return f32x2{__builtin_fmaf(a[0], b[0], -(a[1] * b[1])), __builtin_fmaf(a[0], b[1], a[1] * b[0])}; }
DI f32x2 cexp_l(float zr, float zi, float l) {
  const float mag = __builtin_amdgcn_exp2f((zr * l) * 1.4426950408889634f);
  const float rev = __builtin_amdgcn_fractf((zi * l) * 0.15915494309189535f);
  return f32x2{mag * __builtin_amdgcn_cosf(rev), mag * __builtin_amdgcn_sinf(rev)};
}
constexpr int GEN_PITCH = 272, GEN_X = 0, GEN_Y = 32 * GEN_PITCH, GEN_WAVE = 14592;

DI void gen_wave(int d, const u16* Are, const u16* Aim, const u16* Bre, const u16* Bim, const u16* Cre, const u16* Cim,
                  const u16* logdt, char* ws, char* lds, int lane) {
  const int r = lane & 31, h = lane >> 5;
  u16 pr[2][6];
#pragma unroll
  for (int pass = 0; pass < 2; ++pass) {
    const int idx = d * NST + 64 * pass + lane;
    pr[pass][0] = Are[idx]; pr[pass][1] = Aim[idx]; pr[pass][2] = Bre[idx]; pr[pass][3] = Bim[idx]; pr[pass][4] = Cre[idx]; pr[pass][5] = Cim[idx];
  }
  const float dt = rbf(expf(bf2f(logdt[d])));
  f32x16 acc = {};
#pragma unroll
  for (int pass = 0; pass < 2; ++pass) {
    const float zr = dt * bf2f(pr[pass][0]), zi = dt * bf2f(pr[pass][1]);
    const float dBr = dt * bf2f(pr[pass][2]), dBi = dt * bf2f(pr[pass][3]);
    const float cr = bf2f(pr[pass][4]), ci = bf2f(pr[pass][5]);
    const f32x2 c = {cr * dBr - ci * dBi, cr * dBi + ci * dBr};
    const f32x2 w1 = cexp_l(zr, zi, 1.f), w256 = cexp_l(zr, zi, 256.f);
    const f32x2 v8 = cexp_l(zr, -zi, 8.f), v16 = cmul(v8, v8);
#define PUT(off, v) ldsw<unsigned>(lds, (off) + 4 * lane, cvtpk((v)[0], (v)[1]))
    PUT(GEN_X, (f32x2{1.f, 0.f})); PUT(GEN_X + GEN_PITCH, v8); PUT(GEN_X + 2 * GEN_PITCH, v16);
    f32x2 xe = v16, xo = v8;
#pragma unroll
    for (int m = 3; m < 32; m += 2) {
      xo = cmul(xo, v16); PUT(GEN_X + m * GEN_PITCH, xo);
      if (m < 31) { xe = cmul(xe, v16); PUT(GEN_X + (m + 1) * GEN_PITCH, xe); }
    }
#define PUTY(col, v) PUT(GEN_Y + (col) * GEN_PITCH, v)
    f32x2 y[8];
    y[0] = c;
#pragma unroll
    for (int i = 1; i < 8; ++i) y[i] = cmul(y[i - 1], w1);
#pragma unroll
    for (int i = 0; i < 8; ++i) PUTY(i, y[i]);
#pragma unroll
    for (int i = 0; i < 4; ++i) { const f32x2 t = cmul(y[2 * i], w256); PUTY(8 + i, t); }
    const f32x2 p2 = cmul(w256, w256), p3 = cmul(p2, w256), p4 = cmul(p2, p2);
    { f32x2 t = cmul(y[0], p2); PUTY(12, t); t = cmul(y[4], p2); PUTY(13, t); }
    { f32x2 t = cmul(y[0], p3); PUTY(14, t); t = cmul(y[4], p3); PUTY(15, t); }
    { const f32x2 t4 = cmul(y[0], p4); PUTY(16, t4);
      f32x2 t = cmul(t4, w256); PUTY(17, t); t = cmul(t4, p2); PUTY(18, t); t = cmul(t4, p3); PUTY(19, t); t = cmul(t4, p4); PUTY(20, t); }
#undef PUTY
#undef PUT
    asm volatile("" ::: "memory");
    const int yr = r <= 20 ? r : r - 16;
    u32x4 fa[8], fb[8];
#pragma unroll
    for (int ks = 0; ks < 8; ++ks) {
      fa[ks] = ldsr<u32x4>(lds, GEN_X + r * GEN_PITCH + 32 * ks + 16 * h);
      fb[ks] = ldsr<u32x4>(lds, GEN_Y + yr * GEN_PITCH + 32 * ks + 16 * h);
    }
#pragma unroll
    for (int ks = 0; ks < 8; ++ks) acc = MFMA16(fa[ks], fb[ks], acc);
    asm volatile("" ::: "memory");
  }
  {
    const int c = r;
    const int hi = c < 8 ? c : c < 12 ? 256 + 2 * (c - 8) : c < 16 ? 512 + 256 * ((c - 12) >> 1) + 4 * ((c - 12) & 1) : 1024 + 256 * (c - 16);
    if (c < 20) {
#pragma unroll
      for (int i = 0; i < 16; ++i) ldsw<u16>(lds, 2 * (8 * crow(i, h) + hi), (u16)cvtpk(acc[i], acc[i]));
    } else if (c == 20 && h == 0) {
      ldsw<u16>(lds, 2 * 2048, (u16)cvtpk(acc[0], acc[0]));
    }
  }
  asm volatile("s_waitcnt lgkmcnt(0)" ::: "memory");
  unsigned* E = (unsigned*)(ws + WS_KR + (size_t)d * KR_CH);
  unsigned* Od = E + KR_PAR / 4;
  auto rd = [&](int l) -> unsigned { return ldsr<u16>(lds, 2 * l); };
  auto pk = [](unsigned lo, unsigned hi) -> unsigned { return lo | (hi << 16); };
  const int L = lane;
  {
    const unsigned e0 = rd(4 * L), e1 = rd(4 * L + 1), e2 = rd(4 * L + 2), e3 = rd(4 * L + 3);
    const unsigned pv = L ? rd(4 * L - 1) : 0u;
    const int p0 = 2 * L;
    *(u32x2*)(E + 1028 - p0) = u32x2{pk(e3, e2), pk(e1, e0)};
    *(u32x2*)(Od + 1028 - p0) = u32x2{pk(e2, e1), pk(e0, pv)};
  }
  {
    const int j0 = 256 + 4 * L;
    const unsigned a = rd(j0), b = rd(j0 + 2), c = rd(j0 + 4), pv = L ? a : rd(255);
    const int p0 = j0 / 2;
    *(u32x2*)(E + 1028 - p0) = u32x2{pk(c, b), pk(a, a)};
    *(u32x2*)(Od + 1028 - p0) = u32x2{pk(b, a), pk(a, pv)};
  }
  {
    const int j0 = 512 + 8 * L;
    const unsigned a = rd(j0), b = rd(j0 + 4), c = rd(j0 + 8);
    const unsigned aa = pk(a, a), bb = pk(b, b), cc = pk(c, c);
    unsigned* e = E + 1026 - j0 / 2;
    unsigned* o = Od + 1026 - j0 / 2;
    *(u32x2*)(e) = u32x2{cc, bb};          *(u32x2*)(e + 2) = u32x2{pk(b, a), aa};
    *(u32x2*)(o) = u32x2{pk(c, b), bb};    *(u32x2*)(o + 2) = u32x2{aa, aa};
  }
  {
    const int j0 = 1024 + 16 * L;
    const unsigned a = rd(j0), b = rd(j0 + 8), c = rd(j0 + 16);
    const unsigned aa = pk(a, a), bb = pk(b, b), cc = pk(c, c);
    unsigned* e = E + 1022 - j0 / 2;
    unsigned* o = Od + 1022 - j0 / 2;
    *(u32x2*)(e) = u32x2{cc, cc};          *(u32x2*)(e + 2) = u32x2{bb, bb};
    *(u32x2*)(e + 4) = u32x2{bb, pk(b, a)}; *(u32x2*)(e + 6) = u32x2{aa, aa};
    *(u32x2*)(o) = u32x2{cc, pk(c, b)};    *(u32x2*)(o + 2) = u32x2{bb, bb};
    *(u32x2*)(o + 4) = u32x2{bb, aa};      *(u32x2*)(o + 6) = u32x2{aa, aa};
    if (L == 63) Od[5] = pk(0u, c);
  }
  if (L < 6) E[L] = 0u;
  if (L < 5) Od[L] = 0u;
  if (L < 42) { E[1030 + L] = 0u; Od[1030 + L] = 0u; }
}

DI void kv_block(int kb, const u16* x, const u16* wk, const u16* wv, char* ws, char* lds, int w, int lane) {
  const int r = lane & 31, h = lane >> 5, tl = w >> 2, kq = w & 3, tid = 64 * w + lane;
  const u16* xr = x + (size_t)(64 * kb + 32 * tl + r) * DM + 144 * kq + 8 * h;
  u32x4 xf[9];
#pragma unroll
  for (int ks = 0; ks < 9; ++ks) xf[ks] = *(const u32x4*)(xr + 16 * ks);
  typedef unsigned u32x3 __attribute__((ext_vector_type(3)));
  u32x3 wl[7];
#pragma unroll
  for (int j = 0; j < 7; ++j) {
    const int e = tid + 512 * j, m = e >= 1728, e1 = e < 3456 ? e - 1728 * m : 0;
    __builtin_memcpy(&wl[j], (const char*)(m ? wv : wk) + 12 * e1, 12);
  }
#pragma unroll
  for (int j = 0; j < 7; ++j) {
    const int e = tid + 512 * j, m = e >= 1728, e1 = e - 1728 * m, k = e1 / 3, c = e1 - 3 * k;
    if (e < 3456) {
      const unsigned o = k * 128 + ((64 * m) ^ (((k >> 1) & 1) << 6)) + 12 * c;
      ldsw<unsigned>(lds, o, wl[j][0]); ldsw<unsigned>(lds, o + 4, wl[j][1]); ldsw<unsigned>(lds, o + 8, wl[j][2]);
    }
  }
  BAR();
  const int blk = (lane >> 4) & 1, q = (lane & 15) >> 2, p = lane & 3;
  f32x16 ak = {}, av = {};
#pragma unroll
  for (int ks = 0; ks < 9; ++ks) {
    const int R0 = 144 * kq + 16 * ks + 8 * h + q, R1 = R0 + 4;
    const unsigned c0 = blk * 32 + p * 8, s0 = ((R0 >> 1) & 1) << 6, s1 = ((R1 >> 1) & 1) << 6;
    const auto fk = __builtin_shufflevector(ldstr(lds, R0 * 128 + (c0 ^ s0)), ldstr(lds, R1 * 128 + (c0 ^ s1)), 0, 1, 2, 3, 4, 5, 6, 7);
    const auto fv = __builtin_shufflevector(ldstr(lds, R0 * 128 + ((64 + c0) ^ s0)), ldstr(lds, R1 * 128 + ((64 + c0) ^ s1)), 0, 1, 2, 3, 4, 5, 6, 7);
    ak = MFMA16(fk, xf[ks], ak);
    av = MFMA16(xf[ks], fv, av);
  }
  BAR();
#pragma unroll
  for (int g = 0; g < 4; ++g) {
    ldsw<f32x4>(lds, (2 * w) * 4096 + g * 1024 + lane * 16, f32x4{ak[4 * g], ak[4 * g + 1], ak[4 * g + 2], ak[4 * g + 3]});
    ldsw<f32x4>(lds, (2 * w + 1) * 4096 + g * 1024 + lane * 16, f32x4{av[4 * g], av[4 * g + 1], av[4 * g + 2], av[4 * g + 3]});
  }
  BAR();
  if (kq >= 2) return;
  f32x16 acc;
#pragma unroll
  for (int g = 0; g < 4; ++g) {
    f32x4 t = ldsr<f32x4>(lds, (8 * tl + kq) * 4096 + g * 1024 + lane * 16);
#pragma unroll
    for (int v = 1; v < 4; ++v) t += ldsr<f32x4>(lds, (8 * tl + 2 * v + kq) * 4096 + g * 1024 + lane * 16);
#pragma unroll
    for (int e = 0; e < 4; ++e) acc[4 * g + e] = t[e];
  }
  const int kt = 2 * kb + tl, kh = kt >> 5, t = kt & 31;
  char* kv = ws + WS_KV;
  if (kq == 0) {
    char* img = t ? kv + ((t - 1) >> 2) * KV_SEG + kh * KV_HALF + ((t - 1) & 3) * KV_TILE : kv + KV_HDR + kh * 1152;
    const float sc = 0.23570226039551584f * 1.4426950408889634f;
    const int e = (r & 0x13) | ((r & 4) << 1) | ((r & 8) >> 1);
    const unsigned d0 = cvtpk(acc[0] * sc, acc[1] * sc), d1 = cvtpk(acc[2] * sc, acc[3] * sc);
    const unsigned d2 = cvtpk(acc[4] * sc, acc[5] * sc), d3 = cvtpk(acc[6] * sc, acc[7] * sc);
    const unsigned d4 = h ? 0u : cvtpk(acc[8] * sc, acc[9] * sc);
    *(u32x2*)(img + e * 16 + 8 * h) = u32x2{d0, d1};
    *(u32x2*)(img + 512 + e * 16 + 8 * h) = u32x2{d2, d3};
    if (!h) *(unsigned*)(img + 1024 + e * 4) = d4;
    float n2 = bflo(d0) * bflo(d0) + bfhi(d0) * bfhi(d0) + bflo(d1) * bflo(d1) + bfhi(d1) * bfhi(d1) + bflo(d2) * bflo(d2) + bfhi(d2) * bfhi(d2) +
               bflo(d3) * bflo(d3) + bfhi(d3) * bfhi(d3) + bflo(d4) * bflo(d4) + bfhi(d4) * bfhi(d4);
    n2 = wave_max(half_sum(n2));
    if (lane == 0) ((float*)(ws + WS_KMAX))[kt] = n2;
  } else if (r <= DH + 1) {
    char* img = kv + (t >> 2) * KV_SEG + kh * KV_HALF + (t & 3) * KV_TILE + 1152;
#pragma unroll
    for (int g = 0; g < 4; ++g) {
      u32x2 v = {cvtpk(acc[4 * g], acc[4 * g + 1]), cvtpk(acc[4 * g + 2], acc[4 * g + 3])};
      if (r == DH) v = u32x2{0x3f803f80u, 0x3f803f80u};
      if (r == DH + 1) v = u32x2{0u, 0u};
      *(u32x2*)(img + g * KV_VP + r * 16 + 8 * h) = v;
    }
  }
}

DI void ut_wave(int tile, const u16* u, char* ws, char* lds, int lane) {
  const unsigned lds0 = (unsigned)(uintptr_t)lds;
  const int tb = tile / 9, cb = tile - 9 * tb;
#pragma unroll
  for (int i = 0; i < 16; ++i) {
    const int row = 8 * i + (lane >> 3), c = lane & 7;
    glds16(u + (size_t)(128 * tb + row) * DM + 64 * cb + 8 * (c ^ (wswz(row) << 1)), lds0 + i * 1024);
  }
  u16* uT = (u16*)(ws + WS_UT);
  if (tb == 0 || tb == 15) *(u32x4*)(uT + (size_t)(64 * cb + lane) * UT_ROW + (tb ? 8 + SEQ : 0)) = u32x4{0, 0, 0, 0};
  WAITV(0);
  const int g = lane >> 4, li = lane & 15, q = li >> 2, p = li & 3;
#pragma unroll
  for (int db = 0; db < 4; ++db) {
#pragma unroll
    for (int e = 0; e < 4; ++e) {
      const int oct = 4 * e + g, R0 = 8 * oct + q, R1 = R0 + 4;
      const s16x4 a0 = ldstr(lds, R0 * 128 + ((db ^ wswz(R0)) * 32) + 8 * p), a1 = ldstr(lds, R1 * 128 + ((db ^ wswz(R1)) * 32) + 8 * p);
      const auto v = __builtin_shufflevector(a0, a1, 0, 1, 2, 3, 4, 5, 6, 7);
      *(u32x4*)(uT + (size_t)(64 * cb + 16 * db + li) * UT_ROW + 8 + 128 * tb + 8 * oct) = __builtin_bit_cast(u32x4, v);
    }
  }
}

constexpr int K1_LDS = 77824;
__global__ void __launch_bounds__(512, 4)
k1_prep(const u16* x, const u16* wq, const u16* wk, const u16* wv, const u16* u, const u16* Are, const u16* Aim, const u16* Bre,
        const u16* Bim, const u16* Cre, const u16* Cim, const u16* logdt, char* ws) {
  extern __shared__ __attribute__((aligned(16))) char lds[];
  const int b = BID, lane = TID & 63, w = rfl(TID >> 6);
  const unsigned long long tstart = __builtin_amdgcn_s_memrealtime();
  if (b == 0 && TID == 0) *(unsigned long long*)(ws + WS_T0) = tstart;
  if (b < 200) {
    const int tile = (b & 7) * 25 + (b >> 3), rb = tile / 9, cb = tile - 9 * rb;
    if (tile < 198) q_block(x, 96 * rb, wq + 64 * cb, (u16*)(ws + WS_Q) + 64 * cb, lds, w, lane);
  } else if (b < 232) {
    kv_block(4 * (b & 7) + ((b - 200) >> 3), x, wk, wv, ws, lds, w, lane);
  } else if (b >= 256) {
    const int j = b - 256;
    if (w < 4) gen_wave(4 * j + w, Are, Aim, Bre, Bim, Cre, Cim, logdt, ws, lds + w * GEN_WAVE, lane);
    else if (w == 4) ut_wave(j, u, ws, lds + 4 * GEN_WAVE, lane);
  } else if (b == 255 && w == 0) {
    ((unsigned*)(ws + WS_ZERO))[lane] = 0u;
    if (lane < 40) ((unsigned*)(ws + WS_CNT))[32 * lane] = 0u;
  }
  STAMP(0, 15);
}

constexpr int LDS_CV = 3 * KV_SEG;
constexpr int CV_CH = 31744, CV_K = 4416, CV_U = 17664, CV_UP = 7040;
constexpr int LDS_CMB = 0;
constexpr int LDS_OT = 8 * 3328;
constexpr int LDS_FA = LDS_CV + 82944, LDS_FB = LDS_FA + 9216;
constexpr int LDS_SCR = LDS_CV + 3 * CV_CH;
constexpr int LDS_FLAG = LDS_SCR + 4 * 2048;
constexpr int K2_LDS = LDS_FLAG + 128;
constexpr float FAST_BOUND = 30.f;
#define CONV_PRIO 0
constexpr int kRsrc3 = 0x00020000;
typedef __attribute__((address_space(1))) unsigned gu32;

struct KVAddr { unsigned k, kx, v; };
#define LDK(dst, dstx, t_off) do { dst = ldsr<u32x4>(lds, ad.k + (t_off)); dstx[0] = ldsr<unsigned>(lds, ad.kx + (t_off)); } while (0)
#define LDV(d0, d1, t_off) do { d0 = ldsr<u32x4>(lds, ad.v + (t_off)); d1 = ldsr<u32x4>(lds, ad.v + 2 * KV_VP + (t_off)); } while (0)
#define EX2(x) __builtin_amdgcn_exp2f(x)
#define GRP(MF, S, P, i) do { MF; SBAR(); \
    { const float e0 = EX2(S[4 * (i)]), e1 = EX2(S[4 * (i) + 1]), e2 = EX2(S[4 * (i) + 2]), e3 = EX2(S[4 * (i) + 3]); \
      unsigned t0_ = cvtpk(e0, e1), t1_ = cvtpk(e2, e3); PIN(t0_); PIN(t1_); P[2 * (i)] = t0_; P[2 * (i) + 1] = t1_; } \
    SBAR(); } while (0)
typedef unsigned u32x8 __attribute__((ext_vector_type(8)));
DI u32x4 lo4(const u32x8& p) { return __builtin_shufflevector(p, p, 0, 1, 2, 3); }
DI u32x4 hi4(const u32x8& p) { return __builtin_shufflevector(p, p, 4, 5, 6, 7); }

#define STEP(Kc, Kcx, Kn, Knx, Vp0, Vp1, Vc0, Vc1, off) do { \
    LDK(Kn, Knx, (off)); LDV(Vc0, Vc1, (off)); SBAR(); \
    GRP(sb = MFMA16(Kc, qbm, zero16), sa, pa, 0); \
    GRP(ob = MFMA16(Vp0, lo4(pb), ob), sa, pa, 1); \
    GRP(sb = MFMA8(Kcx, qbx, sb), sa, pa, 2); \
    GRP(ob = MFMA16(Vp1, hi4(pb), ob), sa, pa, 3); \
    GRP(sa = MFMA16(Kn, qam, zero16), sb, pb, 0); \
    GRP(oa = MFMA16(Vc0, lo4(pa), oa), sb, pb, 1); \
    GRP(sa = MFMA8(Knx, qax, sa), sb, pb, 2); \
    GRP(oa = MFMA16(Vc1, hi4(pa), oa), sb, pb, 3); \
  } while (0)

DI void slow_tile(const u32x4& kf, const u32x2& kx, const u32x4& v0, const u32x4& v1, const u32x4& qm, const u32x2& qx, f32x16& o, float& m) {
  f32x16 s = {};
  s = MFMA16(kf, qm, s);
  s = MFMA8(kx, qx, s);
  float mx = s[0];
#pragma unroll
  for (int i = 1; i < 16; ++i) mx = fmaxf(mx, s[i]);
  mx = half_max(mx);
  const float mn = fmaxf(m, mx);
  const float alpha = EX2(m - mn);
  m = mn;
#pragma unroll
  for (int i = 0; i < 16; ++i) o[i] *= alpha;
  u32x8 p;
#pragma unroll
  for (int i = 0; i < 8; ++i) p[i] = cvtpk(EX2(s[2 * i] - mn), EX2(s[2 * i + 1] - mn));
  o = MFMA16(v0, lo4(p), o);
  o = MFMA16(v1, hi4(p), o);
}

constexpr int F_FULL = 0, F_SEG0 = 1, F_CFILL = 2, F_CDONE = 3, F_DONE = 8;
DI void flag_add(char* lds, int word) { __hip_atomic_fetch_add((LDSP unsigned*)((LDSP char*)lds + LDS_FLAG) + word, 1u, __ATOMIC_RELAXED, __HIP_MEMORY_SCOPE_WORKGROUP); }
DI void flag_set(char* lds, int word, unsigned v) {
  asm volatile("" ::: "memory");
  __hip_atomic_store((LDSP unsigned*)((LDSP char*)lds + LDS_FLAG) + word, v, __ATOMIC_RELAXED, __HIP_MEMORY_SCOPE_WORKGROUP);
  asm volatile("" ::: "memory");
}
DI unsigned flag_get(char* lds, int word) { return __hip_atomic_load((LDSP unsigned*)((LDSP char*)lds + LDS_FLAG) + word, __ATOMIC_RELAXED, __HIP_MEMORY_SCOPE_WORKGROUP); }
template <int SLEEP> DI void lds_wait(char* lds, int word, unsigned need) {
#pragma unroll 1
  for (unsigned spins = 0; spins < (1u << 20); ++spins) {
    if (rfl(flag_get(lds, word)) >= need) break;
    __builtin_amdgcn_s_sleep(SLEEP);
  }
  asm volatile("" ::: "memory");
}
template <int SLEEP> DI void done_wait(char* lds, unsigned need, int lane) {
#pragma unroll 1
  for (unsigned spins = 0; spins < (1u << 20); ++spins) {
    if (__builtin_amdgcn_ballot_w64(flag_get(lds, F_DONE + (lane & 7)) < need) == 0) break;
    __builtin_amdgcn_s_sleep(SLEEP);
  }
  asm volatile("" ::: "memory");
}
#define ONE_LANE(stmt) do { asm volatile("" ::: "memory"); if (lane == 0) { stmt; } asm volatile("" ::: "memory"); } while (0)

template <int SW> DI void conv_fill(int d, const char* ws, unsigned cl0, int lane) {
  const char* kr = ws + WS_KR + (size_t)d * KR_CH;
  const unsigned ur = (unsigned)(WS_UT - WS_ZERO) + d * (UT_ROW * 2);
#pragma unroll
  for (int item = SW; item < 34; item += 4) {
    if (item < 20) {
      const int m = item / 5, part = item - 5 * m;
      if (part < 4 || lane < 11) glds16s(kr + (m & 1) * KR_PAR + (m >> 1) * 4 + part * 1024, 16 * lane, cl0 + m * CV_K + part * 1024);
    } else {
      const int bb = (item - 20) / 7, part = item - 20 - 7 * bb, sg = part * 64 + lane, qc = sg - sg / 9, s0 = 8 * qc - 976 + 4 * bb;
      const unsigned voff = (s0 < -8 || sg % 9 == 8) ? 0u : ur + 2 * (8 + s0);
      if (part < 6 || lane <= 40) glds16s(ws + WS_ZERO, voff, cl0 + CV_U + bb * CV_UP + part * 1024);
    }
  }
}
template <int SW> DI void conv_fill3(int d0, int dx, bool third, const char* ws, unsigned lds0, int lane) {
  conv_fill<SW>(d0, ws, lds0 + LDS_CV, lane);
  conv_fill<(SW + 2) & 3>(d0 + 1, ws, lds0 + LDS_CV + CV_CH, lane);
  if (third) conv_fill<SW>(dx, ws, lds0 + LDS_CV + 2 * CV_CH, lane);
}
DI void conv_fill_share(int d0, int dx, bool third, int sw, const char* ws, unsigned lds0, int lane) {
  if (sw == 0) conv_fill3<0>(d0, dx, third, ws, lds0, lane);
  else if (sw == 1) conv_fill3<1>(d0, dx, third, ws, lds0, lane);
  else if (sw == 2) conv_fill3<2>(d0, dx, third, ws, lds0, lane);
  else conv_fill3<3>(d0, dx, third, ws, lds0, lane);
}
template <bool TWO, bool SKIP0> DI void conv_steps(const char* lds, unsigned pa0, unsigned pb0, unsigned pa1, unsigned pb1, f32x16& c0, f32x16& c1) {
  __builtin_amdgcn_s_setprio(CONV_PRIO);
  u32x4 a0[2], b0[2], a1[2], b1[2];
#define CLD(i, n) do { a0[i] = ldsr<u32x4>(lds, pa0 + 32 * (n)); b0[i] = ldsr<u32x4>(lds, pb0 + 32 * (n) + 16 * ((n) >> 2)); \
    if (TWO) { a1[i] = ldsr<u32x4>(lds, pa1 + 32 * (n)); b1[i] = ldsr<u32x4>(lds, pb1 + 32 * (n) + 16 * ((n) >> 2)); } } while (0)
#define CMM(i) do { c0 = MFMA16(a0[i], b0[i], c0); if (TWO) c1 = MFMA16(a1[i], b1[i], c1); } while (0)
  CLD(0, 0); CLD(1, 1);
  c0 = MFMA16(a0[0], b0[0], c0);
  if (TWO && !SKIP0) c1 = MFMA16(a1[0], b1[0], c1);
  SBAR();
#pragma unroll 1
  for (int n4 = 0; n4 < 16; ++n4) {
    const unsigned oa = 128 * n4, ob = 144 * n4;
#define CLD4(i, k) do { a0[i] = ldsr<u32x4>(lds, pa0 + oa + 32 * (k)); b0[i] = ldsr<u32x4>(lds, pb0 + ob + 32 * (k) + 16 * ((k) >> 2)); \
    if (TWO) { a1[i] = ldsr<u32x4>(lds, pa1 + oa + 32 * (k)); b1[i] = ldsr<u32x4>(lds, pb1 + ob + 32 * (k) + 16 * ((k) >> 2)); } } while (0)
    CLD4(0, 2); SBAR(); CMM(1); SBAR();
    CLD4(1, 3); SBAR(); CMM(0); SBAR();
    CLD4(0, 4); SBAR(); CMM(1); SBAR();
    CLD4(1, 5); SBAR(); CMM(0); SBAR();
#undef CLD4
  }
#undef CLD
#undef CMM
  __builtin_amdgcn_s_setprio(3);
}
DI void conv_out(const f32x16& c, int d, int tl, char* ws, char* scratch, int lane) {
  const int r = lane & 31, h = lane >> 5;
#pragma unroll
  for (int g = 0; g < 4; ++g)
    ldsw<u32x2>(scratch, 2 * (16 * g + 8 * h + 4 * (r & 1) + 64 * (r >> 1)), u32x2{cvtpk(c[4 * g], c[4 * g + 1]), cvtpk(c[4 * g + 2], c[4 * g + 3])});
  const __amdgpu_buffer_rsrc_t rsY = __builtin_amdgcn_make_buffer_rsrc((void*)(ws + WS_YT + (size_t)d * (SEQ * 2) + 2048 * tl), 0, 2048, kRsrc3);
#pragma unroll
  for (int j = 0; j < 2; ++j) __builtin_amdgcn_raw_buffer_store_b128(ldsr<u32x4>(scratch, 1024 * j + 16 * lane), rsY, 1024 * j + 16 * lane, 0, 16);
}

DI void skip_tile(int rb, int hg, const u16* u, const u16* dskip, char* ws, u16* out, char* lds, int lane) {
  u32x4 y[9], uu[9];
#pragma unroll
  for (int j = 0; j < 9; ++j) {
    const int ch = 64 * j + lane, row = ch / 9, c = ch - 9 * row;
    uu[j] = *(const u32x4*)(u + (size_t)(64 * rb + row) * DM + 72 * hg + 8 * c);
  }
  gu32* cnt = (gu32*)(ws + WS_CNT) + 32 * (32 + hg);
#pragma unroll 1
  for (unsigned spins = 0; spins < (1u << 20); ++spins) {
    if (rfl(__hip_atomic_load(cnt, __ATOMIC_RELAXED, __HIP_MEMORY_SCOPE_AGENT)) == 32u) break;
    __builtin_amdgcn_s_sleep(4);
  }
  __builtin_amdgcn_fence(__ATOMIC_ACQUIRE, "wavefront");
  const __amdgpu_buffer_rsrc_t rsY = __builtin_amdgcn_make_buffer_rsrc((void*)(ws + WS_YT + (size_t)72 * hg * (SEQ * 2)), 0, 72 * SEQ * 2, kRsrc3);
#pragma unroll
  for (int j = 0; j < 9; ++j) {
    const int ch = 64 * j + lane, row = ch >> 3, c = ch & 7;
    y[j] = __builtin_amdgcn_raw_buffer_load_b128(rsY, row * (SEQ * 2) + 128 * rb + 16 * c, 0, 16);
  }
#pragma unroll
  for (int j = 0; j < 9; ++j) {
    const int ch = 64 * j + lane, row = ch >> 3, c = ch & 7;
    ldsw<u32x4>(lds, LDS_FA + row * 128 + 16 * (c ^ (wswz(row) << 1)), y[j]);
  }
  const int g = lane >> 4, li = lane & 15, q = li >> 2, p = lane & 3;
#pragma unroll
  for (int j = 0; j < 9; ++j) {
    const int R0 = 8 * j + q, R1 = R0 + 4;
    const s16x4 a0 = ldstr(lds, LDS_FA + R0 * 128 + ((g ^ wswz(R0)) * 32) + 8 * p), a1 = ldstr(lds, LDS_FA + R1 * 128 + ((g ^ wswz(R1)) * 32) + 8 * p);
    ldsw<u32x4>(lds, LDS_FB + (16 * g + li) * 144 + 16 * j, __builtin_bit_cast(u32x4, __builtin_shufflevector(a0, a1, 0, 1, 2, 3, 4, 5, 6, 7)));
  }
#pragma unroll
  for (int j = 0; j < 9; ++j) {
    const int ch = 64 * j + lane, row = ch / 9, c = ch - 9 * row;
    const u32x4 yy = ldsr<u32x4>(lds, LDS_FB + ch * 16), dd = *(const u32x4*)(dskip + 72 * hg + 8 * c);
    u32x4 o;
#pragma unroll
    for (int k = 0; k < 4; ++k) {
      const unsigned sk = cvtpk(bflo(uu[j][k]) * bflo(dd[k]), bfhi(uu[j][k]) * bfhi(dd[k]));
      o[k] = cvtpk(bflo(yy[k]) + bflo(sk), bfhi(yy[k]) + bfhi(sk));
    }
    *(u32x4*)(out + (size_t)(64 * rb + row) * (2 * DM) + DM + 72 * hg + 8 * c) = o;
  }
}

DI void tail_part(const f32x16& oa, const f32x16& ob, float ma, float mb, int w, int rb, int hg, const u16* u, const u16* dskip, char* ws, u16* out, char* lds) {
  int lane;
  asm volatile("v_mbcnt_lo_u32_b32 %0, -1, 0\n\tv_mbcnt_hi_u32_b32 %0, -1, %0" : "=v"(lane));
  const int r = lane & 31, h = lane >> 5, pt = w & 1, hp = (w >> 1) & 1, kh = w >> 2;

  {
    const f32x16& give = kh ? oa : ob;
    const float gm = kh ? ma : mb;
    char* own = lds + LDS_CMB + w * 3328;
#pragma unroll
    for (int g = 0; g < 3; ++g) ldsw<f32x4>(own, g * 1024 + lane * 16, f32x4{give[4 * g], give[4 * g + 1], give[4 * g + 2], give[4 * g + 3]});
    ldsw<float>(own, 3072 + lane * 4, gm);
    BAR();
    const char* oth = lds + LDS_CMB + (w ^ 4) * 3328;
    f32x16 keep = kh ? ob : oa;
    const float m1 = kh ? mb : ma, m2 = ldsr<float>(oth, 3072 + lane * 4);
    const float mm = fmaxf(m1, m2), f1 = EX2(m1 - mm), f2 = EX2(m2 - mm);
    float o[12];
#pragma unroll
    for (int g = 0; g < 3; ++g) {
      const f32x4 t = ldsr<f32x4>(oth, g * 1024 + lane * 16);
#pragma unroll
      for (int e = 0; e < 4; ++e) o[4 * g + e] = keep[4 * g + e] * f1 + t[e] * f2;
    }
    const float inv = 1.0f / half_lo(o[10]);
    const unsigned ot = LDS_OT + (32 * pt + r) * 144 + (2 * hp + kh) * 36;
    ldsw<unsigned>(lds, ot + 8 * h, cvtpk(o[0] * inv, o[1] * inv));
    ldsw<unsigned>(lds, ot + 8 * h + 4, cvtpk(o[2] * inv, o[3] * inv));
    ldsw<unsigned>(lds, ot + 16 + 8 * h, cvtpk(o[4] * inv, o[5] * inv));
    ldsw<unsigned>(lds, ot + 16 + 8 * h + 4, cvtpk(o[6] * inv, o[7] * inv));
    if (!h) ldsw<unsigned>(lds, ot + 32, cvtpk(o[8] * inv, o[9] * inv));
  }
  BAR();
  const __amdgpu_buffer_rsrc_t rsO = __builtin_amdgcn_make_buffer_rsrc((void*)(ws + WS_O + (size_t)64 * rb * (DM * 2)), 0, 64 * DM * 2, kRsrc3);
  {
    const int c0 = 64 * w + lane, row = c0 / 9, c = c0 - 9 * row;
    __builtin_amdgcn_raw_buffer_store_b128(ldsr<u32x4>(lds, LDS_OT + c0 * 16), rsO, row * (DM * 2) + hg * 144 + c * 16, 0, 16);
    if (w == 0) {
      const int c1 = 512 + lane, row1 = c1 / 9, cc = c1 - 9 * row1;
      __builtin_amdgcn_raw_buffer_store_b128(ldsr<u32x4>(lds, LDS_OT + c1 * 16), rsO, row1 * (DM * 2) + hg * 144 + cc * 16, 0, 16);
    }
  }
  asm volatile("s_waitcnt vmcnt(0)" ::: "memory");
  BAR();
  STAMP(1, 9);
  gu32* cnt = (gu32*)(ws + WS_CNT) + 32 * rb;
  if (w == 0) {
    if (lane == 0) __hip_atomic_fetch_add(cnt, 1u, __ATOMIC_RELAXED, __HIP_MEMORY_SCOPE_AGENT);
    for (unsigned spins = 0; spins < (1u << 20); ++spins) {
      if (rfl(__hip_atomic_load(cnt, __ATOMIC_RELAXED, __HIP_MEMORY_SCOPE_AGENT)) == 8u) break;
      __builtin_amdgcn_s_sleep(1);
    }
    __builtin_amdgcn_fence(__ATOMIC_ACQUIRE, "wavefront");
  }
  BAR();
  STAMP(1, 10);
  if (w == 4) skip_tile(rb, hg, u, dskip, ws, out, lds, lane);
  if (w >= 4) return;

  const int li = lane & 15, g = lane >> 4, q = li >> 2, p = lane & 3;
  u32x4 xf[12];
#define XLD(ks) __builtin_amdgcn_raw_buffer_load_b128(rsO, (16 * w + li) * (DM * 2) + 64 * (ks) + 16 * g, 0, 16)
#pragma unroll
  for (int ks = 0; ks < 12; ++ks) xf[ks] = XLD(ks);
  STAMP(1, 11);
  f32x4 acc[5] = {};
  const unsigned wb = LDS_CV + (8 * g + q) * 144 + 8 * p;
  s16x4 wa[2][10];
#define WRD(buf, ks) do { _Pragma("unroll") for (int nt = 0; nt < 5; ++nt) { \
    wa[buf][2 * nt] = ldstr(lds, wb + 32 * (ks) * 144 + 32 * nt); wa[buf][2 * nt + 1] = ldstr(lds, wb + (32 * (ks) + 4) * 144 + 32 * nt); } } while (0)
  WRD(0, 0);
#pragma unroll
  for (int ks = 0; ks < 18; ++ks) {
    if (ks + 1 < 18) WRD((ks + 1) & 1, ks + 1);
    SBAR();
#pragma unroll
    for (int nt = 0; nt < 5; ++nt) {
      const auto a = __builtin_shufflevector(wa[ks & 1][2 * nt], wa[ks & 1][2 * nt + 1], 0, 1, 2, 3, 4, 5, 6, 7);
      acc[nt] = __builtin_amdgcn_mfma_f32_16x16x32_bf16(__builtin_bit_cast(bf16x8, a), __builtin_bit_cast(bf16x8, xf[ks % 12]), acc[nt], 0, 0, 0);
    }
    SBAR();
    if (ks < 6) xf[ks] = XLD(ks + 12);
    SBAR();
  }
#undef WRD
#undef XLD
  STAMP(1, 12);
  u16* orow = out + (size_t)(64 * rb + 16 * w + li) * (2 * DM) + 72 * hg + 4 * g;
#pragma unroll
  for (int nt = 0; nt < 5; ++nt)
    if (nt < 4 || lane < 32) *(u32x2*)(orow + 16 * nt) = u32x2{cvtpk(acc[nt][0], acc[nt][1]), cvtpk(acc[nt][2], acc[nt][3])};
  STAMP(1, 15);
}

__global__ void __launch_bounds__(832)
k2_main(const u16* wo, const u16* u, const u16* dskip, char* ws, u16* out) {
  extern __shared__ __attribute__((aligned(16))) char lds[];
  const unsigned lds0 = (unsigned)(uintptr_t)lds;
  const int b = BID, lane = TID & 63, w = rfl(TID >> 6);
  const int rb = (b & 7) + 8 * (b >> 6), hg = (b >> 3) & 7;
  STAMP(1, 0);
  if (TID < 32) ((LDSP unsigned*)((LDSP char*)lds + LDS_FLAG))[TID] = 0u;
  BAR();
  if (w >= 8) {
    const int sw = w - 8, i = b >> 3, xc = b & 7;
    __builtin_amdgcn_s_setprio(3);
    const char* kvg = ws + WS_KV;
    if (sw < 4) {
#pragma unroll
      for (int j = 0; j < 5; ++j) {
        const int ins = sw + 4 * j;
        if (ins < 19) glds16s(kvg + 1024 * ins, 16 * lane, lds0 + 1024 * ins);
      }
      WAITV(0);
      ONE_LANE(flag_add(lds, F_SEG0));
    }
    if (sw == 4) {
      auto issue = [&](int s) {
#pragma unroll
        for (int j = 0; j < 19; ++j) glds16s(kvg + s * KV_SEG + 1024 * j, 16 * lane, lds0 + (s % 3) * KV_SEG + 1024 * j);
      };
      issue(1); issue(2);
      WAITV(19);
      flag_set(lds, F_FULL, 2);
      WAITV(0);
      flag_set(lds, F_FULL, 3);
#pragma unroll 1
      for (int s = 3; s < 8; ++s) {
        done_wait<4>(lds, s - 2, lane);
        issue(s);
        WAITV(0);
        flag_set(lds, F_FULL, s + 1);
      }
      STAMP(1, 15);
      return;
    }
    const int d0 = 72 * xc + 2 * i, dx = 72 * xc + 64 + (i >> 2), third = (i & 3) < 2;
    conv_fill_share(d0, dx, third, sw, ws, lds0, lane);
    WAITV(0);
    ONE_LANE(flag_add(lds, F_CFILL));
    STAMP(1, 6);
    {
      const int r = lane & 31, h = lane >> 5;
      const unsigned kb = LDS_CV + (3 - (r & 3)) * CV_K + 2 * (8 * h - 8 * (r >> 2));
      const unsigned ub = LDS_CV + CV_U + (r & 1) * CV_UP + 144 * (r >> 1) + 16 * h;
      const unsigned A1 = kb + 2 * 1080, A2 = kb + 2 * 56, B1 = ub, B3 = ub + 2304;
      char* scr = lds + LDS_SCR + sw * 2048;
      f32x16 c0 = {}, c1 = {};
      lds_wait<4>(lds, F_CFILL, 4);
      if (sw == 2) {
        conv_steps<true, false>(lds, A1, B1, A1 + CV_CH, B1 + CV_CH, c0, c1);
        conv_out(c0, d0, 0, ws, scr, lane);
        conv_out(c1, d0 + 1, 0, ws, scr, lane);
      } else if (sw < 2 || (i & 3) == 0) {
        const unsigned so = (sw < 2 ? sw : 2) * CV_CH;
        conv_steps<true, true>(lds, A2 + so, B1 + so, A1 + so, B3 + so, c0, c1);
#pragma unroll
        for (int k = 0; k < 16; ++k) c0[k] += c1[k];
        conv_out(c0, sw < 2 ? d0 + sw : dx, 1, ws, scr, lane);
      } else if (third) {
        conv_steps<false, false>(lds, A1 + 2 * CV_CH, B1 + 2 * CV_CH, 0, 0, c0, c1);
        conv_out(c0, dx, 0, ws, scr, lane);
      }
      STAMP(1, 7);
    }
    asm volatile("s_waitcnt vmcnt(0) lgkmcnt(0)" ::: "memory");
    unsigned arrived = 0;
    if (lane == 0) arrived = __hip_atomic_fetch_add((LDSP unsigned*)((LDSP char*)lds + LDS_FLAG) + F_CDONE, 1u, __ATOMIC_RELAXED, __HIP_MEMORY_SCOPE_WORKGROUP);
    if (rfl(arrived) == 3 && lane == 0) __hip_atomic_fetch_add((gu32*)(ws + WS_CNT) + 32 * (32 + xc), 1u, __ATOMIC_RELAXED, __HIP_MEMORY_SCOPE_AGENT);
    lds_wait<4>(lds, F_CDONE, 4);
    {
      unsigned vo[3];
#pragma unroll
      for (int k = 0; k < 3; ++k) { const int ch = 64 * (sw + 4 * k) + lane, row = ch / 9, c = ch - 9 * row; vo[k] = row * (DM * 2) + c * 16; }
      const char* wb = (const char*)(wo + 72 * hg);
#pragma unroll 1
      for (int a = 0; a < 9; ++a) {
#pragma unroll
        for (int k = 0; k < 3; ++k)
          if (k < 2 || sw == 0) glds16s(wb + (size_t)a * 64 * (DM * 2), vo[k], lds0 + LDS_CV + 1024 * (9 * a + sw + 4 * k));
      }
    }
    STAMP(1, 8);
    WAITV(0);
    STAMP(1, 15);
    return;
  }
  const int r = lane & 31, h = lane >> 5;
  const int pt = w & 1, hp = (w >> 1) & 1, kh = w >> 2;
  const int pos = 64 * rb + 32 * pt + r, head = 4 * hg + 2 * hp;

  const char* qrow = ws + WS_Q + (size_t)pos * (DM * 2) + head * (DH * 2);
  const u32x4 qam = *(const u32x4*)(qrow + 16 * h), qbm = *(const u32x4*)(qrow + 36 + 16 * h);
  const unsigned qa16 = *(const unsigned*)(qrow + 32), qb16 = *(const unsigned*)(qrow + 36 + 32);
  const char* k0p = ws + WS_KV + KV_HDR + kh * 1152;
  u32x4 kA = *(const u32x4*)(k0p + h * 512 + r * 16);
  u32x2 kAx = {*(const unsigned*)(k0p + 1024 + r * 4), 0x3f80u};
  const float kmax2 = wave_max(((const float*)(ws + WS_KMAX))[lane]);
  auto sq = [](unsigned u) { return bflo(u) * bflo(u) + bfhi(u) * bfhi(u); };
  const float na = half_sum(sq(qam[0]) + sq(qam[1]) + sq(qam[2]) + sq(qam[3]) + (h ? 0.f : sq(qa16)));
  const float nb = half_sum(sq(qbm[0]) + sq(qbm[1]) + sq(qbm[2]) + sq(qbm[3]) + (h ? 0.f : sq(qb16)));
  const float ba = rbf(__builtin_amdgcn_sqrtf(na * kmax2) * 1.01f + 1e-6f), bb = rbf(__builtin_amdgcn_sqrtf(nb * kmax2) * 1.01f + 1e-6f);
  const bool fast = __builtin_amdgcn_ballot_w64(!(ba <= FAST_BOUND && bb <= FAST_BOUND)) == 0;

  KVAddr ad;
  {
    const int vr = r <= 18 ? r : 19;
    const unsigned tb = kh * KV_HALF;
    ad.k = tb + h * 512 + r * 16; ad.kx = tb + 1024 + r * 4; ad.v = tb + 1152 + h * KV_VP + vr * 16;
  }
  f32x16 oa = {}, ob = {};
  float ma, mb;
  if (fast) {
    ma = 0.f; mb = 0.f;
    u32x2 qax = {h ? 0u : qa16, h ? 0u : (cvtpk(-ba, 0.f))}, qbx = {h ? 0u : qb16, h ? 0u : (cvtpk(-bb, 0.f))};
    u32x4 kB, vA0, vA1, vB0, vB1;
    u32x2 kBx = {0u, 0x3f80u};
    f32x16 zero16 = {}, sa, sb;
    u32x8 pa, pb = {};
    sa = MFMA16(kA, qam, zero16); sa = MFMA8(kAx, qax, sa);
    STAMP(1, 1);
    lds_wait<1>(lds, F_SEG0, 4);
    STAMP(1, 2);
    LDV(vB0, vB1, 0);
    unsigned off = 0;
#pragma unroll 1
    for (int s = 0; s < 8; ++s) {
      STEP(kA, kAx, kB, kBx, vB0, vB1, vA0, vA1, off);
      STEP(kB, kBx, kA, kAx, vA0, vA1, vB0, vB1, off + KV_TILE);
      STEP(kA, kAx, kB, kBx, vB0, vB1, vA0, vA1, off + 2 * KV_TILE);
      const unsigned full = flag_get(lds, F_FULL);
      STEP(kB, kBx, kA, kAx, vA0, vA1, vB0, vB1, off + 3 * KV_TILE);
      flag_set(lds, F_DONE + w, s + 1);
      off = off == 2 * KV_SEG ? 0 : off + KV_SEG;
      if (s < 7 && rfl(full) < s + 2) lds_wait<1>(lds, F_FULL, s + 2);
    }
    ob = MFMA16(vB0, lo4(pb), ob);
    ob = MFMA16(vB1, hi4(pb), ob);
  } else {
    ma = -INFINITY; mb = -INFINITY;
    const u32x2 qax = {h ? 0u : qa16, 0u}, qbx = {h ? 0u : qb16, 0u};
    kAx[1] = 0u;
#pragma unroll 1
    for (int t = 0; t < 32; ++t) {
      if (t == 0) lds_wait<1>(lds, F_SEG0, 4); else if ((t & 3) == 0) lds_wait<1>(lds, F_FULL, (t >> 2) + 1);
      const unsigned off = ((t >> 2) % 3) * KV_SEG + (t & 3) * KV_TILE;
      u32x4 kn, v0, v1; u32x2 knx = {0u, 0u};
      LDK(kn, knx, off); LDV(v0, v1, off);
      if ((t & 3) == 3) flag_set(lds, F_DONE + w, (t >> 2) + 1);
      slow_tile(kA, kAx, v0, v1, qam, qax, oa, ma);
      slow_tile(kA, kAx, v0, v1, qbm, qbx, ob, mb);
      kA = kn; kAx = knx;
    }
  }
  STAMP(1, 5);
  BAR();
  tail_part(oa, ob, ma, mb, w, rb, hg, u, dskip, ws, out, lds);
}

#if defined(__HIP_DEVICE_COMPILE__)
#pragma clang attribute pop
#endif
extern "C" void kernel_launch(void* const* d_in, const int* in_sizes, int n_in, void* d_out, int out_size, void* d_ws, size_t ws_size,
                              hipStream_t stream) {
  const u16* x = (const u16*)d_in[0];
  const u16* wq = (const u16*)d_in[1];
  const u16* wk = (const u16*)d_in[2];
  const u16* wv = (const u16*)d_in[3];
  const u16* wo = (const u16*)d_in[4];
  const u16* u = (const u16*)d_in[5];
  const u16* Are = (const u16*)d_in[6];
  const u16* Aim = (const u16*)d_in[7];
  const u16* Bre = (const u16*)d_in[8];
  const u16* Bim = (const u16*)d_in[9];
  const u16* Cre = (const u16*)d_in[10];
  const u16* Cim = (const u16*)d_in[11];
  const u16* logdt = (const u16*)d_in[12];
  const u16* dskip = (const u16*)d_in[13];
  char* ws = (char*)d_ws;
  (void)hipFuncSetAttribute((const void*)k1_prep, hipFuncAttributeMaxDynamicSharedMemorySize, K1_LDS);
  (void)hipFuncSetAttribute((const void*)k2_main, hipFuncAttributeMaxDynamicSharedMemorySize, K2_LDS);
  k1_prep<<<400, 512, K1_LDS, stream>>>(x, wq, wk, wv, u, Are, Aim, Bre, Bim, Cre, Cim, logdt, ws);
  k2_main<<<256, 832, K2_LDS, stream>>>(wo, u, dskip, ws, (u16*)d_out);
}
```

```cpp
#if defined(__HIP_DEVICE_COMPILE__)
#pragma clang attribute push(__attribute__((target("no-packed-fp32-ops"))), apply_to = function)
#endif
#include <hip/hip_runtime.h>
#include <stdint.h>

typedef __bf16 bf16x2 __attribute__((ext_vector_type(2)));
typedef __bf16 bf16x8 __attribute__((ext_vector_type(8)));
typedef short s16x4 __attribute__((ext_vector_type(4)));
typedef float f32x2 __attribute__((ext_vector_type(2)));
typedef float f32x4 __attribute__((ext_vector_type(4)));
typedef float f32x16 __attribute__((ext_vector_type(16)));
typedef unsigned u32x2 __attribute__((ext_vector_type(2)));
typedef unsigned u32x4 __attribute__((ext_vector_type(4)));
typedef unsigned short u16;
#define DI __device__ __forceinline__
#define BID ((int)__builtin_amdgcn_workgroup_id_x())
#define TID ((int)__builtin_amdgcn_workitem_id_x())
#define LDSP __attribute__((address_space(3)))
#define SBAR() __builtin_amdgcn_sched_barrier(0)
#define PIN(x) asm volatile("" : "+v"(x))
#define MFMA16(a, b, c) __builtin_amdgcn_mfma_f32_32x32x16_bf16(__builtin_bit_cast(bf16x8, a), __builtin_bit_cast(bf16x8, b), c, 0, 0, 0)
#define MFMA8(a, b, c) __builtin_amdgcn_mfma_f32_32x32x8bf16_1k(__builtin_bit_cast(s16x4, a), __builtin_bit_cast(s16x4, b), c, 0, 0, 0)

constexpr int SEQ = 2048, DM = 576, DH = 18, NST = 128;
constexpr int KV_VP = 320;
constexpr int KV_TILE = 1152 + 4 * KV_VP;
constexpr int KV_HALF = 4 * KV_TILE, KV_SEG = 2 * KV_HALF;
constexpr int KV_HDR = 8 * KV_SEG;
constexpr int UT_ROW = 2064;
constexpr size_t WS_EPOCH = 0;
constexpr size_t WS_KVF = 256;
constexpr size_t WS_OF = WS_KVF + 8 * 2048;
constexpr size_t WS_UF = WS_OF + 2048;
constexpr size_t WS_YF = WS_UF + 2048;
constexpr size_t WS_KMAX = WS_YF + 2048;
constexpr size_t WS_KV = WS_KMAX + 1024;
constexpr size_t WS_O = WS_KV + 163840;
constexpr size_t WS_UT = WS_O + (size_t)SEQ * DM * 2;
constexpr size_t WS_YT = WS_UT + (size_t)DM * UT_ROW * 2;

DI unsigned cvtpk(float a, float b) { f32x2 v = {a, b}; return __builtin_bit_cast(unsigned, __builtin_convertvector(v, bf16x2)); }
DI float bflo(unsigned u) { return __uint_as_float(u << 16); }
DI float bfhi(unsigned u) { return __uint_as_float(u & 0xffff0000u); }
DI float bf2f(u16 x) { return __uint_as_float((unsigned)x << 16); }
DI float rbf(float x) { return bflo(cvtpk(x, x)); }
DI int crow(int reg, int h) { return (reg & 3) + 8 * (reg >> 2) + 4 * h; }
DI void glds16s(const void* sbase, unsigned voff, unsigned lds_base) {
  unsigned sv;
  asm volatile("s_mov_b32 %0, m0\n\ts_mov_b32 m0, %3\n\ts_nop 0\n\tglobal_load_lds_dwordx4 %1, %2\n\ts_mov_b32 m0, %0"
               : "=&s"(sv) : "v"(voff), "s"(sbase), "s"(lds_base) : "memory");
}
DI void glds16c(const void* sbase, unsigned voff, unsigned lds_base) {
  unsigned sv;
  asm volatile("s_mov_b32 %0, m0\n\ts_mov_b32 m0, %3\n\ts_nop 0\n\tglobal_load_lds_dwordx4 %1, %2 sc1\n\ts_mov_b32 m0, %0"
               : "=&s"(sv) : "v"(voff), "s"(sbase), "s"(lds_base) : "memory");
}
#define WAITV(n) asm volatile("s_waitcnt vmcnt(" #n ")" ::: "memory")
#define WAITV_BAR(n) asm volatile("s_waitcnt vmcnt(" #n ") lgkmcnt(0)\n\ts_barrier" ::: "memory")
#define SB() asm volatile("s_barrier" ::: "memory")
#define BAR() asm volatile("s_waitcnt lgkmcnt(0)\n\ts_barrier" ::: "memory")
#define STAMP(kern, i)
DI unsigned rfl(unsigned x) { return __builtin_amdgcn_readfirstlane(x); }
template <typename T> DI T ldsr(const char* lds, unsigned off) { return *(const LDSP T*)((const LDSP char*)lds + off); }
template <typename T> DI void ldsw(char* lds, unsigned off, T v) { *(LDSP T*)((LDSP char*)lds + off) = v; }
DI s16x4 ldstr(const char* lds, unsigned off) {
  typedef short v4i16_t __attribute__((ext_vector_type(4)));
  return __builtin_bit_cast(s16x4, __builtin_amdgcn_ds_read_tr16_b64_v4i16((LDSP v4i16_t*)((LDSP char*)lds + off)));
}
DI float half_sum(float v) {
  const auto t = __builtin_amdgcn_permlane32_swap(__float_as_uint(v), __float_as_uint(v), false, false);
  return __uint_as_float(t[0]) + __uint_as_float(t[1]);
}
DI float half_max(float v) {
  const auto t = __builtin_amdgcn_permlane32_swap(__float_as_uint(v), __float_as_uint(v), false, false);
  return fmaxf(__uint_as_float(t[0]), __uint_as_float(t[1]));
}
DI float half_lo(float v) {
  const auto t = __builtin_amdgcn_permlane32_swap(__float_as_uint(v), __float_as_uint(v), false, false);
  return __uint_as_float(t[0]);
}
template <int CTRL> DI float dpp_max(float v) {
  return fmaxf(v, __uint_as_float(__builtin_amdgcn_update_dpp(__float_as_uint(v), __float_as_uint(v), CTRL, 0xf, 0xf, false)));
}
DI float wave_max(float v) {
  v = dpp_max<0xb1>(v);
  v = dpp_max<0x4e>(v);
  v = dpp_max<0x141>(v);
  v = dpp_max<0x140>(v);
  const unsigned u = __float_as_uint(v);
  const float a = __uint_as_float(__builtin_amdgcn_readlane(u, 0)), b = __uint_as_float(__builtin_amdgcn_readlane(u, 16));
  const float c = __uint_as_float(__builtin_amdgcn_readlane(u, 32)), d = __uint_as_float(__builtin_amdgcn_readlane(u, 48));
  return fmaxf(fmaxf(a, b), fmaxf(c, d));
}

DI float xrow16_sum(float x) {
  const auto s = __builtin_amdgcn_permlane16_swap(__float_as_uint(x), __float_as_uint(x), false, false);
  return half_sum(__uint_as_float(s[0]) + __uint_as_float(s[1]));
}
template <int N> DI void waitv() { asm volatile("s_waitcnt vmcnt(%0)" ::"i"(N) : "memory"); }
DI int wswz(int row) { return ((row >> 1) & 1) | (((row >> 3) & 1) << 1); }
constexpr int kRsrc3 = 0x00020000;
typedef __attribute__((address_space(1))) unsigned gu32;
typedef __attribute__((address_space(1))) unsigned long long gu64;
typedef unsigned long long u64;
DI __amdgpu_buffer_rsrc_t rsrc(const void* p, int bytes) { return __builtin_amdgcn_make_buffer_rsrc((void*)p, 0, bytes, kRsrc3); }
constexpr int SC1 = 16;

#define QWIN 2
#define KV_MODE 1
#define KV_DELAY 48
#define KV_POLL 8
#define LPOLL 1
constexpr int LDS_CV = 3 * KV_SEG;
constexpr int LDS_WKV = 0;
constexpr int LDS_UA = 23040;
constexpr int LDS_QT = 40960;
constexpr int LDS_K0 = 53248;
constexpr int GS = 20736;
constexpr int CV_CH = 31744, CV_K = 4416, CV_U = 17664, CV_UP = 7040;
constexpr int LDS_GEN = LDS_CV + 4 * GS;
constexpr int GEN_AREA = 6656, GEN_PITCH = 272, GEN_P = 21 * GEN_PITCH;
constexpr int LDS_CMB = 0;
constexpr int LDS_OT = 8 * 3328;
constexpr int LDS_FA = LDS_CV + 82944, LDS_FB = LDS_FA + 9216;
constexpr int LDS_SCR = LDS_CV + 3 * CV_CH;
constexpr int LDS_FLAG = LDS_SCR + 4 * 2048;
constexpr int K_LDS = LDS_FLAG + 256;
constexpr float FAST_BOUND = 30.f;
#define CONV_PRIO 0

constexpr int F_FULL = 0, F_SEG0 = 1, F_CFILL = 2, F_CDONE = 3, F_TAGOK = 4, F_TAG = 5  , F_WKV = 7, F_DONE = 8  ,
              F_GF = 16  , F_GD = 32  , F_QDONE = 48, F_QRD = 49, F_KVOK = 50, F_UDONE = 51, F_KCOP = 52;
DI void flag_add(char* lds, int word) { __hip_atomic_fetch_add((LDSP unsigned*)((LDSP char*)lds + LDS_FLAG) + word, 1u, __ATOMIC_RELAXED, __HIP_MEMORY_SCOPE_WORKGROUP); }
DI void flag_set(char* lds, int word, unsigned v) {
  asm volatile("" ::: "memory");
  __hip_atomic_store((LDSP unsigned*)((LDSP char*)lds + LDS_FLAG) + word, v, __ATOMIC_RELAXED, __HIP_MEMORY_SCOPE_WORKGROUP);
  asm volatile("" ::: "memory");
}
DI unsigned flag_get(char* lds, int word) { return __hip_atomic_load((LDSP unsigned*)((LDSP char*)lds + LDS_FLAG) + word, __ATOMIC_RELAXED, __HIP_MEMORY_SCOPE_WORKGROUP); }
template <int SLEEP> DI void lds_wait(char* lds, int word, unsigned need) {
#pragma unroll 1
  for (unsigned spins = 0; spins < (1u << 20); ++spins) {
    if (rfl(flag_get(lds, word)) >= need) break;
    __builtin_amdgcn_s_sleep(SLEEP);
  }
  asm volatile("" ::: "memory");
}
template <int SLEEP> DI void done_wait(char* lds, unsigned need, int lane) {
#pragma unroll 1
  for (unsigned spins = 0; spins < (1u << 20); ++spins) {
    if (__builtin_amdgcn_ballot_w64(flag_get(lds, F_DONE + (lane & 7)) < need) == 0) break;
    __builtin_amdgcn_s_sleep(SLEEP);
  }
  asm volatile("" ::: "memory");
}
#define ONE_LANE(stmt) do { asm volatile("" ::: "memory"); if (lane == 0) { stmt; } asm volatile("" ::: "memory"); } while (0)

#define FLAG_ADD(word) ONE_LANE(flag_add(lds, word))
DI u64 get_tag(char* lds) {
  lds_wait<1>(lds, F_TAGOK, 1);
  return (u64)rfl(flag_get(lds, F_TAG)) | ((u64)rfl(flag_get(lds, F_TAG + 1)) << 32);
}
DI void tag_store(char* p, u64 tag) { __hip_atomic_store((gu64*)p, tag, __ATOMIC_RELAXED, __HIP_MEMORY_SCOPE_AGENT); }
template <int N, int SLEEP> DI void tags_wait(const char* p, u64 tag, int lane) {
#pragma unroll 1
  for (unsigned spins = 0; spins < (1u << 20); ++spins) {
    bool ok = true;
#pragma unroll
    for (int j = 0; j < (N + 63) / 64; ++j) {
      const int k = 64 * j + lane;
      ok &= __hip_atomic_load((gu64*)p + (k < N ? k : 0), __ATOMIC_RELAXED, __HIP_MEMORY_SCOPE_AGENT) == tag;
    }
    if (__builtin_amdgcn_ballot_w64(!ok) == 0) break;
    __builtin_amdgcn_s_sleep(SLEEP);
  }
  __builtin_amdgcn_fence(__ATOMIC_ACQUIRE, "wavefront");
  asm volatile("" ::: "memory");
}

template <int SLEEP> DI void tags256_wait(const char* p, u64 tag, int lane) {
  u64 a[4], c[4];
#define TLD(v) do { _Pragma("unroll") for (int j = 0; j < 4; ++j) v[j] = __hip_atomic_load((gu64*)p + 64 * j + lane, __ATOMIC_RELAXED, __HIP_MEMORY_SCOPE_AGENT); } while (0)
#define TOK(v) (__builtin_amdgcn_ballot_w64(!(v[0] == tag && v[1] == tag && v[2] == tag && v[3] == tag)) == 0)
  TLD(a);
#pragma unroll 1
  for (unsigned spins = 0; spins < (1u << 19); ++spins) {
    __builtin_amdgcn_s_sleep(SLEEP);
    TLD(c);
    if (TOK(a)) break;
    __builtin_amdgcn_s_sleep(SLEEP);
    TLD(a);
    if (TOK(c)) break;
  }
#undef TLD
#undef TOK
  __builtin_amdgcn_fence(__ATOMIC_ACQUIRE, "wavefront");
  asm volatile("" ::: "memory");
}

constexpr int NI = 7;
template <int IW> constexpr int q_cnt(int s) { return s > 8 ? 0 : (s >= 1 && s <= 4) ? (21 - IW + NI - 1) / NI : (18 - IW + NI - 1) / NI; }
template <int IW, int S> DI void q_issue(const char* wb, const char* xb, int kvg, const unsigned (&vw)[3], const unsigned (&vx)[3], unsigned lds0, int lane) {
  const unsigned sb = lds0 + LDS_CV + (S & 3) * GS;
#pragma unroll
  for (int j = 0; j < q_cnt<IW>(S); ++j) {
    const int p = IW + NI * j;
    if constexpr (S >= 1 && S <= 4) { if (p < 20 || lane < 16) glds16s(wb + (size_t)144 * (S - 1) * (DM * 2), vw[j], sb + 1024 * p); }
    else glds16s(xb + (size_t)16 * (S == 0 ? kvg : S - 5) * (DM * 2), vx[j], sb + 1024 * p);
  }
}
template <int IW, int S> DI void q_steps(const char* wb, const char* xb, int kvg, const unsigned (&vw)[3], const unsigned (&vx)[3], char* lds, unsigned lds0, int lane) {
  waitv<q_cnt<IW>(S + 1) + (QWIN > 2 ? q_cnt<IW>(S + 2) : 0) + (QWIN > 3 ? q_cnt<IW>(S + 3) : 0)>();
  FLAG_ADD(F_GF + S);
  if constexpr (S + QWIN < 9) {
    if constexpr (S + QWIN >= 4) lds_wait<LPOLL>(lds, F_GD + S + QWIN - 4, S + QWIN == 4 ? 1 : 5);
    q_issue<IW, S + QWIN>(wb, xb, kvg, vw, vx, lds0, lane);
  }
  if constexpr (S + 1 < 9) q_steps<IW, S + 1>(wb, xb, kvg, vw, vx, lds, lds0, lane);
}
DI unsigned x_piece(int p, int lane) {
  const int n = 64 * p + lane, row = n / 72, c = n - 72 * row;
  return row * (DM * 2) + 16 * ((c & ~7) | ((c ^ (row >> 1)) & 7));
}
template <int IW> DI void q_loader(const u16* x, const u16* wq, int rb, int hg, char* lds, unsigned lds0, int lane) {
  unsigned vw[3], vx[3];
#pragma unroll
  for (int j = 0; j < 3; ++j) vx[j] = x_piece(IW + NI * j < 18 ? IW + NI * j : 0, lane);
  const char* wb = (const char*)(wq + 72 * hg);
  const char* xb = (const char*)(x + (size_t)64 * rb * DM);
  q_issue<IW, 0>(wb, xb, hg & 3, vw, vx, lds0, lane);
#pragma unroll
  for (int j = 0; j < 3; ++j) { const int n = 64 * (IW + NI * j) + lane, row = n / 9, c = n - 9 * row; vw[j] = row * (DM * 2) + 16 * c; }
  q_issue<IW, 1>(wb, xb, hg & 3, vw, vx, lds0, lane);
  if constexpr (QWIN > 2) q_issue<IW, 2>(wb, xb, hg & 3, vw, vx, lds0, lane);
  if constexpr (QWIN > 3) q_issue<IW, 3>(wb, xb, hg & 3, vw, vx, lds0, lane);
  q_steps<IW, 0>(wb, xb, hg & 3, vw, vx, lds, lds0, lane);
}
DI void x_bases(int lane, unsigned (&xb)[2]) {
  const int li = lane & 15, g = lane >> 4, xs = (li >> 1) & 7;
  xb[0] = li * (DM * 2) + 16 * (g ^ xs); xb[1] = li * (DM * 2) + 16 * ((4 + g) ^ xs);
}
#define XFRAG(base, ks) ldsr<u32x4>(lds, (base)[(ks) & 1] + 128 * ((ks) >> 1))

DI void q_compute(const u16* wk, const u16* wv, int hg, char* lds, int w, int lane, char* ws) {
  {
    typedef unsigned u32x3 __attribute__((ext_vector_type(3)));
    const char* src = (const char*)(hg >> 2 ? wv : wk);
    u32x3 t[6];
#pragma unroll
    for (int j = 0; j < 6; ++j) { const int i = w + 5 * j; __builtin_memcpy(&t[j], src + 12 * (64 * (i < 27 ? i : 0) + lane), 12); }
#pragma unroll
    for (int j = 0; j < 6; ++j) {
      const int i = w + 5 * j, e = 64 * i + lane, k = e / 3, c = e - 3 * k;
      if (i < 27) { const unsigned o = LDS_WKV + 40 * k + 12 * c; ldsw<unsigned>(lds, o, t[j][0]); ldsw<unsigned>(lds, o + 4, t[j][1]); ldsw<unsigned>(lds, o + 8, t[j][2]); }
    }
    asm volatile("s_waitcnt lgkmcnt(0)" ::: "memory");
    FLAG_ADD(F_WKV);
    STAMP(1, 6);
  }
  const int li = lane & 15, g = lane >> 4, q = li >> 2, p = lane & 3;
  u32x4 wf[18];
#pragma unroll
  for (int s = 0; s < 4; ++s) {
    lds_wait<LPOLL>(lds, F_GF + 1 + s, NI);
    const unsigned sb = LDS_CV + ((s + 1) & 3) * GS;
#pragma unroll
    for (int ks = 0; ks < 18; ++ks) {
#pragma unroll
      for (int t = 0; t < 2; ++t) {
        const int half = 2 * ks;
        const bool lo = half / 9 == s, hi = (half + 1) / 9 == s;
        if (lo || hi) {
          const int R = 8 * (g & 1) + 4 * t + q;
          const int grp = (g >> 1) ? (hi ? half + 1 - 9 * s : 0) : (lo ? half - 9 * s : 0);
          const u32x2 vv = __builtin_bit_cast(u32x2, ldstr(lds, sb + (16 * grp + R) * 144 + 32 * w + 8 * p));
          const bool mine = (g >> 1) ? hi : lo;
          if (lo) { wf[ks][2 * t] = vv[0]; wf[ks][2 * t + 1] = vv[1]; }
          else { wf[ks][2 * t] = mine ? vv[0] : wf[ks][2 * t]; wf[ks][2 * t + 1] = mine ? vv[1] : wf[ks][2 * t + 1]; }
        }
      }
    }
    asm volatile("s_waitcnt lgkmcnt(0)" ::: "memory");
    FLAG_ADD(F_GD + 1 + s);
  }
  STAMP(1, 7);
  unsigned qo[4];
#pragma unroll
  for (int e = 0; e < 4; ++e) { const int n = 16 * w + 4 * g + e, hh = n / 18; qo[e] = LDS_QT + li * 192 + hh * 48 + 2 * (n - 18 * hh); }
#pragma unroll 1
  for (int pt = 0; pt < 4; ++pt) {
    lds_wait<LPOLL>(lds, F_GF + 5 + pt, NI);
    const unsigned sb = LDS_CV + ((5 + pt) & 3) * GS;
    unsigned xb[2];
    x_bases(lane, xb);
    xb[0] += sb; xb[1] += sb;
    f32x4 acc = {};
    u32x4 bf[8];
#pragma unroll
    for (int ks = 0; ks < 8; ++ks) bf[ks] = XFRAG(xb, ks);
    SBAR();
#pragma unroll
    for (int ks = 0; ks < 18; ++ks) {
      acc = __builtin_amdgcn_mfma_f32_16x16x32_bf16(__builtin_bit_cast(bf16x8, wf[ks]), __builtin_bit_cast(bf16x8, bf[ks % 8]), acc, 0, 0, 0);
      SBAR();
      if (ks < 10) bf[ks % 8] = XFRAG(xb, ks + 8);
      SBAR();
    }
    asm volatile("" ::: "memory");
    const unsigned a = cvtpk(acc[0], acc[1]), b2 = cvtpk(acc[2], acc[3]);
    if (16 * w + 4 * g < 72) {
      ldsw<u16>(lds, qo[0] + 3072 * pt, (u16)a); ldsw<u16>(lds, qo[1] + 3072 * pt, (u16)(a >> 16));
      ldsw<u16>(lds, qo[2] + 3072 * pt, (u16)b2); ldsw<u16>(lds, qo[3] + 3072 * pt, (u16)(b2 >> 16));
    }
  }
  asm volatile("s_waitcnt lgkmcnt(0)" ::: "memory");
  FLAG_ADD(F_QDONE);
}

template <bool ISV> DI void kv_products(const char* lds, int lane, const u32x4 (&xf)[18], f32x4 (&acc)[2]) {
  const int li = lane & 15, g = lane >> 4, q = li >> 2, p = lane & 3;
  f32x4 a0[2] = {}, a1[2] = {};
  const unsigned wo_ = LDS_WKV + (8 * g + q) * 40 + 8 * p;
  s16x4 wr[4][4];
#define WKV_RD(ks) do { _Pragma("unroll") for (int e = 0; e < 4; ++e) wr[(ks) & 3][e] = ldstr(lds, wo_ + 32 * (ks) * 40 + (e >> 1) * 32 + (e & 1) * 160); } while (0)
  WKV_RD(0); WKV_RD(1); WKV_RD(2);
#pragma unroll
  for (int ks = 0; ks < 18; ++ks) {
    if (ks + 3 < 18) WKV_RD(ks + 3);
    SBAR();
#pragma unroll
    for (int nt = 0; nt < 2; ++nt) {
      const auto wfr = __builtin_shufflevector(wr[ks & 3][2 * nt], wr[ks & 3][2 * nt + 1], 0, 1, 2, 3, 4, 5, 6, 7);
      f32x4& a = (ks & 1) ? a1[nt] : a0[nt];
      if (ISV) a = __builtin_amdgcn_mfma_f32_16x16x32_bf16(__builtin_bit_cast(bf16x8, xf[ks]), __builtin_bit_cast(bf16x8, wfr), a, 0, 0, 0);
      else a = __builtin_amdgcn_mfma_f32_16x16x32_bf16(__builtin_bit_cast(bf16x8, wfr), __builtin_bit_cast(bf16x8, xf[ks]), a, 0, 0, 0);
    }
    SBAR();
  }
#undef WKV_RD
#pragma unroll
  for (int e = 0; e < 4; ++e) { acc[0][e] = a0[0][e] + a1[0][e]; acc[1][e] = a0[1][e] + a1[1][e]; }
}
DI void kv_wave(int b, int rb, int hg, const u16* x, char* ws, char* lds, unsigned lds0, int lane) {
  const int li = lane & 15, g = lane >> 4;
  const bool isv = hg >> 2;
  u32x4 xf[18];
  lds_wait<LPOLL>(lds, F_GF + 0, NI);
  unsigned xb[2];
  x_bases(lane, xb);
  xb[0] += LDS_CV; xb[1] += LDS_CV;
#pragma unroll
  for (int ks = 0; ks < 18; ++ks) xf[ks] = XFRAG(xb, ks);
  asm volatile("s_waitcnt lgkmcnt(0)" ::: "memory");
  FLAG_ADD(F_GD + 0);
  lds_wait<LPOLL>(lds, F_WKV, 5);
  STAMP(1, 2);
  f32x4 acc[2];
  if (isv) kv_products<true>(lds, lane, xf, acc); else kv_products<false>(lds, lane, xf, acc);
  STAMP(1, 4);
  const int kt = 2 * rb + ((hg & 3) >> 1), s = hg & 1, kh = kt >> 5, t = kt & 31;
  const __amdgpu_buffer_rsrc_t rs = rsrc(ws + WS_KV, 163840);
  if (!isv) {
    const int img = t ? ((t - 1) >> 2) * KV_SEG + kh * KV_HALF + ((t - 1) & 3) * KV_TILE : KV_HDR + kh * 1152;
    const float sc = 0.23570226039551584f * 1.4426950408889634f;
    const int key = 16 * s + li, e = (key & 0x13) | ((key & 4) << 1) | ((key & 8) >> 1);
    const unsigned d0 = cvtpk(acc[0][0] * sc, acc[0][1] * sc), d1 = cvtpk(acc[0][2] * sc, acc[0][3] * sc);
    const unsigned d2 = g ? 0u : cvtpk(acc[1][0] * sc, acc[1][1] * sc);
    __builtin_amdgcn_raw_buffer_store_b64(u32x2{d0, d1}, rs, img + (g >> 1) * 512 + e * 16 + 8 * (g & 1), 0, SC1);
    if (g == 0) __builtin_amdgcn_raw_buffer_store_b32(d2, rs, img + 1024 + e * 4, 0, SC1);
    float n2 = bflo(d0) * bflo(d0) + bfhi(d0) * bfhi(d0) + bflo(d1) * bflo(d1) + bfhi(d1) * bfhi(d1) + bflo(d2) * bflo(d2) + bfhi(d2) * bfhi(d2);
    n2 = wave_max(xrow16_sum(n2));
    if (lane == 0) __hip_atomic_store((gu32*)(ws + WS_KMAX) + 4 * rb + hg, __float_as_uint(n2), __ATOMIC_RELAXED, __HIP_MEMORY_SCOPE_AGENT);
  } else {
    const int img = (t >> 2) * KV_SEG + kh * KV_HALF + (t & 3) * KV_TILE + 1152 + (2 * s + (g >> 1)) * KV_VP + 8 * (g & 1);
    __builtin_amdgcn_raw_buffer_store_b64(u32x2{cvtpk(acc[0][0], acc[0][1]), cvtpk(acc[0][2], acc[0][3])}, rs, img + li * 16, 0, SC1);
    u32x2 v = {cvtpk(acc[1][0], acc[1][1]), cvtpk(acc[1][2], acc[1][3])};
    if (li == 2) v = u32x2{0x3f803f80u, 0x3f803f80u};
    if (li == 3) v = u32x2{0u, 0u};
    if (li < 4) __builtin_amdgcn_raw_buffer_store_b64(v, rs, img + (16 + li) * 16, 0, SC1);
  }
  const u64 tag = get_tag(lds);
  WAITV(0);
  if (lane < 8) tag_store(ws + WS_KVF + 2048 * lane + 8 * b, tag);
  STAMP(1, 3);
  __builtin_amdgcn_s_sleep(KV_DELAY);
  tags_wait<256, KV_POLL>(ws + WS_KVF + 2048 * (b & 7), tag, lane);
  flag_set(lds, F_KVOK, 1);
  if (b == 0 && lane == 0) tag_store(ws + WS_EPOCH, tag);
}

DI void ut_wave(int rb, int hg, const u16* u, char* ws, char* lds, int lane) {
  u32x4 v[9];
#pragma unroll
  for (int j = 0; j < 9; ++j) { const int ch = 64 * j + lane, row = ch / 9, c = ch - 9 * row; v[j] = *(const u32x4*)(u + (size_t)(64 * rb + row) * DM + 72 * hg + 8 * c); }
#pragma unroll
  for (int j = 0; j < 9; ++j) ldsw<u32x4>(lds, LDS_UA + (64 * j + lane) * 16, v[j]);
  const __amdgpu_buffer_rsrc_t rs = rsrc(ws + WS_UT + (size_t)72 * hg * (UT_ROW * 2), 72 * UT_ROW * 2);
  const int g = lane >> 4, li = lane & 15, q = li >> 2, p = lane & 3;
#pragma unroll
  for (int db = 0; db < 5; ++db)
#pragma unroll
    for (int e = 0; e < 2; ++e) {
      const unsigned ad = LDS_UA + (8 * (4 * e + g) + q) * 144 + 32 * db + 8 * p;
      const u32x4 t = __builtin_bit_cast(u32x4, __builtin_shufflevector(ldstr(lds, ad), ldstr(lds, ad + 4 * 144), 0, 1, 2, 3, 4, 5, 6, 7));
      if (db < 4 || li < 8) __builtin_amdgcn_raw_buffer_store_b128(t, rs, (16 * db + li) * (UT_ROW * 2) + 16 + 128 * rb + 16 * (4 * e + g), 0, SC1);
    }
  if (rb == 0 || rb == 31) {
#pragma unroll
    for (int j = 0; j < 2; ++j)
      if (64 * j + lane < 72) __builtin_amdgcn_raw_buffer_store_b128(u32x4{0, 0, 0, 0}, rs, (64 * j + lane) * (UT_ROW * 2) + (rb ? 16 + SEQ * 2 : 0), 0, SC1);
  }
  asm volatile("s_waitcnt lgkmcnt(0)" ::: "memory");
  FLAG_ADD(F_UDONE);
  const u64 tag = get_tag(lds);
  WAITV(0);
  if (lane == 0) tag_store(ws + WS_UF + 8 * (32 * hg + rb), tag);
}

DI f32x2 cmul(f32x2 a, f32x2 b) { return f32x2{__builtin_fmaf(a[0], b[0], -(a[1] * b[1])), __builtin_fmaf(a[0], b[1], a[1] * b[0])}; }
DI f32x2 cexp_l(float zr, float zi, float l) {
  const float mag = __builtin_amdgcn_exp2f((zr * l) * 1.4426950408889634f);
  const float rev = __builtin_amdgcn_fractf((zi * l) * 0.15915494309189535f);
  return f32x2{mag * __builtin_amdgcn_cosf(rev), mag * __builtin_amdgcn_sinf(rev)};
}
DI void gen_wave(int d, const u16* Are, const u16* Aim, const u16* Bre, const u16* Bim, const u16* Cre, const u16* Cim,
                 const u16* logdt, char* lds, int lane) {
  const int r = lane & 31, h = lane >> 5;
  u16 pr[2][6];
#pragma unroll
  for (int pass = 0; pass < 2; ++pass) {
    const int idx = d * NST + 64 * pass + lane;
    pr[pass][0] = Are[idx]; pr[pass][1] = Aim[idx]; pr[pass][2] = Bre[idx]; pr[pass][3] = Bim[idx]; pr[pass][4] = Cre[idx]; pr[pass][5] = Cim[idx];
  }
  const float dt = rbf(expf(bf2f(logdt[d])));
  const float fm = (float)(8 * r);
  f32x16 acc = {};
#pragma unroll 1
  for (int pass = 0; pass < 2; ++pass) {
    const u16 p0 = pass ? pr[1][0] : pr[0][0], p1 = pass ? pr[1][1] : pr[0][1], p2_ = pass ? pr[1][2] : pr[0][2];
    const u16 p3_ = pass ? pr[1][3] : pr[0][3], p4_ = pass ? pr[1][4] : pr[0][4], p5 = pass ? pr[1][5] : pr[0][5];
    const float zr = dt * bf2f(p0), zi = dt * bf2f(p1);
    const float dBr = dt * bf2f(p2_), dBi = dt * bf2f(p3_);
    const float cr = bf2f(p4_), ci = bf2f(p5);
    const f32x2 c = {cr * dBr - ci * dBi, cr * dBi + ci * dBr};
    ldsw<f32x2>(lds, GEN_P + 8 * lane, f32x2{zr, -zi});
    const f32x2 w1 = cexp_l(zr, zi, 1.f), w256 = cexp_l(zr, zi, 256.f);
#define PUTY(col, v) ldsw<unsigned>(lds, (col) * GEN_PITCH + 4 * lane, cvtpk((v)[0], (v)[1]))
    f32x2 y[8];
    y[0] = c;
#pragma unroll
    for (int i = 1; i < 8; ++i) y[i] = cmul(y[i - 1], w1);
#pragma unroll
    for (int i = 0; i < 8; ++i) PUTY(i, y[i]);
#pragma unroll
    for (int i = 0; i < 4; ++i) { const f32x2 t = cmul(y[2 * i], w256); PUTY(8 + i, t); }
    const f32x2 p2 = cmul(w256, w256), p3 = cmul(p2, w256), p4 = cmul(p2, p2);
    { f32x2 t = cmul(y[0], p2); PUTY(12, t); t = cmul(y[4], p2); PUTY(13, t); }
    { f32x2 t = cmul(y[0], p3); PUTY(14, t); t = cmul(y[4], p3); PUTY(15, t); }
    { const f32x2 t4 = cmul(y[0], p4); PUTY(16, t4);
      f32x2 t = cmul(t4, w256); PUTY(17, t); t = cmul(t4, p2); PUTY(18, t); t = cmul(t4, p3); PUTY(19, t); t = cmul(t4, p4); PUTY(20, t); }
#undef PUTY
    asm volatile("" ::: "memory");
    const int yr = r <= 20 ? r : r - 16;
#pragma unroll 2
    for (int ks = 0; ks < 8; ++ks) {
      const f32x4 z0 = ldsr<f32x4>(lds, GEN_P + 64 * ks + 32 * h), z1 = ldsr<f32x4>(lds, GEN_P + 64 * ks + 32 * h + 16);
      const u32x4 fb = ldsr<u32x4>(lds, yr * GEN_PITCH + 32 * ks + 16 * h);
      const f32x2 a0 = cexp_l(z0[0], z0[1], fm), a1 = cexp_l(z0[2], z0[3], fm), a2 = cexp_l(z1[0], z1[1], fm), a3 = cexp_l(z1[2], z1[3], fm);
      const u32x4 fa = {cvtpk(a0[0], a0[1]), cvtpk(a1[0], a1[1]), cvtpk(a2[0], a2[1]), cvtpk(a3[0], a3[1])};
      acc = MFMA16(fa, fb, acc);
    }
    asm volatile("" ::: "memory");
  }
  const int c = r;
  const int hi = c < 8 ? c : c < 12 ? 256 + 2 * (c - 8) : c < 16 ? 512 + 256 * ((c - 12) >> 1) + 4 * ((c - 12) & 1) : 1024 + 256 * (c - 16);
  if (c < 20) {
#pragma unroll
    for (int i = 0; i < 16; ++i) ldsw<u16>(lds, 2 * (8 * crow(i, h) + hi), (u16)cvtpk(acc[i], acc[i]));
  } else if (c == 20 && h == 0) {
    ldsw<u16>(lds, 2 * 2048, (u16)cvtpk(acc[0], acc[0]));
  }
  asm volatile("" ::: "memory");
}
DI void build_copies(const char* kc, char* cl, int lane) {
  auto rd = [&](int l) -> unsigned { return ldsr<u16>(kc, 2 * l); };
  auto pk = [](unsigned lo, unsigned hi) -> unsigned { return lo | (hi << 16); };
  auto put2 = [&](int par, int y, unsigned v0, unsigned v1) {
    ldsw<u32x2>(cl, par * CV_K + 4 * y, u32x2{v0, v1});
    ldsw<unsigned>(cl, (par + 2) * CV_K + 4 * y - 4, v0); ldsw<unsigned>(cl, (par + 2) * CV_K + 4 * y, v1);
  };
  const int L = lane;
  {
    const unsigned e0 = rd(4 * L), e1 = rd(4 * L + 1), e2 = rd(4 * L + 2), e3 = rd(4 * L + 3);
    const unsigned pv = L ? rd(4 * L - 1) : 0u;
    put2(0, 1028 - 2 * L, pk(e3, e2), pk(e1, e0));
    put2(1, 1028 - 2 * L, pk(e2, e1), pk(e0, pv));
  }
  {
    const int j0 = 256 + 4 * L;
    const unsigned a = rd(j0), b = rd(j0 + 2), c = rd(j0 + 4), pv = L ? a : rd(255);
    put2(0, 1028 - j0 / 2, pk(c, b), pk(a, a));
    put2(1, 1028 - j0 / 2, pk(b, a), pk(a, pv));
  }
  {
    const int j0 = 512 + 8 * L, y = 1026 - j0 / 2;
    const unsigned a = rd(j0), b = rd(j0 + 4), c = rd(j0 + 8);
    const unsigned aa = pk(a, a), bb = pk(b, b), cc = pk(c, c);
    put2(0, y, cc, bb);        put2(0, y + 2, pk(b, a), aa);
    put2(1, y, pk(c, b), bb);  put2(1, y + 2, aa, aa);
  }
  {
    const int j0 = 1024 + 16 * L, y = 1022 - j0 / 2;
    const unsigned a = rd(j0), b = rd(j0 + 8), c = rd(j0 + 16);
    const unsigned aa = pk(a, a), bb = pk(b, b), cc = pk(c, c);
    put2(0, y, cc, cc);        put2(0, y + 2, bb, bb);  put2(0, y + 4, bb, pk(b, a));  put2(0, y + 6, aa, aa);
    put2(1, y, cc, pk(c, b));  put2(1, y + 2, bb, bb);  put2(1, y + 4, bb, aa);        put2(1, y + 6, aa, aa);
    if (L == 63) {
      ldsw<unsigned>(cl, CV_K + 4 * 5, pk(0u, c)); ldsw<unsigned>(cl, 3 * CV_K + 4 * 4, pk(0u, c));
    }
  }
  if (L < 6) { ldsw<unsigned>(cl, 4 * L, 0u); if (L) ldsw<unsigned>(cl, 2 * CV_K + 4 * L - 4, 0u); }
  if (L < 5) { ldsw<unsigned>(cl, CV_K + 4 * L, 0u); if (L) ldsw<unsigned>(cl, 3 * CV_K + 4 * L - 4, 0u); }
  if (L < 42) {
    ldsw<unsigned>(cl, 4 * (1030 + L), 0u); ldsw<unsigned>(cl, CV_K + 4 * (1030 + L), 0u);
    ldsw<unsigned>(cl, 2 * CV_K + 4 * (1029 + L), 0u); ldsw<unsigned>(cl, 3 * CV_K + 4 * (1029 + L), 0u);
  }
}
template <int SW> DI void u_fill(int d, const char* ws, char* cl, unsigned cl0, int lane) {
  const char* ur = ws + WS_UT + (size_t)d * (UT_ROW * 2);
#pragma unroll
  for (int item = SW; item < 10; item += 4) {
    const int bb = item / 5, part = item - 5 * bb, sg = 136 + part * 64 + lane, qc = sg - sg / 9, s0 = 8 * qc - 976 + 4 * bb;
    if (sg <= 424) glds16c(ur, sg % 9 == 8 ? 0u : 2 * (8 + s0), cl0 + CV_U + bb * CV_UP + (136 + part * 64) * 16);
  }
  if (SW < 2) {
#pragma unroll
    for (int j = 0; j < 3; ++j)
      if (64 * j + lane < 136) ldsw<u32x4>(cl, CV_U + SW * CV_UP + (64 * j + lane) * 16, u32x4{0, 0, 0, 0});
  }
}
template <int SW> DI void u_fill3(int d0, int dx, bool third, const char* ws, char* lds, unsigned lds0, int lane) {
  u_fill<SW>(d0, ws, lds + LDS_CV, lds0 + LDS_CV, lane);
  u_fill<(SW + 2) & 3>(d0 + 1, ws, lds + LDS_CV + CV_CH, lds0 + LDS_CV + CV_CH, lane);
  if (third) u_fill<(SW + 1) & 3>(dx, ws, lds + LDS_CV + 2 * CV_CH, lds0 + LDS_CV + 2 * CV_CH, lane);
}

struct KVAddr { unsigned k, kx, v; };
#define LDK(dst, dstx, t_off) do { dst = ldsr<u32x4>(lds, ad.k + (t_off)); dstx[0] = ldsr<unsigned>(lds, ad.kx + (t_off)); } while (0)
#define LDV(d0, d1, t_off) do { d0 = ldsr<u32x4>(lds, ad.v + (t_off)); d1 = ldsr<u32x4>(lds, ad.v + 2 * KV_VP + (t_off)); } while (0)
#define EX2(x) __builtin_amdgcn_exp2f(x)
#define GRP(MF, S, P, i) do { MF; SBAR(); \
    { const float e0 = EX2(S[4 * (i)]), e1 = EX2(S[4 * (i) + 1]), e2 = EX2(S[4 * (i) + 2]), e3 = EX2(S[4 * (i) + 3]); \
      unsigned t0_ = cvtpk(e0, e1), t1_ = cvtpk(e2, e3); PIN(t0_); PIN(t1_); P[2 * (i)] = t0_; P[2 * (i) + 1] = t1_; } \
    SBAR(); } while (0)
typedef unsigned u32x8 __attribute__((ext_vector_type(8)));
DI u32x4 lo4(const u32x8& p) { return __builtin_shufflevector(p, p, 0, 1, 2, 3); }
DI u32x4 hi4(const u32x8& p) { return __builtin_shufflevector(p, p, 4, 5, 6, 7); }

#define STEP(Kc, Kcx, Kn, Knx, Vp0, Vp1, Vc0, Vc1, off) do { \
    LDK(Kn, Knx, (off)); LDV(Vc0, Vc1, (off)); SBAR(); \
    GRP(sb = MFMA16(Kc, qbm, zero16), sa, pa, 0); \
    GRP(ob = MFMA16(Vp0, lo4(pb), ob), sa, pa, 1); \
    GRP(sb = MFMA8(Kcx, qbx, sb), sa, pa, 2); \
    GRP(ob = MFMA16(Vp1, hi4(pb), ob), sa, pa, 3); \
    GRP(sa = MFMA16(Kn, qam, zero16), sb, pb, 0); \
    GRP(oa = MFMA16(Vc0, lo4(pa), oa), sb, pb, 1); \
    GRP(sa = MFMA8(Knx, qax, sa), sb, pb, 2); \
    GRP(oa = MFMA16(Vc1, hi4(pa), oa), sb, pb, 3); \
  } while (0)

DI void slow_tile(const u32x4& kf, const u32x2& kx, const u32x4& v0, const u32x4& v1, const u32x4& qm, const u32x2& qx, f32x16& o, float& m) {
  f32x16 s = {};
  s = MFMA16(kf, qm, s);
  s = MFMA8(kx, qx, s);
  float mx = s[0];
#pragma unroll
  for (int i = 1; i < 16; ++i) mx = fmaxf(mx, s[i]);
  mx = half_max(mx);
  const float mn = fmaxf(m, mx);
  const float alpha = EX2(m - mn);
  m = mn;
#pragma unroll
  for (int i = 0; i < 16; ++i) o[i] *= alpha;
  u32x8 p;
#pragma unroll
  for (int i = 0; i < 8; ++i) p[i] = cvtpk(EX2(s[2 * i] - mn), EX2(s[2 * i + 1] - mn));
  o = MFMA16(v0, lo4(p), o);
  o = MFMA16(v1, hi4(p), o);
}

template <bool TWO, bool SKIP0> DI void conv_steps(const char* lds, unsigned pa0, unsigned pb0, unsigned pa1, unsigned pb1, f32x16& c0, f32x16& c1) {
  __builtin_amdgcn_s_setprio(CONV_PRIO);
  u32x4 a0[2], b0[2], a1[2], b1[2];
#define CLD(i, n) do { a0[i] = ldsr<u32x4>(lds, pa0 + 32 * (n)); b0[i] = ldsr<u32x4>(lds, pb0 + 32 * (n) + 16 * ((n) >> 2)); \
    if (TWO) { a1[i] = ldsr<u32x4>(lds, pa1 + 32 * (n)); b1[i] = ldsr<u32x4>(lds, pb1 + 32 * (n) + 16 * ((n) >> 2)); } } while (0)
#define CMM(i) do { c0 = MFMA16(a0[i], b0[i], c0); if (TWO) c1 = MFMA16(a1[i], b1[i], c1); } while (0)
  CLD(0, 0); CLD(1, 1);
  c0 = MFMA16(a0[0], b0[0], c0);
  if (TWO && !SKIP0) c1 = MFMA16(a1[0], b1[0], c1);
  SBAR();
#pragma unroll 1
  for (int n4 = 0; n4 < 16; ++n4) {
    const unsigned oa = 128 * n4, ob = 144 * n4;
#define CLD4(i, k) do { a0[i] = ldsr<u32x4>(lds, pa0 + oa + 32 * (k)); b0[i] = ldsr<u32x4>(lds, pb0 + ob + 32 * (k) + 16 * ((k) >> 2)); \
    if (TWO) { a1[i] = ldsr<u32x4>(lds, pa1 + oa + 32 * (k)); b1[i] = ldsr<u32x4>(lds, pb1 + ob + 32 * (k) + 16 * ((k) >> 2)); } } while (0)
    CLD4(0, 2); SBAR(); CMM(1); SBAR();
    CLD4(1, 3); SBAR(); CMM(0); SBAR();
    CLD4(0, 4); SBAR(); CMM(1); SBAR();
    CLD4(1, 5); SBAR(); CMM(0); SBAR();
#undef CLD4
  }
#undef CLD
#undef CMM
  __builtin_amdgcn_s_setprio(3);
}
DI void conv_out(const f32x16& c, int d, int tl, char* ws, char* scratch, int lane) {
  const int r = lane & 31, h = lane >> 5;
#pragma unroll
  for (int g = 0; g < 4; ++g)
    ldsw<u32x2>(scratch, 2 * (16 * g + 8 * h + 4 * (r & 1) + 64 * (r >> 1)), u32x2{cvtpk(c[4 * g], c[4 * g + 1]), cvtpk(c[4 * g + 2], c[4 * g + 3])});
  const __amdgpu_buffer_rsrc_t rsY = __builtin_amdgcn_make_buffer_rsrc((void*)(ws + WS_YT + (size_t)d * (SEQ * 2) + 2048 * tl), 0, 2048, kRsrc3);
#pragma unroll
  for (int j = 0; j < 2; ++j) __builtin_amdgcn_raw_buffer_store_b128(ldsr<u32x4>(scratch, 1024 * j + 16 * lane), rsY, 1024 * j + 16 * lane, 0, 16);
}

DI void skip_tile(int rb, int hg, const u16* u, const u16* dskip, char* ws, u16* out, char* lds, int lane) {
  u32x4 y[9], uu[9];
#pragma unroll
  for (int j = 0; j < 9; ++j) {
    const int ch = 64 * j + lane, row = ch / 9, c = ch - 9 * row;
    uu[j] = *(const u32x4*)(u + (size_t)(64 * rb + row) * DM + 72 * hg + 8 * c);
  }
  tags_wait<32, 4>(ws + WS_YF + 8 * 32 * hg, get_tag(lds), lane);
  const __amdgpu_buffer_rsrc_t rsY = __builtin_amdgcn_make_buffer_rsrc((void*)(ws + WS_YT + (size_t)72 * hg * (SEQ * 2)), 0, 72 * SEQ * 2, kRsrc3);
#pragma unroll
  for (int j = 0; j < 9; ++j) {
    const int ch = 64 * j + lane, row = ch >> 3, c = ch & 7;
    y[j] = __builtin_amdgcn_raw_buffer_load_b128(rsY, row * (SEQ * 2) + 128 * rb + 16 * c, 0, 16);
  }
#pragma unroll
  for (int j = 0; j < 9; ++j) {
    const int ch = 64 * j + lane, row = ch >> 3, c = ch & 7;
    ldsw<u32x4>(lds, LDS_FA + row * 128 + 16 * (c ^ (wswz(row) << 1)), y[j]);
  }
  const int g = lane >> 4, li = lane & 15, q = li >> 2, p = lane & 3;
#pragma unroll
  for (int j = 0; j < 9; ++j) {
    const int R0 = 8 * j + q, R1 = R0 + 4;
    const s16x4 a0 = ldstr(lds, LDS_FA + R0 * 128 + ((g ^ wswz(R0)) * 32) + 8 * p), a1 = ldstr(lds, LDS_FA + R1 * 128 + ((g ^ wswz(R1)) * 32) + 8 * p);
    ldsw<u32x4>(lds, LDS_FB + (16 * g + li) * 144 + 16 * j, __builtin_bit_cast(u32x4, __builtin_shufflevector(a0, a1, 0, 1, 2, 3, 4, 5, 6, 7)));
  }
#pragma unroll
  for (int j = 0; j < 9; ++j) {
    const int ch = 64 * j + lane, row = ch / 9, c = ch - 9 * row;
    const u32x4 yy = ldsr<u32x4>(lds, LDS_FB + ch * 16), dd = *(const u32x4*)(dskip + 72 * hg + 8 * c);
    u32x4 o;
#pragma unroll
    for (int k = 0; k < 4; ++k) {
      const unsigned sk = cvtpk(bflo(uu[j][k]) * bflo(dd[k]), bfhi(uu[j][k]) * bfhi(dd[k]));
      o[k] = cvtpk(bflo(yy[k]) + bflo(sk), bfhi(yy[k]) + bfhi(sk));
    }
    *(u32x4*)(out + (size_t)(64 * rb + row) * (2 * DM) + DM + 72 * hg + 8 * c) = o;
  }
}

DI void tail_part(const f32x16& oa, const f32x16& ob, float ma, float mb, int w, int rb, int hg, const u16* u, const u16* dskip, char* ws, u16* out, char* lds) {
  int lane;
  asm volatile("v_mbcnt_lo_u32_b32 %0, -1, 0\n\tv_mbcnt_hi_u32_b32 %0, -1, %0" : "=v"(lane));
  const int r = lane & 31, h = lane >> 5, pt = w & 1, hp = (w >> 1) & 1, kh = w >> 2;

  {
    const f32x16& give = kh ? oa : ob;
    const float gm = kh ? ma : mb;
    char* own = lds + LDS_CMB + w * 3328;
#pragma unroll
    for (int g = 0; g < 3; ++g) ldsw<f32x4>(own, g * 1024 + lane * 16, f32x4{give[4 * g], give[4 * g + 1], give[4 * g + 2], give[4 * g + 3]});
    ldsw<float>(own, 3072 + lane * 4, gm);
    BAR();
    const char* oth = lds + LDS_CMB + (w ^ 4) * 3328;
    f32x16 keep = kh ? ob : oa;
    const float m1 = kh ? mb : ma, m2 = ldsr<float>(oth, 3072 + lane * 4);
    const float mm = fmaxf(m1, m2), f1 = EX2(m1 - mm), f2 = EX2(m2 - mm);
    float o[12];
#pragma unroll
    for (int g = 0; g < 3; ++g) {
      const f32x4 t = ldsr<f32x4>(oth, g * 1024 + lane * 16);
#pragma unroll
      for (int e = 0; e < 4; ++e) o[4 * g + e] = keep[4 * g + e] * f1 + t[e] * f2;
    }
    const float inv = 1.0f / half_lo(o[10]);
    const unsigned ot = LDS_OT + (32 * pt + r) * 144 + (2 * hp + kh) * 36;
    ldsw<unsigned>(lds, ot + 8 * h, cvtpk(o[0] * inv, o[1] * inv));
    ldsw<unsigned>(lds, ot + 8 * h + 4, cvtpk(o[2] * inv, o[3] * inv));
    ldsw<unsigned>(lds, ot + 16 + 8 * h, cvtpk(o[4] * inv, o[5] * inv));
    ldsw<unsigned>(lds, ot + 16 + 8 * h + 4, cvtpk(o[6] * inv, o[7] * inv));
    if (!h) ldsw<unsigned>(lds, ot + 32, cvtpk(o[8] * inv, o[9] * inv));
  }
  BAR();
  const __amdgpu_buffer_rsrc_t rsO = __builtin_amdgcn_make_buffer_rsrc((void*)(ws + WS_O + (size_t)64 * rb * (DM * 2)), 0, 64 * DM * 2, kRsrc3);
  {
    const int c0 = 64 * w + lane, row = c0 / 9, c = c0 - 9 * row;
    __builtin_amdgcn_raw_buffer_store_b128(ldsr<u32x4>(lds, LDS_OT + c0 * 16), rsO, row * (DM * 2) + hg * 144 + c * 16, 0, 16);
    if (w == 0) {
      const int c1 = 512 + lane, row1 = c1 / 9, cc = c1 - 9 * row1;
      __builtin_amdgcn_raw_buffer_store_b128(ldsr<u32x4>(lds, LDS_OT + c1 * 16), rsO, row1 * (DM * 2) + hg * 144 + cc * 16, 0, 16);
    }
  }
  asm volatile("s_waitcnt vmcnt(0)" ::: "memory");
  BAR();
  STAMP(1, 9);
  if (w == 0) {
    const u64 tag = get_tag(lds);
    if (lane == 0) tag_store(ws + WS_OF + 8 * (8 * rb + hg), tag);
    tags_wait<8, 1>(ws + WS_OF + 64 * rb, tag, lane);
  }
  BAR();
  STAMP(1, 10);
  if (w == 4) skip_tile(rb, hg, u, dskip, ws, out, lds, lane);
  if (w >= 4) return;

  const int li = lane & 15, g = lane >> 4, q = li >> 2, p = lane & 3;
  u32x4 xf[12];
#define XLD(ks) __builtin_amdgcn_raw_buffer_load_b128(rsO, (16 * w + li) * (DM * 2) + 64 * (ks) + 16 * g, 0, 16)
#pragma unroll
  for (int ks = 0; ks < 12; ++ks) xf[ks] = XLD(ks);
  STAMP(1, 11);
  f32x4 acc[5] = {};
  const unsigned wb = LDS_CV + (8 * g + q) * 144 + 8 * p;
  s16x4 wa[2][10];
#define WRD(buf, ks) do { _Pragma("unroll") for (int nt = 0; nt < 5; ++nt) { \
    wa[buf][2 * nt] = ldstr(lds, wb + 32 * (ks) * 144 + 32 * nt); wa[buf][2 * nt + 1] = ldstr(lds, wb + (32 * (ks) + 4) * 144 + 32 * nt); } } while (0)
  WRD(0, 0);
#pragma unroll
  for (int ks = 0; ks < 18; ++ks) {
    if (ks + 1 < 18) WRD((ks + 1) & 1, ks + 1);
    SBAR();
#pragma unroll
    for (int nt = 0; nt < 5; ++nt) {
      const auto a = __builtin_shufflevector(wa[ks & 1][2 * nt], wa[ks & 1][2 * nt + 1], 0, 1, 2, 3, 4, 5, 6, 7);
      acc[nt] = __builtin_amdgcn_mfma_f32_16x16x32_bf16(__builtin_bit_cast(bf16x8, a), __builtin_bit_cast(bf16x8, xf[ks % 12]), acc[nt], 0, 0, 0);
    }
    SBAR();
    if (ks < 6) xf[ks] = XLD(ks + 12);
    SBAR();
  }
#undef WRD
#undef XLD
  STAMP(1, 12);
  u16* orow = out + (size_t)(64 * rb + 16 * w + li) * (2 * DM) + 72 * hg + 4 * g;
#pragma unroll
  for (int nt = 0; nt < 5; ++nt)
    if (nt < 4 || lane < 32) *(u32x2*)(orow + 16 * nt) = u32x2{cvtpk(acc[nt][0], acc[nt][1]), cvtpk(acc[nt][2], acc[nt][3])};
  STAMP(1, 15);
}

DI void seg0_share(int i, const char* ws, char* lds, unsigned lds0, int lane) {
  const char* kvg = ws + WS_KV;
  lds_wait<LPOLL>(lds, F_KVOK, 1);
#pragma unroll
  for (int j = 0; j < 4; ++j) {
    const int p = i + 6 * j;
    if (p < 19) glds16c(kvg + 1024 * p, 16 * lane, lds0 + 1024 * p);
    else if (p < 22) { if (p < 21 || lane < 16) glds16c(kvg + KV_HDR + 1024 * (p - 19), 16 * lane, lds0 + LDS_K0 + 1024 * (p - 19)); }
    else if (p == 22) { if (lane < 32) glds16c(ws + WS_KMAX, 16 * lane, lds0 + LDS_K0 + 3072); }
  }
  WAITV(0);
  FLAG_ADD(F_SEG0);
}
DI void l2_touch(const char* base, int line0, int nlines, unsigned lds0, int lane) {
  unsigned sv;
  if (line0 + lane < nlines)
    asm volatile("s_mov_b32 %0, m0\n\ts_mov_b32 m0, %3\n\ts_nop 0\n\tglobal_load_lds_dword %1, %2\n\ts_mov_b32 m0, %0"
                 : "=&s"(sv) : "v"(128 * (line0 + lane)), "s"(base), "s"(lds0 + LDS_GEN + 3 * GEN_AREA) : "memory");
}

__global__ void __launch_bounds__(1024)
fused_kernel(const u16* x, const u16* wq, const u16* wk, const u16* wv, const u16* wo, const u16* u, const u16* Are, const u16* Aim,
             const u16* Bre, const u16* Bim, const u16* Cre, const u16* Cim, const u16* logdt, const u16* dskip, char* ws, u16* out) {
  extern __shared__ __attribute__((aligned(16))) char lds[];
  const unsigned lds0 = (unsigned)(uintptr_t)lds;
  const int b = BID, lane = TID & 63, w = rfl(TID >> 6);
  const int rb = (b & 7) + 8 * (b >> 6), hg = (b >> 3) & 7;
  STAMP(1, 0);
  if (TID < 64) ((LDSP unsigned*)((LDSP char*)lds + LDS_FLAG))[TID] = 0u;
  BAR();
  const char* kvg = ws + WS_KV;
  if (w >= 14) {
    __builtin_amdgcn_s_setprio(2);
    if (w == 14) q_loader<5>(x, wq, rb, hg, lds, lds0, lane); else q_loader<6>(x, wq, rb, hg, lds, lds0, lane);
    seg0_share(w - 10, ws, lds, lds0, lane);
    return;
  }
  if (w == 13) {
    __builtin_amdgcn_s_setprio(2);
    q_loader<4>(x, wq, rb, hg, lds, lds0, lane);
    ut_wave(rb, hg, u, ws, lds, lane);
    STAMP(1, 15);
    return;
  }
  if (w == 12) {
    __builtin_amdgcn_s_setprio(3);
    const u64 tag = __hip_atomic_load((gu64*)(ws + WS_EPOCH), __ATOMIC_RELAXED, __HIP_MEMORY_SCOPE_AGENT) + 1;
    flag_set(lds, F_TAG, (unsigned)tag); flag_set(lds, F_TAG + 1, (unsigned)(tag >> 32)); flag_set(lds, F_TAGOK, 1);
    q_loader<3>(x, wq, rb, hg, lds, lds0, lane);
    STAMP(1, 3);
    seg0_share(3, ws, lds, lds0, lane);
    STAMP(1, 4);
    auto issue = [&](int s) {
#pragma unroll
      for (int j = 0; j < 19; ++j) glds16c(kvg + s * KV_SEG + 1024 * j, 16 * lane, lds0 + (s % 3) * KV_SEG + 1024 * j);
    };
    lds_wait<1>(lds, F_UDONE, 1);
    issue(1);
    lds_wait<1>(lds, F_QRD, 8);
    issue(2);
    WAITV(19);
    flag_set(lds, F_FULL, 2);
    WAITV(0);
    flag_set(lds, F_FULL, 3);
#pragma unroll 1
    for (int s = 3; s < 8; ++s) {
      done_wait<4>(lds, s - 2, lane);
      issue(s);
      WAITV(0);
      flag_set(lds, F_FULL, s + 1);
    }
    STAMP(1, 15);
    return;
  }
  if (w >= 8) {
    const int sw = w - 8, i = b >> 3, xc = b & 7;
    const int d0 = 72 * xc + 2 * i, dx = 72 * xc + 64 + (i >> 2), third = (i & 3) < 2;
    char* area = lds + LDS_GEN + (sw - 1) * GEN_AREA;
    if (sw == 0) {
      __builtin_amdgcn_s_setprio(3);
      kv_wave(b, rb, hg, x, ws, lds, lds0, lane);
      STAMP(1, 5);
    } else if (sw < 3 || third) {
      gen_wave(sw < 3 ? d0 + sw - 1 : dx, Are, Aim, Bre, Bim, Cre, Cim, logdt, area, lane);
      STAMP(1, 3);
    }
    __builtin_amdgcn_s_setprio(3);
    lds_wait<2>(lds, F_QDONE, 5);
    if (sw != 0 && (sw < 3 || third)) build_copies(area, lds + LDS_CV + (sw - 1) * CV_CH, lane);
    asm volatile("s_waitcnt lgkmcnt(0)" ::: "memory");
    FLAG_ADD(F_KCOP);
    tags_wait<32, 2>(ws + WS_UF + 8 * 32 * xc, get_tag(lds), lane);
    lds_wait<2>(lds, F_KCOP, 4);
    if (sw == 0) u_fill3<0>(d0, dx, third, ws, lds, lds0, lane);
    else if (sw == 1) u_fill3<1>(d0, dx, third, ws, lds, lds0, lane);
    else if (sw == 2) u_fill3<2>(d0, dx, third, ws, lds, lds0, lane);
    else u_fill3<3>(d0, dx, third, ws, lds, lds0, lane);
    asm volatile("s_waitcnt vmcnt(0) lgkmcnt(0)" ::: "memory");
    FLAG_ADD(F_CFILL);
    STAMP(1, 6);
    {
      const int r = lane & 31, h = lane >> 5;
      const unsigned kb = LDS_CV + (3 - (r & 3)) * CV_K + 2 * (8 * h - 8 * (r >> 2));
      const unsigned ub = LDS_CV + CV_U + (r & 1) * CV_UP + 144 * (r >> 1) + 16 * h;
      const unsigned A1 = kb + 2 * 1080, A2 = kb + 2 * 56, B1 = ub, B3 = ub + 2304;
      char* scr = lds + LDS_SCR + sw * 2048;
      f32x16 c0 = {}, c1 = {};
      lds_wait<4>(lds, F_CFILL, 4);
      if (sw == 2) {
        conv_steps<true, false>(lds, A1, B1, A1 + CV_CH, B1 + CV_CH, c0, c1);
        conv_out(c0, d0, 0, ws, scr, lane);
        conv_out(c1, d0 + 1, 0, ws, scr, lane);
      } else if (sw < 2 || (i & 3) == 0) {
        const unsigned so = (sw < 2 ? sw : 2) * CV_CH;
        conv_steps<true, true>(lds, A2 + so, B1 + so, A1 + so, B3 + so, c0, c1);
#pragma unroll
        for (int k = 0; k < 16; ++k) c0[k] += c1[k];
        conv_out(c0, sw < 2 ? d0 + sw : dx, 1, ws, scr, lane);
      } else if (third) {
        conv_steps<false, false>(lds, A1 + 2 * CV_CH, B1 + 2 * CV_CH, 0, 0, c0, c1);
        conv_out(c0, dx, 0, ws, scr, lane);
      }
      STAMP(1, 7);
    }
    asm volatile("s_waitcnt vmcnt(0) lgkmcnt(0)" ::: "memory");
    unsigned arrived = 0;
    if (lane == 0) arrived = __hip_atomic_fetch_add((LDSP unsigned*)((LDSP char*)lds + LDS_FLAG) + F_CDONE, 1u, __ATOMIC_RELAXED, __HIP_MEMORY_SCOPE_WORKGROUP);
    if (rfl(arrived) == 3) { const u64 tag = get_tag(lds); if (lane == 0) tag_store(ws + WS_YF + 8 * (32 * xc + i), tag); }
    lds_wait<4>(lds, F_CDONE, 4);
    {
      unsigned vo[3];
#pragma unroll
      for (int k = 0; k < 3; ++k) { const int ch = 64 * (sw + 4 * k) + lane, row = ch / 9, c = ch - 9 * row; vo[k] = row * (DM * 2) + c * 16; }
      const char* wb = (const char*)(wo + 72 * hg);
#pragma unroll 1
      for (int a = 0; a < 9; ++a) {
#pragma unroll
        for (int k = 0; k < 3; ++k)
          if (k < 2 || sw == 0) glds16s(wb + (size_t)a * 64 * (DM * 2), vo[k], lds0 + LDS_CV + 1024 * (9 * a + sw + 4 * k));
      }
    }
    STAMP(1, 8);
    WAITV(0);
    STAMP(1, 15);
    return;
  }
  if (w < 5) {
    q_compute(wk, wv, hg, lds, w, lane, ws);
  } else {
    if (w == 5) q_loader<0>(x, wq, rb, hg, lds, lds0, lane);
    else if (w == 6) q_loader<1>(x, wq, rb, hg, lds, lds0, lane);
    else q_loader<2>(x, wq, rb, hg, lds, lds0, lane);
    STAMP(1, 6);
    seg0_share(w - 5, ws, lds, lds0, lane);
  }
  STAMP(1, 3);
  const int r = lane & 31, h = lane >> 5;
  const int pt = w & 1, hp = (w >> 1) & 1, kh = w >> 2;

  lds_wait<LPOLL>(lds, F_QDONE, 5);
  const unsigned qo = LDS_QT + (32 * pt + r) * 192 + 2 * hp * 48;
  const u32x4 qam = ldsr<u32x4>(lds, qo + 16 * h), qbm = ldsr<u32x4>(lds, qo + 48 + 16 * h);
  const unsigned qa16 = ldsr<unsigned>(lds, qo + 32), qb16 = ldsr<unsigned>(lds, qo + 48 + 32);
  lds_wait<LPOLL>(lds, F_SEG0, 6);
  u32x4 kA = ldsr<u32x4>(lds, LDS_K0 + kh * 1152 + h * 512 + r * 16);
  u32x2 kAx = {ldsr<unsigned>(lds, LDS_K0 + kh * 1152 + 1024 + r * 4), 0x3f80u};
  const float km0 = ldsr<float>(lds, LDS_K0 + 3072 + 4 * lane), km1 = ldsr<float>(lds, LDS_K0 + 3072 + 256 + 4 * lane);
  asm volatile("s_waitcnt lgkmcnt(0)" ::: "memory");
  FLAG_ADD(F_QRD);
  const float kmax2 = wave_max(fmaxf(km0, km1));
  auto sq = [](unsigned u) { return bflo(u) * bflo(u) + bfhi(u) * bfhi(u); };
  const float na = half_sum(sq(qam[0]) + sq(qam[1]) + sq(qam[2]) + sq(qam[3]) + (h ? 0.f : sq(qa16)));
  const float nb = half_sum(sq(qbm[0]) + sq(qbm[1]) + sq(qbm[2]) + sq(qbm[3]) + (h ? 0.f : sq(qb16)));
  const float ba = rbf(__builtin_amdgcn_sqrtf(na * kmax2) * 1.01f + 1e-6f), bb = rbf(__builtin_amdgcn_sqrtf(nb * kmax2) * 1.01f + 1e-6f);
  const bool fast = __builtin_amdgcn_ballot_w64(!(ba <= FAST_BOUND && bb <= FAST_BOUND)) == 0;

  KVAddr ad;
  {
    const int vr = r <= 18 ? r : 19;
    const unsigned tb = kh * KV_HALF;
    ad.k = tb + h * 512 + r * 16; ad.kx = tb + 1024 + r * 4; ad.v = tb + 1152 + h * KV_VP + vr * 16;
  }
  f32x16 oa = {}, ob = {};
  float ma, mb;
  if (fast) {
    ma = 0.f; mb = 0.f;
    u32x2 qax = {h ? 0u : qa16, h ? 0u : (cvtpk(-ba, 0.f))}, qbx = {h ? 0u : qb16, h ? 0u : (cvtpk(-bb, 0.f))};
    u32x4 kB, vA0, vA1, vB0, vB1;
    u32x2 kBx = {0u, 0x3f80u};
    f32x16 zero16 = {}, sa, sb;
    u32x8 pa, pb = {};
    sa = MFMA16(kA, qam, zero16); sa = MFMA8(kAx, qax, sa);
    STAMP(1, 1);
    STAMP(1, 2);
    LDV(vB0, vB1, 0);
    unsigned off = 0;
#pragma unroll 1
    for (int s = 0; s < 8; ++s) {
      STEP(kA, kAx, kB, kBx, vB0, vB1, vA0, vA1, off);
      STEP(kB, kBx, kA, kAx, vA0, vA1, vB0, vB1, off + KV_TILE);
      STEP(kA, kAx, kB, kBx, vB0, vB1, vA0, vA1, off + 2 * KV_TILE);
      const unsigned full = flag_get(lds, F_FULL);
      STEP(kB, kBx, kA, kAx, vA0, vA1, vB0, vB1, off + 3 * KV_TILE);
      flag_set(lds, F_DONE + w, s + 1);
      off = off == 2 * KV_SEG ? 0 : off + KV_SEG;
      if (s < 7 && rfl(full) < s + 2) lds_wait<1>(lds, F_FULL, s + 2);
    }
    ob = MFMA16(vB0, lo4(pb), ob);
    ob = MFMA16(vB1, hi4(pb), ob);
  } else {
    ma = -INFINITY; mb = -INFINITY;
    const u32x2 qax = {h ? 0u : qa16, 0u}, qbx = {h ? 0u : qb16, 0u};
    kAx[1] = 0u;
#pragma unroll 1
    for (int t = 0; t < 32; ++t) {
      if (t && (t & 3) == 0) lds_wait<1>(lds, F_FULL, (t >> 2) + 1);
      const unsigned off = ((t >> 2) % 3) * KV_SEG + (t & 3) * KV_TILE;
      u32x4 kn, v0, v1; u32x2 knx = {0u, 0u};
      LDK(kn, knx, off); LDV(v0, v1, off);
      if ((t & 3) == 3) flag_set(lds, F_DONE + w, (t >> 2) + 1);
      slow_tile(kA, kAx, v0, v1, qam, qax, oa, ma);
      slow_tile(kA, kAx, v0, v1, qbm, qbx, ob, mb);
      kA = kn; kAx = knx;
    }
  }
  STAMP(1, 5);
  BAR();
  tail_part(oa, ob, ma, mb, w, rb, hg, u, dskip, ws, out, lds);
}

#if defined(__HIP_DEVICE_COMPILE__)
#pragma clang attribute pop
#endif
extern "C" void kernel_launch(void* const* d_in, const int* in_sizes, int n_in, void* d_out, int out_size, void* d_ws, size_t ws_size,
                              hipStream_t stream) {
  (void)hipFuncSetAttribute((const void*)fused_kernel, hipFuncAttributeMaxDynamicSharedMemorySize, K_LDS);
  fused_kernel<<<256, 1024, K_LDS, stream>>>((const u16*)d_in[0], (const u16*)d_in[1], (const u16*)d_in[2], (const u16*)d_in[3], (const u16*)d_in[4],
                                            (const u16*)d_in[5], (const u16*)d_in[6], (const u16*)d_in[7], (const u16*)d_in[8], (const u16*)d_in[9],
                                            (const u16*)d_in[10], (const u16*)d_in[11], (const u16*)d_in[12], (const u16*)d_in[13], (char*)d_ws,
                                            (u16*)d_out);
}
```

```cpp
#if defined(__HIP_DEVICE_COMPILE__)
#pragma clang attribute push(__attribute__((target("no-packed-fp32-ops"))), apply_to = function)
#endif
#include <hip/hip_runtime.h>
#include <stdint.h>

typedef __bf16 bf16x2 __attribute__((ext_vector_type(2)));
typedef __bf16 bf16x8 __attribute__((ext_vector_type(8)));
typedef short s16x4 __attribute__((ext_vector_type(4)));
typedef float f32x2 __attribute__((ext_vector_type(2)));
typedef float f32x4 __attribute__((ext_vector_type(4)));
typedef float f32x16 __attribute__((ext_vector_type(16)));
typedef unsigned u32x2 __attribute__((ext_vector_type(2)));
typedef unsigned u32x4 __attribute__((ext_vector_type(4)));
typedef unsigned short u16;
#define DI __device__ __forceinline__
#define BID ((int)__builtin_amdgcn_workgroup_id_x())
#define TID ((int)__builtin_amdgcn_workitem_id_x())
#define LDSP __attribute__((address_space(3)))
#define SBAR() __builtin_amdgcn_sched_barrier(0)
#define PIN(x) asm volatile("" : "+v"(x))
#define MFMA16(a, b, c) __builtin_amdgcn_mfma_f32_32x32x16_bf16(__builtin_bit_cast(bf16x8, a), __builtin_bit_cast(bf16x8, b), c, 0, 0, 0)
#define MFMA8(a, b, c) __builtin_amdgcn_mfma_f32_32x32x8bf16_1k(__builtin_bit_cast(s16x4, a), __builtin_bit_cast(s16x4, b), c, 0, 0, 0)

constexpr int SEQ = 2048, DM = 576, DH = 18, NST = 128;
constexpr int KV_VP = 320;
constexpr int KV_TILE = 1152 + 4 * KV_VP;
constexpr int KV_HALF = 4 * KV_TILE, KV_SEG = 2 * KV_HALF;
constexpr int KV_HDR = 8 * KV_SEG;
constexpr int UT_ROW = 2064;
constexpr size_t WS_EPOCH = 0;
constexpr size_t WS_KVF = 256;
constexpr size_t WS_OF = WS_KVF + 8 * 2048;
constexpr size_t WS_UF = WS_OF + 2048;
constexpr size_t WS_YF = WS_UF + 2048;
constexpr size_t WS_KMAX = WS_YF + 2048;
constexpr size_t WS_KV = WS_KMAX + 1024;
constexpr size_t WS_O = WS_KV + 163840;
constexpr size_t WS_UT = WS_O + (size_t)SEQ * DM * 2;
constexpr size_t WS_YT = WS_UT + (size_t)DM * UT_ROW * 2;

DI unsigned cvtpk(float a, float b) { f32x2 v = {a, b}; return __builtin_bit_cast(unsigned, __builtin_convertvector(v, bf16x2)); }
DI float bflo(unsigned u) { return __uint_as_float(u << 16); }
DI float bfhi(unsigned u) { return __uint_as_float(u & 0xffff0000u); }
DI float bf2f(u16 x) { return __uint_as_float((unsigned)x << 16); }
DI float rbf(float x) { return bflo(cvtpk(x, x)); }
DI int crow(int reg, int h) { return (reg & 3) + 8 * (reg >> 2) + 4 * h; }
DI void glds16s(const void* sbase, unsigned voff, unsigned lds_base) {
  unsigned sv;
  asm volatile("s_mov_b32 %0, m0\n\ts_mov_b32 m0, %3\n\ts_nop 0\n\tglobal_load_lds_dwordx4 %1, %2\n\ts_mov_b32 m0, %0"
               : "=&s"(sv) : "v"(voff), "s"(sbase), "s"(lds_base) : "memory");
}
DI void glds16c(const void* sbase, unsigned voff, unsigned lds_base) {
  unsigned sv;
  asm volatile("s_mov_b32 %0, m0\n\ts_mov_b32 m0, %3\n\ts_nop 0\n\tglobal_load_lds_dwordx4 %1, %2 sc1\n\ts_mov_b32 m0, %0"
               : "=&s"(sv) : "v"(voff), "s"(sbase), "s"(lds_base) : "memory");
}
#define WAITV(n) asm volatile("s_waitcnt vmcnt(" #n ")" ::: "memory")
#define WAITV_BAR(n) asm volatile("s_waitcnt vmcnt(" #n ") lgkmcnt(0)\n\ts_barrier" ::: "memory")
#define SB() asm volatile("s_barrier" ::: "memory")
#define BAR() asm volatile("s_waitcnt lgkmcnt(0)\n\ts_barrier" ::: "memory")
#define STAMP(kern, i)
DI unsigned rfl(unsigned x) { return __builtin_amdgcn_readfirstlane(x); }
template <typename T> DI T ldsr(const char* lds, unsigned off) { return *(const LDSP T*)((const LDSP char*)lds + off); }
template <typename T> DI void ldsw(char* lds, unsigned off, T v) { *(LDSP T*)((LDSP char*)lds + off) = v; }
DI s16x4 ldstr(const char* lds, unsigned off) {
  typedef short v4i16_t __attribute__((ext_vector_type(4)));
  return __builtin_bit_cast(s16x4, __builtin_amdgcn_ds_read_tr16_b64_v4i16((LDSP v4i16_t*)((LDSP char*)lds + off)));
}
DI float half_sum(float v) {
  const auto t = __builtin_amdgcn_permlane32_swap(__float_as_uint(v), __float_as_uint(v), false, false);
  return __uint_as_float(t[0]) + __uint_as_float(t[1]);
}
DI float half_max(float v) {
  const auto t = __builtin_amdgcn_permlane32_swap(__float_as_uint(v), __float_as_uint(v), false, false);
  return fmaxf(__uint_as_float(t[0]), __uint_as_float(t[1]));
}
DI float half_lo(float v) {
  const auto t = __builtin_amdgcn_permlane32_swap(__float_as_uint(v), __float_as_uint(v), false, false);
  return __uint_as_float(t[0]);
}
template <int CTRL> DI float dpp_max(float v) {
  return fmaxf(v, __uint_as_float(__builtin_amdgcn_update_dpp(__float_as_uint(v), __float_as_uint(v), CTRL, 0xf, 0xf, false)));
}
DI float wave_max(float v) {
  v = dpp_max<0xb1>(v);
  v = dpp_max<0x4e>(v);
  v = dpp_max<0x141>(v);
  v = dpp_max<0x140>(v);
  const unsigned u = __float_as_uint(v);
  const float a = __uint_as_float(__builtin_amdgcn_readlane(u, 0)), b = __uint_as_float(__builtin_amdgcn_readlane(u, 16));
  const float c = __uint_as_float(__builtin_amdgcn_readlane(u, 32)), d = __uint_as_float(__builtin_amdgcn_readlane(u, 48));
  return fmaxf(fmaxf(a, b), fmaxf(c, d));
}

DI float xrow16_sum(float x) {
  const auto s = __builtin_amdgcn_permlane16_swap(__float_as_uint(x), __float_as_uint(x), false, false);
  return half_sum(__uint_as_float(s[0]) + __uint_as_float(s[1]));
}
template <int N> DI void waitv() { asm volatile("s_waitcnt vmcnt(%0)" ::"i"(N) : "memory"); }
DI int wswz(int row) { return ((row >> 1) & 1) | (((row >> 3) & 1) << 1); }
constexpr int kRsrc3 = 0x00020000;
typedef __attribute__((address_space(1))) unsigned gu32;
typedef __attribute__((address_space(1))) unsigned long long gu64;
typedef unsigned long long u64;
DI __amdgpu_buffer_rsrc_t rsrc(const void* p, int bytes) { return __builtin_amdgcn_make_buffer_rsrc((void*)p, 0, bytes, kRsrc3); }
constexpr int SC1 = 16;

#define UT_WAIT F_KVPUB
#define UT_DELAY 16
#define QHOLD 99
#define QRAMP 3
#define GEN_PRIO 2
#define ATT_PRIO 0
#define KV_MODE 1
#define KV_DELAY 32
#define KV_POLL 8
#define LPOLL 1
constexpr int LDS_CV = 3 * KV_SEG;
constexpr int LDS_WKV = 0;
constexpr int LDS_UA = 23040;
constexpr int LDS_QT = 40960;
constexpr int LDS_K0 = 53248;
constexpr int GS = 20736;
constexpr int CV_CH = 31744, CV_K = 4416, CV_U = 17664, CV_UP = 7040;
constexpr int LDS_GEN = LDS_CV + 4 * GS;
constexpr int GEN_AREA = 6656, GEN_PITCH = 272, GEN_P = 21 * GEN_PITCH;
constexpr int LDS_CMB = 0;
constexpr int LDS_OT = 8 * 3328;
constexpr int LDS_FA = LDS_CV + 82944, LDS_FB = LDS_FA + 9216;
constexpr int LDS_SCR = LDS_CV + 3 * CV_CH;
constexpr int LDS_FLAG = LDS_SCR + 4 * 2048;
constexpr int K_LDS = LDS_FLAG + 256;
constexpr float FAST_BOUND = 30.f;
#define CONV_PRIO 0

constexpr int F_FULL = 0, F_SEG0 = 1, F_CFILL = 2, F_CDONE = 3, F_TAGOK = 4, F_TAG = 5  , F_WKV = 7, F_DONE = 8  ,
              F_GF = 16  , F_GD = 32  , F_QDONE = 48, F_QRD = 49, F_KVOK = 50, F_UDONE = 51, F_KCOP = 52, F_WO = 53, F_KVPUB = 54, F_TB = 56  ;
DI void flag_add(char* lds, int word) { __hip_atomic_fetch_add((LDSP unsigned*)((LDSP char*)lds + LDS_FLAG) + word, 1u, __ATOMIC_RELAXED, __HIP_MEMORY_SCOPE_WORKGROUP); }
DI void flag_set(char* lds, int word, unsigned v) {
  asm volatile("" ::: "memory");
  __hip_atomic_store((LDSP unsigned*)((LDSP char*)lds + LDS_FLAG) + word, v, __ATOMIC_RELAXED, __HIP_MEMORY_SCOPE_WORKGROUP);
  asm volatile("" ::: "memory");
}
DI unsigned flag_get(char* lds, int word) { return __hip_atomic_load((LDSP unsigned*)((LDSP char*)lds + LDS_FLAG) + word, __ATOMIC_RELAXED, __HIP_MEMORY_SCOPE_WORKGROUP); }
template <int SLEEP, int PRIO = -1> DI void lds_wait(char* lds, int word, unsigned need) {
  if (rfl(flag_get(lds, word)) < need) {
    if (PRIO >= 0) __builtin_amdgcn_s_setprio(0);
#pragma unroll 1
    for (unsigned spins = 0; spins < (1u << 20); ++spins) {
      __builtin_amdgcn_s_sleep(SLEEP);
      if (rfl(flag_get(lds, word)) >= need) break;
    }
    if (PRIO >= 0) __builtin_amdgcn_s_setprio(PRIO);
  }
  asm volatile("" ::: "memory");
}
template <int SLEEP> DI void done_wait(char* lds, unsigned need, int lane) {
#pragma unroll 1
  for (unsigned spins = 0; spins < (1u << 20); ++spins) {
    if (__builtin_amdgcn_ballot_w64(flag_get(lds, F_DONE + (lane & 7)) < need) == 0) break;
    __builtin_amdgcn_s_sleep(SLEEP);
  }
  asm volatile("" ::: "memory");
}
#define ONE_LANE(stmt) do { asm volatile("" ::: "memory"); if (lane == 0) { stmt; } asm volatile("" ::: "memory"); } while (0)

#define FLAG_ADD(word) ONE_LANE(flag_add(lds, word))
#define TBAR(k) do { asm volatile("s_waitcnt lgkmcnt(0)" ::: "memory"); FLAG_ADD(F_TB + (k)); lds_wait<0>(lds, F_TB + (k), 8); } while (0)
DI u64 get_tag(char* lds) {
  lds_wait<1>(lds, F_TAGOK, 1);
  return (u64)rfl(flag_get(lds, F_TAG)) | ((u64)rfl(flag_get(lds, F_TAG + 1)) << 32);
}
DI void tag_store(char* p, u64 tag) { __hip_atomic_store((gu64*)p, tag, __ATOMIC_RELAXED, __HIP_MEMORY_SCOPE_AGENT); }
template <int N, int SLEEP> DI void tags_wait(const char* p, u64 tag, int lane) {
#pragma unroll 1
  for (unsigned spins = 0; spins < (1u << 20); ++spins) {
    bool ok = true;
#pragma unroll
    for (int j = 0; j < (N + 63) / 64; ++j) {
      const int k = 64 * j + lane;
      ok &= __hip_atomic_load((gu64*)p + (k < N ? k : 0), __ATOMIC_RELAXED, __HIP_MEMORY_SCOPE_AGENT) == tag;
    }
    if (__builtin_amdgcn_ballot_w64(!ok) == 0) break;
    __builtin_amdgcn_s_sleep(SLEEP);
  }
  __builtin_amdgcn_fence(__ATOMIC_ACQUIRE, "wavefront");
  asm volatile("" ::: "memory");
}

template <int SLEEP> DI void tags256_wait(const char* p, u64 tag, int lane) {
  u64 a[4], c[4];
#define TLD(v) do { _Pragma("unroll") for (int j = 0; j < 4; ++j) v[j] = __hip_atomic_load((gu64*)p + 64 * j + lane, __ATOMIC_RELAXED, __HIP_MEMORY_SCOPE_AGENT); } while (0)
#define TOK(v) (__builtin_amdgcn_ballot_w64(!(v[0] == tag && v[1] == tag && v[2] == tag && v[3] == tag)) == 0)
  TLD(a);
#pragma unroll 1
  for (unsigned spins = 0; spins < (1u << 19); ++spins) {
    __builtin_amdgcn_s_sleep(SLEEP);
    TLD(c);
    if (TOK(a)) break;
    __builtin_amdgcn_s_sleep(SLEEP);
    TLD(a);
    if (TOK(c)) break;
  }
#undef TLD
#undef TOK
  __builtin_amdgcn_fence(__ATOMIC_ACQUIRE, "wavefront");
  asm volatile("" ::: "memory");
}

constexpr int NI = 7;
template <int IW> constexpr int q_cnt(int s) { return s > 7 ? 0 : s < 4 ? (18 - IW + NI - 1) / NI : (21 - IW + NI - 1) / NI; }
template <int IW, int S> DI void q_issue(const char* wb, const char* xb, int kvg, const unsigned (&vw)[3], const unsigned (&vx)[3], unsigned lds0, int lane) {
  const unsigned sb = lds0 + LDS_CV + (S & 3) * GS;
#pragma unroll
  for (int j = 0; j < q_cnt<IW>(S); ++j) {
    const int p = IW + NI * j;
    if constexpr (S >= 4) { if (p < 20 || lane < 16) glds16s(wb + (size_t)144 * (S - 4) * (DM * 2), vw[j], sb + 1024 * p); }
    else glds16s(xb + (size_t)16 * ((kvg + S) & 3) * (DM * 2), vx[j], sb + 1024 * p);
  }
}
constexpr int q_last(int S) { return S < 0 ? 1 : S < QRAMP ? S + 2 : (S + 3 > 7 ? 7 : S + 3); }
template <int IW> constexpr int q_pending(int S) { int n = 0; for (int k = S + 1; k <= q_last(S - 1); ++k) n += q_cnt<IW>(k); return n; }
template <int IW, int S, int K> DI void q_issue_upto(const char* wb, const char* xb, int kvg, const unsigned (&vw)[3], const unsigned (&vx)[3], char* lds, unsigned lds0, int lane) {
  if constexpr (K <= q_last(S) && K <= 7) {
    if constexpr (K == QHOLD) lds_wait<LPOLL, 3>(lds, F_KVPUB, 1);
    if constexpr (K >= 4) lds_wait<LPOLL, 3>(lds, F_GD + K - 4, K == 4 ? 2 : 1);
    q_issue<IW, K>(wb, xb, kvg, vw, vx, lds0, lane);
    q_issue_upto<IW, S, K + 1>(wb, xb, kvg, vw, vx, lds, lds0, lane);
  }
}
template <int IW, int S> DI void q_steps(const char* wb, const char* xb, int kvg, const unsigned (&vw)[3], const unsigned (&vx)[3], char* lds, unsigned lds0, int lane) {
  waitv<q_pending<IW>(S)>();
  FLAG_ADD(F_GF + S);
  q_issue_upto<IW, S, q_last(S - 1) + 1>(wb, xb, kvg, vw, vx, lds, lds0, lane);
  if constexpr (S + 1 < 8) q_steps<IW, S + 1>(wb, xb, kvg, vw, vx, lds, lds0, lane);
}
DI unsigned x_piece(int p, int lane) {
  const int n = 64 * p + lane, row = n / 72, c = n - 72 * row;
  return row * (DM * 2) + 16 * ((c & ~7) | ((c ^ (row >> 1)) & 7));
}
template <int IW> DI void q_loader(const u16* x, const u16* wq, int rb, int hg, char* lds, unsigned lds0, int lane) {
  unsigned vw[3], vx[3];
#pragma unroll
  for (int j = 0; j < 3; ++j) vx[j] = x_piece(IW + NI * j < 18 ? IW + NI * j : 0, lane);
  const char* wb = (const char*)(wq + 72 * hg);
  const char* xb = (const char*)(x + (size_t)64 * rb * DM);
  q_issue<IW, 0>(wb, xb, hg & 3, vw, vx, lds0, lane);
  q_issue<IW, 1>(wb, xb, hg & 3, vw, vx, lds0, lane);
#pragma unroll
  for (int j = 0; j < 3; ++j) { const int n = 64 * (IW + NI * j) + lane, row = n / 9, c = n - 9 * row; vw[j] = row * (DM * 2) + 16 * c; }
  q_steps<IW, 0>(wb, xb, hg & 3, vw, vx, lds, lds0, lane);
}
DI void x_bases(int lane, unsigned (&xb)[2]) {
  const int li = lane & 15, g = lane >> 4, xs = (li >> 1) & 7;
  xb[0] = li * (DM * 2) + 16 * (g ^ xs); xb[1] = li * (DM * 2) + 16 * ((4 + g) ^ xs);
}
#define XFRAG(base, ks) ldsr<u32x4>(lds, (base)[(ks) & 1] + 128 * ((ks) >> 1))

constexpr int QD0 = 5;
constexpr int QD1(int i) { return QD0 + 1 + i / 5 > 12 ? 12 : QD0 + 1 + i / 5; }
constexpr int qp_end(int i) { return i < 20 ? 20 : i < 45 ? 45 : i < 65 ? 65 : 90; }
constexpr int qp_ahead(int i) { return i + 1 + (QD0 + i / 5 > 12 ? 12 : QD0 + i / 5) < qp_end(i) ? i + 1 + (QD0 + i / 5 > 12 ? 12 : QD0 + i / 5) : qp_end(i); }
template <int FROM, int TO> DI void q_reads(const char* lds, unsigned wn, unsigned wx, u32x4 (&wa)[90]) {
  if constexpr (FROM < TO) {
    constexpr int ks = FROM / 5, nt = FROM % 5;
    const unsigned b_ = ((2 * ks) % 9 == 8 ? wx : wn) + ((2 * ks) / 9) * GS + ((2 * ks) % 9) * 2304 + 32 * nt;
    wa[FROM] = __builtin_bit_cast(u32x4, __builtin_shufflevector(ldstr(lds, b_), ldstr(lds, b_ + 4 * 144), 0, 1, 2, 3, 4, 5, 6, 7));
    q_reads<FROM + 1, TO>(lds, wn, wx, wa);
  }
}
template <int I, int NXT> DI void q_products(char* lds, unsigned wn, unsigned wx, const u32x4 (&xf)[18], u32x4 (&wa)[90], f32x4 (&acc)[5]) {
  if constexpr (I < 90) {
    constexpr bool first = I == 0 || I == 20 || I == 45 || I == 65;
    constexpr int start = first ? (I + QD1(I) < qp_end(I) ? I + QD1(I) : qp_end(I)) : NXT;
    if constexpr (first) {
      lds_wait<LPOLL, 3>(lds, F_GF + 4 + (2 * (I / 5) + 1) / 9, NI);
      q_reads<I, start>(lds, wn, wx, wa);
      SBAR();
    }
    acc[I % 5] = __builtin_amdgcn_mfma_f32_16x16x32_bf16(__builtin_bit_cast(bf16x8, wa[I]), __builtin_bit_cast(bf16x8, xf[I / 5]), acc[I % 5], 0, 0, 0);
    SBAR();
    constexpr int upto = qp_ahead(I) > start ? qp_ahead(I) : start;
    q_reads<start, upto>(lds, wn, wx, wa);
    SBAR();
    q_products<I + 1, upto>(lds, wn, wx, xf, wa, acc);
  }
}
DI void q_compute(const u16* wk, const u16* wv, int hg, char* lds, int w, int lane, char* ws) {
  {
    typedef unsigned u32x3 __attribute__((ext_vector_type(3)));
    const char* src = (const char*)(hg >> 2 ? wv : wk);
    u32x3 t[7];
#pragma unroll
    for (int j = 0; j < 7; ++j) { const int i = w + 4 * j; __builtin_memcpy(&t[j], src + 12 * (64 * (i < 27 ? i : 0) + lane), 12); }
#pragma unroll
    for (int j = 0; j < 7; ++j) {
      const int i = w + 4 * j, e = 64 * i + lane, k = e / 3, c = e - 3 * k;
      if (i < 27) { const unsigned o = LDS_WKV + 40 * k + 12 * c; ldsw<unsigned>(lds, o, t[j][0]); ldsw<unsigned>(lds, o + 4, t[j][1]); ldsw<unsigned>(lds, o + 8, t[j][2]); }
    }
    asm volatile("s_waitcnt lgkmcnt(0)" ::: "memory");
    FLAG_ADD(F_WKV);
    STAMP(1, 6);
  }
  const int li = lane & 15, g = lane >> 4, q = li >> 2, p = lane & 3, st = (w - hg) & 3;
  u32x4 xf[18];
  {
    unsigned xb[2];
    x_bases(lane, xb);
    xb[0] += LDS_CV + st * GS; xb[1] += LDS_CV + st * GS;
    lds_wait<LPOLL, 3>(lds, F_GF + st, NI);
#pragma unroll
    for (int ks = 0; ks < 18; ++ks) xf[ks] = XFRAG(xb, ks);
    asm volatile("s_waitcnt lgkmcnt(0)" ::: "memory");
    FLAG_ADD(F_GD + st);
  }
  STAMP(1, 7);
  f32x4 acc[5] = {};
  const unsigned wbase = LDS_CV + (8 * (g & 1) + q) * 144 + 8 * p;
  const unsigned wn = wbase + (g >> 1) * 2304;
  const unsigned wx = wbase + (g >> 1) * (GS - 8 * 2304);
  u32x4 wa[90];
  q_products<0, 0>(lds, wn, wx, xf, wa, acc);
#pragma unroll
  for (int nt = 0; nt < 5; ++nt) {
    const unsigned a = cvtpk(acc[nt][0], acc[nt][1]), b2 = cvtpk(acc[nt][2], acc[nt][3]);
#pragma unroll
    for (int e = 0; e < 4; ++e) {
      const int n = 16 * nt + 4 * g + e, hh = n / 18;
      const unsigned v = e < 2 ? a : b2;
      if (nt < 4 || g < 2) ldsw<u16>(lds, LDS_QT + (16 * w + li) * 192 + hh * 48 + 2 * (n - 18 * hh), (u16)((e & 1) ? v >> 16 : v));
    }
  }
  asm volatile("s_waitcnt lgkmcnt(0)" ::: "memory");
  FLAG_ADD(F_QDONE);
}

template <bool ISV> DI void kv_products(const char* lds, int lane, const u32x4 (&xf)[18], f32x4 (&acc)[2]) {
  const int li = lane & 15, g = lane >> 4, q = li >> 2, p = lane & 3;
  f32x4 a0[2] = {}, a1[2] = {};
  const unsigned wo_ = LDS_WKV + (8 * g + q) * 40 + 8 * p;
  s16x4 wr[4][4];
#define WKV_RD(ks) do { _Pragma("unroll") for (int e = 0; e < 4; ++e) wr[(ks) & 3][e] = ldstr(lds, wo_ + 32 * (ks) * 40 + (e >> 1) * 32 + (e & 1) * 160); } while (0)
  WKV_RD(0); WKV_RD(1); WKV_RD(2);
#pragma unroll
  for (int ks = 0; ks < 18; ++ks) {
    if (ks + 3 < 18) WKV_RD(ks + 3);
    SBAR();
#pragma unroll
    for (int nt = 0; nt < 2; ++nt) {
      const auto wfr = __builtin_shufflevector(wr[ks & 3][2 * nt], wr[ks & 3][2 * nt + 1], 0, 1, 2, 3, 4, 5, 6, 7);
      f32x4& a = (ks & 1) ? a1[nt] : a0[nt];
      if (ISV) a = __builtin_amdgcn_mfma_f32_16x16x32_bf16(__builtin_bit_cast(bf16x8, xf[ks]), __builtin_bit_cast(bf16x8, wfr), a, 0, 0, 0);
      else a = __builtin_amdgcn_mfma_f32_16x16x32_bf16(__builtin_bit_cast(bf16x8, wfr), __builtin_bit_cast(bf16x8, xf[ks]), a, 0, 0, 0);
    }
    SBAR();
  }
#undef WKV_RD
#pragma unroll
  for (int e = 0; e < 4; ++e) { acc[0][e] = a0[0][e] + a1[0][e]; acc[1][e] = a0[1][e] + a1[1][e]; }
}
DI void kv_wave(int b, int rb, int hg, const u16* x, char* ws, char* lds, unsigned lds0, int lane) {
  const int li = lane & 15, g = lane >> 4;
  const bool isv = hg >> 2;
  u32x4 xf[18];
  lds_wait<LPOLL, 3>(lds, F_GF + 0, NI);
  unsigned xb[2];
  x_bases(lane, xb);
  xb[0] += LDS_CV; xb[1] += LDS_CV;
#pragma unroll
  for (int ks = 0; ks < 18; ++ks) xf[ks] = XFRAG(xb, ks);
  asm volatile("s_waitcnt lgkmcnt(0)" ::: "memory");
  FLAG_ADD(F_GD + 0);
  lds_wait<LPOLL, 3>(lds, F_WKV, 4);
  STAMP(1, 2);
  f32x4 acc[2];
  if (isv) kv_products<true>(lds, lane, xf, acc); else kv_products<false>(lds, lane, xf, acc);
  STAMP(1, 4);
  const int kt = 2 * rb + ((hg & 3) >> 1), s = hg & 1, kh = kt >> 5, t = kt & 31;
  const __amdgpu_buffer_rsrc_t rs = rsrc(ws + WS_KV, 163840);
  if (!isv) {
    const int img = t ? ((t - 1) >> 2) * KV_SEG + kh * KV_HALF + ((t - 1) & 3) * KV_TILE : KV_HDR + kh * 1152;
    const float sc = 0.23570226039551584f * 1.4426950408889634f;
    const int key = 16 * s + li, e = (key & 0x13) | ((key & 4) << 1) | ((key & 8) >> 1);
    const unsigned d0 = cvtpk(acc[0][0] * sc, acc[0][1] * sc), d1 = cvtpk(acc[0][2] * sc, acc[0][3] * sc);
    const unsigned d2 = g ? 0u : cvtpk(acc[1][0] * sc, acc[1][1] * sc);
    __builtin_amdgcn_raw_buffer_store_b64(u32x2{d0, d1}, rs, img + (g >> 1) * 512 + e * 16 + 8 * (g & 1), 0, SC1);
    if (g == 0) __builtin_amdgcn_raw_buffer_store_b32(d2, rs, img + 1024 + e * 4, 0, SC1);
    float n2 = bflo(d0) * bflo(d0) + bfhi(d0) * bfhi(d0) + bflo(d1) * bflo(d1) + bfhi(d1) * bfhi(d1) + bflo(d2) * bflo(d2) + bfhi(d2) * bfhi(d2);
    n2 = wave_max(xrow16_sum(n2));
    if (lane == 0) __hip_atomic_store((gu32*)(ws + WS_KMAX) + 4 * rb + hg, __float_as_uint(n2), __ATOMIC_RELAXED, __HIP_MEMORY_SCOPE_AGENT);
  } else {
    const int img = (t >> 2) * KV_SEG + kh * KV_HALF + (t & 3) * KV_TILE + 1152 + (2 * s + (g >> 1)) * KV_VP + 8 * (g & 1);
    __builtin_amdgcn_raw_buffer_store_b64(u32x2{cvtpk(acc[0][0], acc[0][1]), cvtpk(acc[0][2], acc[0][3])}, rs, img + li * 16, 0, SC1);
    u32x2 v = {cvtpk(acc[1][0], acc[1][1]), cvtpk(acc[1][2], acc[1][3])};
    if (li == 2) v = u32x2{0x3f803f80u, 0x3f803f80u};
    if (li == 3) v = u32x2{0u, 0u};
    if (li < 4) __builtin_amdgcn_raw_buffer_store_b64(v, rs, img + (16 + li) * 16, 0, SC1);
  }
  const u64 tag = get_tag(lds);
  WAITV(0);
  if (lane < 8) tag_store(ws + WS_KVF + 2048 * lane + 8 * b, tag);
  flag_set(lds, F_KVPUB, 1);
  STAMP(1, 3);
  __builtin_amdgcn_s_sleep(KV_DELAY);
  tags_wait<256, KV_POLL>(ws + WS_KVF + 2048 * (b & 7), tag, lane);
  flag_set(lds, F_KVOK, 1);
  if (b == 0 && lane == 0) tag_store(ws + WS_EPOCH, tag);
}

DI void ut_wave(int rb, int hg, const u16* u, char* ws, char* lds, int lane) {
  u32x4 v[9];
#pragma unroll
  for (int j = 0; j < 9; ++j) { const int ch = 64 * j + lane, row = ch / 9, c = ch - 9 * row; v[j] = *(const u32x4*)(u + (size_t)(64 * rb + row) * DM + 72 * hg + 8 * c); }
#pragma unroll
  for (int j = 0; j < 9; ++j) ldsw<u32x4>(lds, LDS_UA + (64 * j + lane) * 16, v[j]);
  const __amdgpu_buffer_rsrc_t rs = rsrc(ws + WS_UT + (size_t)72 * hg * (UT_ROW * 2), 72 * UT_ROW * 2);
  const int g = lane >> 4, li = lane & 15, q = li >> 2, p = lane & 3;
  u32x4 t[10];
#pragma unroll
  for (int i = 0; i < 10; ++i) {
    const unsigned ad = LDS_UA + (8 * (4 * (i & 1) + g) + q) * 144 + 32 * (i >> 1) + 8 * p;
    t[i] = __builtin_bit_cast(u32x4, __builtin_shufflevector(ldstr(lds, ad), ldstr(lds, ad + 4 * 144), 0, 1, 2, 3, 4, 5, 6, 7));
  }
  asm volatile("s_waitcnt lgkmcnt(0)" ::: "memory");
  FLAG_ADD(F_UDONE);
  lds_wait<4>(lds, UT_WAIT, 1);
#pragma unroll
  for (int i = 0; i < 10; ++i)
    if (i < 8 || li < 8) __builtin_amdgcn_raw_buffer_store_b128(t[i], rs, (16 * (i >> 1) + li) * (UT_ROW * 2) + 16 + 128 * rb + 16 * (4 * (i & 1) + g), 0, SC1);
  if (rb == 0 || rb == 31) {
#pragma unroll
    for (int j = 0; j < 2; ++j)
      if (64 * j + lane < 72) __builtin_amdgcn_raw_buffer_store_b128(u32x4{0, 0, 0, 0}, rs, (64 * j + lane) * (UT_ROW * 2) + (rb ? 16 + SEQ * 2 : 0), 0, SC1);
  }
  const u64 tag = get_tag(lds);
  WAITV(0);
  if (lane == 0) tag_store(ws + WS_UF + 8 * (32 * hg + rb), tag);
}

DI f32x2 cmul(f32x2 a, f32x2 b) { return f32x2{__builtin_fmaf(a[0], b[0], -(a[1] * b[1])), __builtin_fmaf(a[0], b[1], a[1] * b[0])}; }
DI f32x2 cexp_l(float zr, float zi, float l) {
  const float mag = __builtin_amdgcn_exp2f((zr * l) * 1.4426950408889634f);
  const float rev = __builtin_amdgcn_fractf((zi * l) * 0.15915494309189535f);
  return f32x2{mag * __builtin_amdgcn_cosf(rev), mag * __builtin_amdgcn_sinf(rev)};
}
DI f32x2 cis2(float e2, float rev) {
  const float mag = __builtin_amdgcn_exp2f(e2), f = __builtin_amdgcn_fractf(rev);
  return f32x2{mag * __builtin_amdgcn_cosf(f), mag * __builtin_amdgcn_sinf(f)};
}
DI void gen_wave(int d, const u16* Are, const u16* Aim, const u16* Bre, const u16* Bim, const u16* Cre, const u16* Cim,
                 const u16* logdt, char* lds, int lane) {
  const int r = lane & 31, h = lane >> 5;
  u16 pr[2][6];
#pragma unroll
  for (int pass = 0; pass < 2; ++pass) {
    const int idx = d * NST + 64 * pass + lane;
    pr[pass][0] = Are[idx]; pr[pass][1] = Aim[idx]; pr[pass][2] = Bre[idx]; pr[pass][3] = Bim[idx]; pr[pass][4] = Cre[idx]; pr[pass][5] = Cim[idx];
  }
  const float dt = rbf(expf(bf2f(logdt[d])));
  const float fm = (float)r;
  f32x16 acc = {};
#pragma unroll 1
  for (int pass = 0; pass < 2; ++pass) {
    const u16 p0 = pass ? pr[1][0] : pr[0][0], p1 = pass ? pr[1][1] : pr[0][1], p2_ = pass ? pr[1][2] : pr[0][2];
    const u16 p3_ = pass ? pr[1][3] : pr[0][3], p4_ = pass ? pr[1][4] : pr[0][4], p5 = pass ? pr[1][5] : pr[0][5];
    const float zr = dt * bf2f(p0), zi = dt * bf2f(p1);
    const float dBr = dt * bf2f(p2_), dBi = dt * bf2f(p3_);
    const float cr = bf2f(p4_), ci = bf2f(p5);
    const f32x2 c = {cr * dBr - ci * dBi, cr * dBi + ci * dBr};
    ldsw<f32x2>(lds, GEN_P + 8 * lane, f32x2{zr * (8.f * 1.4426950408889634f), zi * (-8.f * 0.15915494309189535f)});
    const f32x2 w1 = cexp_l(zr, zi, 1.f), w256 = cexp_l(zr, zi, 256.f);
#define PUTY(col, v) ldsw<unsigned>(lds, (col) * GEN_PITCH + 4 * lane, cvtpk((v)[0], (v)[1]))
    f32x2 y[8];
    y[0] = c;
#pragma unroll
    for (int i = 1; i < 8; ++i) y[i] = cmul(y[i - 1], w1);
#pragma unroll
    for (int i = 0; i < 8; ++i) PUTY(i, y[i]);
#pragma unroll
    for (int i = 0; i < 4; ++i) { const f32x2 t = cmul(y[2 * i], w256); PUTY(8 + i, t); }
    const f32x2 p2 = cmul(w256, w256), p3 = cmul(p2, w256), p4 = cmul(p2, p2);
    { f32x2 t = cmul(y[0], p2); PUTY(12, t); t = cmul(y[4], p2); PUTY(13, t); }
    { f32x2 t = cmul(y[0], p3); PUTY(14, t); t = cmul(y[4], p3); PUTY(15, t); }
    { const f32x2 t4 = cmul(y[0], p4); PUTY(16, t4);
      f32x2 t = cmul(t4, w256); PUTY(17, t); t = cmul(t4, p2); PUTY(18, t); t = cmul(t4, p3); PUTY(19, t); t = cmul(t4, p4); PUTY(20, t); }
#undef PUTY
    asm volatile("" ::: "memory");
    const int yr = r <= 20 ? r : r - 16;
#pragma unroll 2
    for (int ks = 0; ks < 8; ++ks) {
      const f32x4 z0 = ldsr<f32x4>(lds, GEN_P + 64 * ks + 32 * h), z1 = ldsr<f32x4>(lds, GEN_P + 64 * ks + 32 * h + 16);
      const u32x4 fb = ldsr<u32x4>(lds, yr * GEN_PITCH + 32 * ks + 16 * h);
      const f32x2 a0 = cis2(z0[0] * fm, z0[1] * fm), a1 = cis2(z0[2] * fm, z0[3] * fm), a2 = cis2(z1[0] * fm, z1[1] * fm), a3 = cis2(z1[2] * fm, z1[3] * fm);
      const u32x4 fa = {cvtpk(a0[0], a0[1]), cvtpk(a1[0], a1[1]), cvtpk(a2[0], a2[1]), cvtpk(a3[0], a3[1])};
      acc = MFMA16(fa, fb, acc);
    }
    asm volatile("" ::: "memory");
  }
  const int c = r;
  const int hi = c < 8 ? c : c < 12 ? 256 + 2 * (c - 8) : c < 16 ? 512 + 256 * ((c - 12) >> 1) + 4 * ((c - 12) & 1) : 1024 + 256 * (c - 16);
  if (c < 20) {
#pragma unroll
    for (int i = 0; i < 16; ++i) ldsw<u16>(lds, 2 * (8 * crow(i, h) + hi), (u16)cvtpk(acc[i], acc[i]));
  } else if (c == 20 && h == 0) {
    ldsw<u16>(lds, 2 * 2048, (u16)cvtpk(acc[0], acc[0]));
  }
  asm volatile("" ::: "memory");
}
DI void build_copies(const char* kc, char* cl, int lane) {
  auto rd = [&](int l) -> unsigned { return ldsr<u16>(kc, 2 * l); };
  auto pk = [](unsigned lo, unsigned hi) -> unsigned { return lo | (hi << 16); };
  auto put2 = [&](int par, int y, unsigned v0, unsigned v1) {
    ldsw<u32x2>(cl, par * CV_K + 4 * y, u32x2{v0, v1});
    ldsw<unsigned>(cl, (par + 2) * CV_K + 4 * y - 4, v0); ldsw<unsigned>(cl, (par + 2) * CV_K + 4 * y, v1);
  };
  const int L = lane;
  {
    const unsigned e0 = rd(4 * L), e1 = rd(4 * L + 1), e2 = rd(4 * L + 2), e3 = rd(4 * L + 3);
    const unsigned pv = L ? rd(4 * L - 1) : 0u;
    put2(0, 1028 - 2 * L, pk(e3, e2), pk(e1, e0));
    put2(1, 1028 - 2 * L, pk(e2, e1), pk(e0, pv));
  }
  {
    const int j0 = 256 + 4 * L;
    const unsigned a = rd(j0), b = rd(j0 + 2), c = rd(j0 + 4), pv = L ? a : rd(255);
    put2(0, 1028 - j0 / 2, pk(c, b), pk(a, a));
    put2(1, 1028 - j0 / 2, pk(b, a), pk(a, pv));
  }
  {
    const int j0 = 512 + 8 * L, y = 1026 - j0 / 2;
    const unsigned a = rd(j0), b = rd(j0 + 4), c = rd(j0 + 8);
    const unsigned aa = pk(a, a), bb = pk(b, b), cc = pk(c, c);
    put2(0, y, cc, bb);        put2(0, y + 2, pk(b, a), aa);
    put2(1, y, pk(c, b), bb);  put2(1, y + 2, aa, aa);
  }
  {
    const int j0 = 1024 + 16 * L, y = 1022 - j0 / 2;
    const unsigned a = rd(j0), b = rd(j0 + 8), c = rd(j0 + 16);
    const unsigned aa = pk(a, a), bb = pk(b, b), cc = pk(c, c);
    put2(0, y, cc, cc);        put2(0, y + 2, bb, bb);  put2(0, y + 4, bb, pk(b, a));  put2(0, y + 6, aa, aa);
    put2(1, y, cc, pk(c, b));  put2(1, y + 2, bb, bb);  put2(1, y + 4, bb, aa);        put2(1, y + 6, aa, aa);
    if (L == 63) {
      ldsw<unsigned>(cl, CV_K + 4 * 5, pk(0u, c)); ldsw<unsigned>(cl, 3 * CV_K + 4 * 4, pk(0u, c));
    }
  }
  if (L < 6) { ldsw<unsigned>(cl, 4 * L, 0u); if (L) ldsw<unsigned>(cl, 2 * CV_K + 4 * L - 4, 0u); }
  if (L < 5) { ldsw<unsigned>(cl, CV_K + 4 * L, 0u); if (L) ldsw<unsigned>(cl, 3 * CV_K + 4 * L - 4, 0u); }
  if (L < 42) {
    ldsw<unsigned>(cl, 4 * (1030 + L), 0u); ldsw<unsigned>(cl, CV_K + 4 * (1030 + L), 0u);
    ldsw<unsigned>(cl, 2 * CV_K + 4 * (1029 + L), 0u); ldsw<unsigned>(cl, 3 * CV_K + 4 * (1029 + L), 0u);
  }
}
template <int SW> DI void u_fill(int d, const char* ws, char* cl, unsigned cl0, int lane) {
  const char* ur = ws + WS_UT + (size_t)d * (UT_ROW * 2);
#pragma unroll
  for (int item = SW; item < 10; item += 4) {
    const int bb = item / 5, part = item - 5 * bb, sg = 136 + part * 64 + lane, qc = sg - sg / 9, s0 = 8 * qc - 976 + 4 * bb;
    if (sg <= 424) glds16c(ur, sg % 9 == 8 ? 0u : 2 * (8 + s0), cl0 + CV_U + bb * CV_UP + (136 + part * 64) * 16);
  }
  if (SW < 2) {
#pragma unroll
    for (int j = 0; j < 3; ++j)
      if (64 * j + lane < 136) ldsw<u32x4>(cl, CV_U + SW * CV_UP + (64 * j + lane) * 16, u32x4{0, 0, 0, 0});
  }
}
template <int SW> DI void u_fill2(int d0, const char* ws, char* lds, unsigned lds0, int lane) {
  u_fill<SW>(d0, ws, lds + LDS_CV, lds0 + LDS_CV, lane);
  u_fill<(SW + 2) & 3>(d0 + 1, ws, lds + LDS_CV + CV_CH, lds0 + LDS_CV + CV_CH, lane);
}

struct KVAddr { unsigned k, kx, v; };
#define LDK(dst, dstx, t_off) do { dst = ldsr<u32x4>(lds, ad.k + (t_off)); dstx[0] = ldsr<unsigned>(lds, ad.kx + (t_off)); } while (0)
#define LDV(d0, d1, t_off) do { d0 = ldsr<u32x4>(lds, ad.v + (t_off)); d1 = ldsr<u32x4>(lds, ad.v + 2 * KV_VP + (t_off)); } while (0)
#define EX2(x) __builtin_amdgcn_exp2f(x)
#define GRP(MF, S, P, i) do { MF; SBAR(); \
    { const float e0 = EX2(S[4 * (i)]), e1 = EX2(S[4 * (i) + 1]), e2 = EX2(S[4 * (i) + 2]), e3 = EX2(S[4 * (i) + 3]); \
      unsigned t0_ = cvtpk(e0, e1), t1_ = cvtpk(e2, e3); PIN(t0_); PIN(t1_); P[2 * (i)] = t0_; P[2 * (i) + 1] = t1_; } \
    SBAR(); } while (0)
typedef unsigned u32x8 __attribute__((ext_vector_type(8)));
DI u32x4 lo4(const u32x8& p) { return __builtin_shufflevector(p, p, 0, 1, 2, 3); }
DI u32x4 hi4(const u32x8& p) { return __builtin_shufflevector(p, p, 4, 5, 6, 7); }

#define STEP(Kc, Kcx, Kn, Knx, Vp0, Vp1, Vc0, Vc1, off) do { \
    LDK(Kn, Knx, (off)); LDV(Vc0, Vc1, (off)); SBAR(); \
    GRP(sb = MFMA16(Kc, qbm, zero16), sa, pa, 0); \
    GRP(ob = MFMA16(Vp0, lo4(pb), ob), sa, pa, 1); \
    GRP(sb = MFMA8(Kcx, qbx, sb), sa, pa, 2); \
    GRP(ob = MFMA16(Vp1, hi4(pb), ob), sa, pa, 3); \
    GRP(sa = MFMA16(Kn, qam, zero16), sb, pb, 0); \
    GRP(oa = MFMA16(Vc0, lo4(pa), oa), sb, pb, 1); \
    GRP(sa = MFMA8(Knx, qax, sa), sb, pb, 2); \
    GRP(oa = MFMA16(Vc1, hi4(pa), oa), sb, pb, 3); \
  } while (0)

DI void slow_tile(const u32x4& kf, const u32x2& kx, const u32x4& v0, const u32x4& v1, const u32x4& qm, const u32x2& qx, f32x16& o, float& m) {
  f32x16 s = {};
  s = MFMA16(kf, qm, s);
  s = MFMA8(kx, qx, s);
  float mx = s[0];
#pragma unroll
  for (int i = 1; i < 16; ++i) mx = fmaxf(mx, s[i]);
  mx = half_max(mx);
  const float mn = fmaxf(m, mx);
  const float alpha = EX2(m - mn);
  m = mn;
#pragma unroll
  for (int i = 0; i < 16; ++i) o[i] *= alpha;
  u32x8 p;
#pragma unroll
  for (int i = 0; i < 8; ++i) p[i] = cvtpk(EX2(s[2 * i] - mn), EX2(s[2 * i + 1] - mn));
  o = MFMA16(v0, lo4(p), o);
  o = MFMA16(v1, hi4(p), o);
}

template <bool TWO, bool SKIP0> DI void conv_steps(const char* lds, unsigned pa0, unsigned pb0, unsigned pa1, unsigned pb1, f32x16& c0, f32x16& c1) {
  __builtin_amdgcn_s_setprio(CONV_PRIO);
  u32x4 a0[2], b0[2], a1[2], b1[2];
#define CLD(i, n) do { a0[i] = ldsr<u32x4>(lds, pa0 + 32 * (n)); b0[i] = ldsr<u32x4>(lds, pb0 + 32 * (n) + 16 * ((n) >> 2)); \
    if (TWO) { a1[i] = ldsr<u32x4>(lds, pa1 + 32 * (n)); b1[i] = ldsr<u32x4>(lds, pb1 + 32 * (n) + 16 * ((n) >> 2)); } } while (0)
#define CMM(i) do { c0 = MFMA16(a0[i], b0[i], c0); if (TWO) c1 = MFMA16(a1[i], b1[i], c1); } while (0)
  CLD(0, 0); CLD(1, 1);
  c0 = MFMA16(a0[0], b0[0], c0);
  if (TWO && !SKIP0) c1 = MFMA16(a1[0], b1[0], c1);
  SBAR();
#pragma unroll 1
  for (int n4 = 0; n4 < 16; ++n4) {
    const unsigned oa = 128 * n4, ob = 144 * n4;
#define CLD4(i, k) do { a0[i] = ldsr<u32x4>(lds, pa0 + oa + 32 * (k)); b0[i] = ldsr<u32x4>(lds, pb0 + ob + 32 * (k) + 16 * ((k) >> 2)); \
    if (TWO) { a1[i] = ldsr<u32x4>(lds, pa1 + oa + 32 * (k)); b1[i] = ldsr<u32x4>(lds, pb1 + ob + 32 * (k) + 16 * ((k) >> 2)); } } while (0)
    CLD4(0, 2); SBAR(); CMM(1); SBAR();
    CLD4(1, 3); SBAR(); CMM(0); SBAR();
    CLD4(0, 4); SBAR(); CMM(1); SBAR();
    CLD4(1, 5); SBAR(); CMM(0); SBAR();
#undef CLD4
  }
#undef CLD
#undef CMM
  __builtin_amdgcn_s_setprio(3);
}
DI void conv_out(const f32x16& c, int d, int tl, char* ws, char* scratch, int lane) {
  const int r = lane & 31, h = lane >> 5;
#pragma unroll
  for (int g = 0; g < 4; ++g)
    ldsw<u32x2>(scratch, 2 * (16 * g + 8 * h + 4 * (r & 1) + 64 * (r >> 1)), u32x2{cvtpk(c[4 * g], c[4 * g + 1]), cvtpk(c[4 * g + 2], c[4 * g + 3])});
  const __amdgpu_buffer_rsrc_t rsY = __builtin_amdgcn_make_buffer_rsrc((void*)(ws + WS_YT + (size_t)d * (SEQ * 2) + 2048 * tl), 0, 2048, kRsrc3);
#pragma unroll
  for (int j = 0; j < 2; ++j) __builtin_amdgcn_raw_buffer_store_b128(ldsr<u32x4>(scratch, 1024 * j + 16 * lane), rsY, 1024 * j + 16 * lane, 0, 16);
}

DI void skip_tile(int rb, int hg, const u16* u, const u16* dskip, char* ws, u16* out, char* lds, int lane) {
  u32x4 y[9];
  tags_wait<32, 4>(ws + WS_YF + 8 * 32 * hg, get_tag(lds), lane);
  const __amdgpu_buffer_rsrc_t rsY = __builtin_amdgcn_make_buffer_rsrc((void*)(ws + WS_YT + (size_t)72 * hg * (SEQ * 2)), 0, 72 * SEQ * 2, kRsrc3);
#pragma unroll
  for (int j = 0; j < 9; ++j) {
    const int ch = 64 * j + lane, row = ch >> 3, c = ch & 7;
    y[j] = __builtin_amdgcn_raw_buffer_load_b128(rsY, row * (SEQ * 2) + 128 * rb + 16 * c, 0, 16);
  }
#pragma unroll
  for (int j = 0; j < 9; ++j) {
    const int ch = 64 * j + lane, row = ch >> 3, c = ch & 7;
    ldsw<u32x4>(lds, LDS_FA + row * 128 + 16 * (c ^ (wswz(row) << 1)), y[j]);
  }
  SBAR();
  u32x4 uu[9];
#pragma unroll
  for (int j = 0; j < 9; ++j) {
    const int ch = 64 * j + lane, row = ch / 9, c = ch - 9 * row;
    uu[j] = *(const u32x4*)(u + (size_t)(64 * rb + row) * DM + 72 * hg + 8 * c);
  }
  SBAR();
  const int g = lane >> 4, li = lane & 15, q = li >> 2, p = lane & 3;
#pragma unroll
  for (int j = 0; j < 9; ++j) {
    const int R0 = 8 * j + q, R1 = R0 + 4;
    const s16x4 a0 = ldstr(lds, LDS_FA + R0 * 128 + ((g ^ wswz(R0)) * 32) + 8 * p), a1 = ldstr(lds, LDS_FA + R1 * 128 + ((g ^ wswz(R1)) * 32) + 8 * p);
    ldsw<u32x4>(lds, LDS_FB + (16 * g + li) * 144 + 16 * j, __builtin_bit_cast(u32x4, __builtin_shufflevector(a0, a1, 0, 1, 2, 3, 4, 5, 6, 7)));
  }
#pragma unroll
  for (int j = 0; j < 9; ++j) {
    const int ch = 64 * j + lane, row = ch / 9, c = ch - 9 * row;
    const u32x4 yy = ldsr<u32x4>(lds, LDS_FB + ch * 16), dd = *(const u32x4*)(dskip + 72 * hg + 8 * c);
    u32x4 o;
#pragma unroll
    for (int k = 0; k < 4; ++k) {
      const unsigned sk = cvtpk(bflo(uu[j][k]) * bflo(dd[k]), bfhi(uu[j][k]) * bfhi(dd[k]));
      o[k] = cvtpk(bflo(yy[k]) + bflo(sk), bfhi(yy[k]) + bfhi(sk));
    }
    *(u32x4*)(out + (size_t)(64 * rb + row) * (2 * DM) + DM + 72 * hg + 8 * c) = o;
  }
}

DI void tail_part(const f32x16& oa, const f32x16& ob, float ma, float mb, int w, int rb, int hg, const u16* u, const u16* dskip, char* ws, u16* out, char* lds) {
  int lane;
  asm volatile("v_mbcnt_lo_u32_b32 %0, -1, 0\n\tv_mbcnt_hi_u32_b32 %0, -1, %0" : "=v"(lane));
  const int r = lane & 31, h = lane >> 5, pt = w & 1, hp = (w >> 1) & 1, kh = w >> 2;

  {
    const f32x16& give = kh ? oa : ob;
    const float gm = kh ? ma : mb;
    char* own = lds + LDS_CMB + w * 3328;
#pragma unroll
    for (int g = 0; g < 3; ++g) ldsw<f32x4>(own, g * 1024 + lane * 16, f32x4{give[4 * g], give[4 * g + 1], give[4 * g + 2], give[4 * g + 3]});
    ldsw<float>(own, 3072 + lane * 4, gm);
    TBAR(1);
    const char* oth = lds + LDS_CMB + (w ^ 4) * 3328;
    f32x16 keep = kh ? ob : oa;
    const float m1 = kh ? mb : ma, m2 = ldsr<float>(oth, 3072 + lane * 4);
    const float mm = fmaxf(m1, m2), f1 = EX2(m1 - mm), f2 = EX2(m2 - mm);
    float o[12];
#pragma unroll
    for (int g = 0; g < 3; ++g) {
      const f32x4 t = ldsr<f32x4>(oth, g * 1024 + lane * 16);
#pragma unroll
      for (int e = 0; e < 4; ++e) o[4 * g + e] = keep[4 * g + e] * f1 + t[e] * f2;
    }
    const float inv = 1.0f / half_lo(o[10]);
    const unsigned ot = LDS_OT + (32 * pt + r) * 144 + (2 * hp + kh) * 36;
    ldsw<unsigned>(lds, ot + 8 * h, cvtpk(o[0] * inv, o[1] * inv));
    ldsw<unsigned>(lds, ot + 8 * h + 4, cvtpk(o[2] * inv, o[3] * inv));
    ldsw<unsigned>(lds, ot + 16 + 8 * h, cvtpk(o[4] * inv, o[5] * inv));
    ldsw<unsigned>(lds, ot + 16 + 8 * h + 4, cvtpk(o[6] * inv, o[7] * inv));
    if (!h) ldsw<unsigned>(lds, ot + 32, cvtpk(o[8] * inv, o[9] * inv));
  }
  TBAR(2);
  const __amdgpu_buffer_rsrc_t rsO = rsrc(ws + WS_O + (size_t)64 * rb * (DM * 2), 64 * DM * 2);
#pragma unroll
  for (int n = 0; n < 2; ++n) {
    if (n == 0 || w == 0) {
      const int pair = 4 * (n ? 8 : w) + (lane >> 4), pt4 = pair / 9, c = pair - 9 * pt4, li = lane & 15, c8 = 9 * hg + c;
      __builtin_amdgcn_raw_buffer_store_b128(ldsr<u32x4>(lds, LDS_OT + (16 * pt4 + li) * 144 + 16 * c), rsO,
                                             ((pt4 * 18 + (c8 >> 2)) * 64 + (c8 & 3) * 16 + li) * 16, 0, SC1);
    }
  }
  asm volatile("s_waitcnt vmcnt(0)" ::: "memory");
  TBAR(3);
  STAMP(1, 9);
  if (w == 0) {
    const u64 tag = get_tag(lds);
    if (lane == 0) tag_store(ws + WS_OF + 8 * (8 * rb + hg), tag);
    tags_wait<8, 1>(ws + WS_OF + 64 * rb, tag, lane);
  }
  TBAR(4);
  STAMP(1, 10);
  lds_wait<1>(lds, F_WO, 3);
  const int li = lane & 15, g = lane >> 4, q = li >> 2, p = lane & 3, pt4 = w & 3, k2 = w >> 2;
  u32x4 xf[9];
#pragma unroll
  for (int ks = 0; ks < 9; ++ks) xf[ks] = __builtin_amdgcn_raw_buffer_load_b128(rsO, ((pt4 * 18 + 9 * k2 + ks) * 64 + lane) * 16, 0, SC1);
  STAMP(1, 11);
  f32x4 acc[5] = {};
  const unsigned wb = LDS_CV + (32 * 9 * k2 + 8 * g + q) * 144 + 8 * p;
  s16x4 wa[2][10];
#define WRD(buf, ks) do { _Pragma("unroll") for (int nt = 0; nt < 5; ++nt) { \
    wa[buf][2 * nt] = ldstr(lds, wb + 32 * (ks) * 144 + 32 * nt); wa[buf][2 * nt + 1] = ldstr(lds, wb + (32 * (ks) + 4) * 144 + 32 * nt); } } while (0)
  WRD(0, 0);
#pragma unroll
  for (int ks = 0; ks < 9; ++ks) {
    if (ks + 1 < 9) WRD((ks + 1) & 1, ks + 1);
    SBAR();
#pragma unroll
    for (int nt = 0; nt < 5; ++nt) {
      const auto a = __builtin_shufflevector(wa[ks & 1][2 * nt], wa[ks & 1][2 * nt + 1], 0, 1, 2, 3, 4, 5, 6, 7);
      acc[nt] = __builtin_amdgcn_mfma_f32_16x16x32_bf16(__builtin_bit_cast(bf16x8, a), __builtin_bit_cast(bf16x8, xf[ks]), acc[nt], 0, 0, 0);
    }
    SBAR();
  }
#undef WRD
  STAMP(1, 12);
  if (k2) {
#pragma unroll
    for (int nt = 0; nt < 5; ++nt) ldsw<f32x4>(lds, LDS_CMB + (pt4 * 5 + nt) * 1024 + lane * 16, acc[nt]);
  }
  TBAR(5);
  if (k2) return;
  u16* orow = out + (size_t)(64 * rb + 16 * pt4 + li) * (2 * DM) + 72 * hg + 4 * g;
#pragma unroll
  for (int nt = 0; nt < 5; ++nt) {
    const f32x4 t = ldsr<f32x4>(lds, LDS_CMB + (pt4 * 5 + nt) * 1024 + lane * 16);
    if (nt < 4 || lane < 32)
      *(u32x2*)(orow + 16 * nt) = u32x2{cvtpk(acc[nt][0] + t[0], acc[nt][1] + t[1]), cvtpk(acc[nt][2] + t[2], acc[nt][3] + t[3])};
  }
  asm volatile("fk_pf_end:");
  STAMP(1, 15);
}

DI void seg0_share(int i, const char* ws, char* lds, unsigned lds0, int lane) {
  const char* kvg = ws + WS_KV;
  lds_wait<LPOLL, 3>(lds, F_KVOK, 1);
#pragma unroll
  for (int j = 0; j < 4; ++j) {
    const int p = i + 6 * j;
    if (p < 19) glds16c(kvg + 1024 * p, 16 * lane, lds0 + 1024 * p);
    else if (p < 22) { if (p < 21 || lane < 16) glds16c(kvg + KV_HDR + 1024 * (p - 19), 16 * lane, lds0 + LDS_K0 + 1024 * (p - 19)); }
    else if (p == 22) { if (lane < 32) glds16c(ws + WS_KMAX, 16 * lane, lds0 + LDS_K0 + 3072); }
  }
  WAITV(0);
  FLAG_ADD(F_SEG0);
}
DI void l2_touch(const char* base, int line0, int nlines, unsigned lds0, int lane) {
  unsigned sv;
  if (line0 + lane < nlines)
    asm volatile("s_mov_b32 %0, m0\n\ts_mov_b32 m0, %3\n\ts_nop 0\n\tglobal_load_lds_dword %1, %2\n\ts_mov_b32 m0, %0"
                 : "=&s"(sv) : "v"(128 * (line0 + lane)), "s"(base), "s"(lds0 + LDS_GEN + 3 * GEN_AREA) : "memory");
}

__global__ void __launch_bounds__(1024)
fused_kernel(const u16* x, const u16* wq, const u16* wk, const u16* wv, const u16* wo, const u16* u, const u16* Are, const u16* Aim,
             const u16* Bre, const u16* Bim, const u16* Cre, const u16* Cim, const u16* logdt, const u16* dskip, char* ws, u16* out) {
  extern __shared__ __attribute__((aligned(16))) char lds[];
  const unsigned lds0 = (unsigned)(uintptr_t)lds;
  const char* pc0;
  asm volatile("s_getpc_b64 %0" : "=s"(pc0));
  const int b = BID, lane = TID & 63, w = rfl(TID >> 6);
  const int rb = (b & 7) + 8 * (b >> 6), hg = (b >> 3) & 7;
  STAMP(1, 0);
  if (TID < 64) ((LDSP unsigned*)((LDSP char*)lds + LDS_FLAG))[TID] = 0u;
  BAR();
  const char* kvg = ws + WS_KV;
  if (w >= 14) {
    __builtin_amdgcn_s_setprio(2);
    if (w == 15) {
      const char* pc1;
      int dend;
      asm volatile("s_getpc_b64 %0\nfk_pf_here:\n\ts_mov_b32 %1, fk_pf_end-fk_pf_here" : "=s"(pc1), "=s"(dend));
      const int len = (int)(pc1 - pc0) + (dend > 0 ? dend : 0);
      l2_touch(pc0, (b >> 3) + 31 * lane, len >> 7, lds0, lane);
    }
    if (w == 14) q_loader<4>(x, wq, rb, hg, lds, lds0, lane); else q_loader<5>(x, wq, rb, hg, lds, lds0, lane);
    seg0_share(w - 10, ws, lds, lds0, lane);
    return;
  }
  if (w == 13) {
    __builtin_amdgcn_s_setprio(2);
    __builtin_amdgcn_s_sleep(UT_DELAY);
    ut_wave(rb, hg, u, ws, lds, lane);
    STAMP(1, 15);
    return;
  }
  if (w == 12) {
    __builtin_amdgcn_s_setprio(3);
    const u64 tag = __hip_atomic_load((gu64*)(ws + WS_EPOCH), __ATOMIC_RELAXED, __HIP_MEMORY_SCOPE_AGENT) + 1;
    flag_set(lds, F_TAG, (unsigned)tag); flag_set(lds, F_TAG + 1, (unsigned)(tag >> 32)); flag_set(lds, F_TAGOK, 1);
    q_loader<3>(x, wq, rb, hg, lds, lds0, lane);
    STAMP(1, 3);
    seg0_share(3, ws, lds, lds0, lane);
    STAMP(1, 4);
    auto issue = [&](int s) {
#pragma unroll
      for (int j = 0; j < 19; ++j) glds16c(kvg + s * KV_SEG + 1024 * j, 16 * lane, lds0 + (s % 3) * KV_SEG + 1024 * j);
    };
    lds_wait<1>(lds, F_UDONE, 1);
    issue(1);
    lds_wait<1>(lds, F_QRD, 8);
    issue(2);
    WAITV(19);
    flag_set(lds, F_FULL, 2);
    WAITV(0);
    flag_set(lds, F_FULL, 3);
#pragma unroll 1
    for (int s = 3; s < 8; ++s) {
      done_wait<4>(lds, s - 2, lane);
      issue(s);
      WAITV(0);
      flag_set(lds, F_FULL, s + 1);
    }
    STAMP(1, 15);
    return;
  }
  if (w >= 8) {
    const int sw = w - 8, i = b >> 3, xc = b & 7;
    const int d0 = 72 * xc + 2 * i, dx = 72 * xc + 64 + (i >> 2), third = (i & 3) < 2;
    char* area = lds + LDS_GEN + (sw - 1) * GEN_AREA;
    if (sw == 0) {
      __builtin_amdgcn_s_setprio(3);
      kv_wave(b, rb, hg, x, ws, lds, lds0, lane);
      STAMP(1, 5);
    } else if (sw < 3 || third) {
      __builtin_amdgcn_s_setprio(GEN_PRIO);
      gen_wave(sw < 3 ? d0 + sw - 1 : dx, Are, Aim, Bre, Bim, Cre, Cim, logdt, area, lane);
      STAMP(1, 3);
    }
    __builtin_amdgcn_s_setprio(3);
    tags_wait<32, 4>(ws + WS_UF + 8 * 32 * xc, get_tag(lds), lane);
    lds_wait<2, 3>(lds, F_QDONE, 4);
    if (sw == 0) u_fill2<0>(d0, ws, lds, lds0, lane);
    else if (sw == 1) u_fill2<1>(d0, ws, lds, lds0, lane);
    else if (sw == 2) u_fill2<2>(d0, ws, lds, lds0, lane);
    else u_fill2<3>(d0, ws, lds, lds0, lane);
    if (sw != 0 && (sw < 3 || third)) build_copies(area, lds + LDS_CV + (sw - 1) * CV_CH, lane);
    if (third) {
      asm volatile("s_waitcnt lgkmcnt(0)" ::: "memory");
      FLAG_ADD(F_KCOP);
      lds_wait<LPOLL>(lds, F_KCOP, 4);
      char* c2 = lds + LDS_CV + 2 * CV_CH;
      const unsigned c20 = lds0 + LDS_CV + 2 * CV_CH;
      if (sw == 0) u_fill<0>(dx, ws, c2, c20, lane);
      else if (sw == 1) u_fill<1>(dx, ws, c2, c20, lane);
      else if (sw == 2) u_fill<2>(dx, ws, c2, c20, lane);
      else u_fill<3>(dx, ws, c2, c20, lane);
    }
    asm volatile("s_waitcnt vmcnt(0) lgkmcnt(0)" ::: "memory");
    FLAG_ADD(F_CFILL);
    STAMP(1, 6);
    {
      const int r = lane & 31, h = lane >> 5;
      const unsigned kb = LDS_CV + (3 - (r & 3)) * CV_K + 2 * (8 * h - 8 * (r >> 2));
      const unsigned ub = LDS_CV + CV_U + (r & 1) * CV_UP + 144 * (r >> 1) + 16 * h;
      const unsigned A1 = kb + 2 * 1080, A2 = kb + 2 * 56, B1 = ub, B3 = ub + 2304;
      char* scr = lds + LDS_SCR + sw * 2048;
      f32x16 c0 = {}, c1 = {};
      lds_wait<4>(lds, F_CFILL, 4);
      if (sw == 2) {
        conv_steps<true, false>(lds, A1, B1, A1 + CV_CH, B1 + CV_CH, c0, c1);
        conv_out(c0, d0, 0, ws, scr, lane);
        conv_out(c1, d0 + 1, 0, ws, scr, lane);
      } else if (sw < 2 || (i & 3) == 0) {
        const unsigned so = (sw < 2 ? sw : 2) * CV_CH;
        conv_steps<true, true>(lds, A2 + so, B1 + so, A1 + so, B3 + so, c0, c1);
#pragma unroll
        for (int k = 0; k < 16; ++k) c0[k] += c1[k];
        conv_out(c0, sw < 2 ? d0 + sw : dx, 1, ws, scr, lane);
      } else if (third) {
        conv_steps<false, false>(lds, A1 + 2 * CV_CH, B1 + 2 * CV_CH, 0, 0, c0, c1);
        conv_out(c0, dx, 0, ws, scr, lane);
      }
      STAMP(1, 7);
    }
    asm volatile("s_waitcnt vmcnt(0) lgkmcnt(0)" ::: "memory");
    unsigned arrived = 0;
    if (lane == 0) arrived = __hip_atomic_fetch_add((LDSP unsigned*)((LDSP char*)lds + LDS_FLAG) + F_CDONE, 1u, __ATOMIC_RELAXED, __HIP_MEMORY_SCOPE_WORKGROUP);
    if (rfl(arrived) == 3) { const u64 tag = get_tag(lds); if (lane == 0) tag_store(ws + WS_YF + 8 * (32 * xc + i), tag); }
    lds_wait<4>(lds, F_CDONE, 4);
    if (sw == 2) {
      skip_tile(rb, hg, u, dskip, ws, out, lds, lane);
      STAMP(1, 8);
    } else {
      const int q3 = sw == 3 ? 2 : sw;
      unsigned vo[3];
#pragma unroll
      for (int k = 0; k < 3; ++k) { const int ch = 64 * (3 * q3 + k) + lane, row = ch / 9, c = ch - 9 * row; vo[k] = row * (DM * 2) + c * 16; }
      const char* wb = (const char*)(wo + 72 * hg);
#pragma unroll 1
      for (int a = 0; a < 9; ++a) {
#pragma unroll
        for (int k = 0; k < 3; ++k) glds16s(wb + (size_t)a * 64 * (DM * 2), vo[k], lds0 + LDS_CV + 1024 * (9 * a + 3 * q3 + k));
      }
      STAMP(1, 8);
      WAITV(0);
      FLAG_ADD(F_WO);
    }
    STAMP(1, 15);
    return;
  }
  __builtin_amdgcn_s_setprio(3);
  if (w < 4) {
    q_compute(wk, wv, hg, lds, w, lane, ws);
  } else if (w == 4) {
    q_loader<6>(x, wq, rb, hg, lds, lds0, lane);
  } else {
    if (w == 5) q_loader<0>(x, wq, rb, hg, lds, lds0, lane);
    else if (w == 6) q_loader<1>(x, wq, rb, hg, lds, lds0, lane);
    else q_loader<2>(x, wq, rb, hg, lds, lds0, lane);
    STAMP(1, 6);
    seg0_share(w - 5, ws, lds, lds0, lane);
  }
  STAMP(1, 3);
  const int r = lane & 31, h = lane >> 5;
  const int pt = w & 1, hp = (w >> 1) & 1, kh = w >> 2;

  lds_wait<LPOLL, 3>(lds, F_QDONE, 4);
  const unsigned qo = LDS_QT + (32 * pt + r) * 192 + 2 * hp * 48;
  const u32x4 qam = ldsr<u32x4>(lds, qo + 16 * h), qbm = ldsr<u32x4>(lds, qo + 48 + 16 * h);
  const unsigned qa16 = ldsr<unsigned>(lds, qo + 32), qb16 = ldsr<unsigned>(lds, qo + 48 + 32);
  auto sq = [](unsigned u) { return bflo(u) * bflo(u) + bfhi(u) * bfhi(u); };
  float na = half_sum(sq(qam[0]) + sq(qam[1]) + sq(qam[2]) + sq(qam[3]) + (h ? 0.f : sq(qa16)));
  float nb = half_sum(sq(qbm[0]) + sq(qbm[1]) + sq(qbm[2]) + sq(qbm[3]) + (h ? 0.f : sq(qb16)));
  PIN(na); PIN(nb);
  lds_wait<LPOLL, 3>(lds, F_SEG0, 6);
  u32x4 kA = ldsr<u32x4>(lds, LDS_K0 + kh * 1152 + h * 512 + r * 16);
  u32x2 kAx = {ldsr<unsigned>(lds, LDS_K0 + kh * 1152 + 1024 + r * 4), 0x3f80u};
  const float km0 = ldsr<float>(lds, LDS_K0 + 3072 + 4 * lane), km1 = ldsr<float>(lds, LDS_K0 + 3072 + 256 + 4 * lane);
  asm volatile("s_waitcnt lgkmcnt(0)" ::: "memory");
  FLAG_ADD(F_QRD);
  const float kmax2 = wave_max(fmaxf(km0, km1));
  const float ba = rbf(__builtin_amdgcn_sqrtf(na * kmax2) * 1.01f + 1e-6f), bb = rbf(__builtin_amdgcn_sqrtf(nb * kmax2) * 1.01f + 1e-6f);
  const bool fast = __builtin_amdgcn_ballot_w64(!(ba <= FAST_BOUND && bb <= FAST_BOUND)) == 0;

  KVAddr ad;
  {
    const int vr = r <= 18 ? r : 19;
    const unsigned tb = kh * KV_HALF;
    ad.k = tb + h * 512 + r * 16; ad.kx = tb + 1024 + r * 4; ad.v = tb + 1152 + h * KV_VP + vr * 16;
  }
  __builtin_amdgcn_s_setprio(ATT_PRIO);
  f32x16 oa = {}, ob = {};
  float ma, mb;
  if (fast) {
    ma = 0.f; mb = 0.f;
    u32x2 qax = {h ? 0u : qa16, h ? 0u : (cvtpk(-ba, 0.f))}, qbx = {h ? 0u : qb16, h ? 0u : (cvtpk(-bb, 0.f))};
    u32x4 kB, vA0, vA1, vB0, vB1;
    u32x2 kBx = {0u, 0x3f80u};
    f32x16 zero16 = {}, sa, sb;
    u32x8 pa, pb = {};
    sa = MFMA16(kA, qam, zero16); sa = MFMA8(kAx, qax, sa);
    STAMP(1, 1);
    STAMP(1, 2);
    LDV(vB0, vB1, 0);
    unsigned off = 0;
#pragma unroll 1
    for (int s = 0; s < 8; ++s) {
      STEP(kA, kAx, kB, kBx, vB0, vB1, vA0, vA1, off);
      STEP(kB, kBx, kA, kAx, vA0, vA1, vB0, vB1, off + KV_TILE);
      STEP(kA, kAx, kB, kBx, vB0, vB1, vA0, vA1, off + 2 * KV_TILE);
      const unsigned full = flag_get(lds, F_FULL);
      STEP(kB, kBx, kA, kAx, vA0, vA1, vB0, vB1, off + 3 * KV_TILE);
      flag_set(lds, F_DONE + w, s + 1);
      off = off == 2 * KV_SEG ? 0 : off + KV_SEG;
      if (s < 7 && rfl(full) < s + 2) lds_wait<1>(lds, F_FULL, s + 2);
    }
    ob = MFMA16(vB0, lo4(pb), ob);
    ob = MFMA16(vB1, hi4(pb), ob);
  } else {
    ma = -INFINITY; mb = -INFINITY;
    const u32x2 qax = {h ? 0u : qa16, 0u}, qbx = {h ? 0u : qb16, 0u};
    kAx[1] = 0u;
#pragma unroll 1
    for (int t = 0; t < 32; ++t) {
      if (t && (t & 3) == 0) lds_wait<1>(lds, F_FULL, (t >> 2) + 1);
      const unsigned off = ((t >> 2) % 3) * KV_SEG + (t & 3) * KV_TILE;
      u32x4 kn, v0, v1; u32x2 knx = {0u, 0u};
      LDK(kn, knx, off); LDV(v0, v1, off);
      if ((t & 3) == 3) flag_set(lds, F_DONE + w, (t >> 2) + 1);
      slow_tile(kA, kAx, v0, v1, qam, qax, oa, ma);
      slow_tile(kA, kAx, v0, v1, qbm, qbx, ob, mb);
      kA = kn; kAx = knx;
    }
  }
  STAMP(1, 5);
  TBAR(0);
  tail_part(oa, ob, ma, mb, w, rb, hg, u, dskip, ws, out, lds);
}

#if defined(__HIP_DEVICE_COMPILE__)
#pragma clang attribute pop
#endif
extern "C" void kernel_launch(void* const* d_in, const int* in_sizes, int n_in, void* d_out, int out_size, void* d_ws, size_t ws_size,
                              hipStream_t stream) {
  (void)hipFuncSetAttribute((const void*)fused_kernel, hipFuncAttributeMaxDynamicSharedMemorySize, K_LDS);
  fused_kernel<<<256, 1024, K_LDS, stream>>>((const u16*)d_in[0], (const u16*)d_in[1], (const u16*)d_in[2], (const u16*)d_in[3], (const u16*)d_in[4],
                                            (const u16*)d_in[5], (const u16*)d_in[6], (const u16*)d_in[7], (const u16*)d_in[8], (const u16*)d_in[9],
                                            (const u16*)d_in[10], (const u16*)d_in[11], (const u16*)d_in[12], (const u16*)d_in[13], (char*)d_ws,
                                            (u16*)d_out);
}
```

```cpp
#if defined(__HIP_DEVICE_COMPILE__)
#pragma clang attribute push(__attribute__((target("no-packed-fp32-ops"))), apply_to = function)
#endif
#include <hip/hip_runtime.h>
#include <stdint.h>

typedef __bf16 bf16x2 __attribute__((ext_vector_type(2)));
typedef __bf16 bf16x8 __attribute__((ext_vector_type(8)));
typedef short s16x4 __attribute__((ext_vector_type(4)));
typedef float f32x2 __attribute__((ext_vector_type(2)));
typedef float f32x4 __attribute__((ext_vector_type(4)));
typedef float f32x16 __attribute__((ext_vector_type(16)));
typedef unsigned u32x2 __attribute__((ext_vector_type(2)));
typedef unsigned u32x4 __attribute__((ext_vector_type(4)));
typedef unsigned short u16;
#define DI __device__ __forceinline__
#define BID ((int)__builtin_amdgcn_workgroup_id_x())
#define TID ((int)__builtin_amdgcn_workitem_id_x())
#define LDSP __attribute__((address_space(3)))
#define SBAR() __builtin_amdgcn_sched_barrier(0)
#define PIN(x) asm volatile("" : "+v"(x))
#define MFMA16(a, b, c) __builtin_amdgcn_mfma_f32_32x32x16_bf16(__builtin_bit_cast(bf16x8, a), __builtin_bit_cast(bf16x8, b), c, 0, 0, 0)
#define MFMA8(a, b, c) __builtin_amdgcn_mfma_f32_32x32x8bf16_1k(__builtin_bit_cast(s16x4, a), __builtin_bit_cast(s16x4, b), c, 0, 0, 0)

constexpr int SEQ = 2048, DM = 576, DH = 18, NST = 128;
constexpr int KV_VP = 320;
constexpr int KV_TILE = 1152 + 4 * KV_VP;
constexpr int KV_HALF = 4 * KV_TILE, KV_SEG = 2 * KV_HALF;
constexpr int KV_HDR = 8 * KV_SEG;
constexpr int UT_ROW = 2064;
constexpr size_t WS_EPOCH = 0;
constexpr size_t WS_KVF = 256;
constexpr size_t WS_OF = WS_KVF + 8 * 2048;
constexpr size_t WS_UF = WS_OF + 2048;
constexpr size_t WS_YF = WS_UF + 2048;
constexpr size_t WS_KMAX = WS_YF + 2048;
constexpr size_t WS_KV = WS_KMAX + 1024;
constexpr size_t WS_O = WS_KV + 163840;
constexpr size_t WS_UT = WS_O + (size_t)SEQ * DM * 2;
constexpr size_t WS_YT = WS_UT + (size_t)DM * UT_ROW * 2;

DI unsigned cvtpk(float a, float b) { f32x2 v = {a, b}; return __builtin_bit_cast(unsigned, __builtin_convertvector(v, bf16x2)); }
DI float bflo(unsigned u) { return __uint_as_float(u << 16); }
DI float bfhi(unsigned u) { return __uint_as_float(u & 0xffff0000u); }
DI float bf2f(u16 x) { return __uint_as_float((unsigned)x << 16); }
DI float rbf(float x) { return bflo(cvtpk(x, x)); }
DI int crow(int reg, int h) { return (reg & 3) + 8 * (reg >> 2) + 4 * h; }
DI void glds16s(const void* sbase, unsigned voff, unsigned lds_base) {
  unsigned sv;
  asm volatile("s_mov_b32 %0, m0\n\ts_mov_b32 m0, %3\n\ts_nop 0\n\tglobal_load_lds_dwordx4 %1, %2\n\ts_mov_b32 m0, %0"
               : "=&s"(sv) : "v"(voff), "s"(sbase), "s"(lds_base) : "memory");
}
DI void glds16c(const void* sbase, unsigned voff, unsigned lds_base) {
  unsigned sv;
  asm volatile("s_mov_b32 %0, m0\n\ts_mov_b32 m0, %3\n\ts_nop 0\n\tglobal_load_lds_dwordx4 %1, %2 sc1\n\ts_mov_b32 m0, %0"
               : "=&s"(sv) : "v"(voff), "s"(sbase), "s"(lds_base) : "memory");
}
#define WAITV(n) asm volatile("s_waitcnt vmcnt(" #n ")" ::: "memory")
#define WAITV_BAR(n) asm volatile("s_waitcnt vmcnt(" #n ") lgkmcnt(0)\n\ts_barrier" ::: "memory")
#define SB() asm volatile("s_barrier" ::: "memory")
#define BAR() asm volatile("s_waitcnt lgkmcnt(0)\n\ts_barrier" ::: "memory")
#define STAMP(kern, i)
DI unsigned rfl(unsigned x) { return __builtin_amdgcn_readfirstlane(x); }
template <typename T> DI T ldsr(const char* lds, unsigned off) { return *(const LDSP T*)((const LDSP char*)lds + off); }
template <typename T> DI void ldsw(char* lds, unsigned off, T v) { *(LDSP T*)((LDSP char*)lds + off) = v; }
DI s16x4 ldstr(const char* lds, unsigned off) {
  typedef short v4i16_t __attribute__((ext_vector_type(4)));
  return __builtin_bit_cast(s16x4, __builtin_amdgcn_ds_read_tr16_b64_v4i16((LDSP v4i16_t*)((LDSP char*)lds + off)));
}
DI float half_sum(float v) {
  const auto t = __builtin_amdgcn_permlane32_swap(__float_as_uint(v), __float_as_uint(v), false, false);
  return __uint_as_float(t[0]) + __uint_as_float(t[1]);
}
DI float half_max(float v) {
  const auto t = __builtin_amdgcn_permlane32_swap(__float_as_uint(v), __float_as_uint(v), false, false);
  return fmaxf(__uint_as_float(t[0]), __uint_as_float(t[1]));
}
DI float half_lo(float v) {
  const auto t = __builtin_amdgcn_permlane32_swap(__float_as_uint(v), __float_as_uint(v), false, false);
  return __uint_as_float(t[0]);
}
template <int CTRL> DI float dpp_max(float v) {
  return fmaxf(v, __uint_as_float(__builtin_amdgcn_update_dpp(__float_as_uint(v), __float_as_uint(v), CTRL, 0xf, 0xf, false)));
}
DI float wave_max(float v) {
  v = dpp_max<0xb1>(v);
  v = dpp_max<0x4e>(v);
  v = dpp_max<0x141>(v);
  v = dpp_max<0x140>(v);
  const unsigned u = __float_as_uint(v);
  const float a = __uint_as_float(__builtin_amdgcn_readlane(u, 0)), b = __uint_as_float(__builtin_amdgcn_readlane(u, 16));
  const float c = __uint_as_float(__builtin_amdgcn_readlane(u, 32)), d = __uint_as_float(__builtin_amdgcn_readlane(u, 48));
  return fmaxf(fmaxf(a, b), fmaxf(c, d));
}

DI float xrow16_sum(float x) {
  const auto s = __builtin_amdgcn_permlane16_swap(__float_as_uint(x), __float_as_uint(x), false, false);
  return half_sum(__uint_as_float(s[0]) + __uint_as_float(s[1]));
}
template <int N> DI void waitv() { asm volatile("s_waitcnt vmcnt(%0)" ::"i"(N) : "memory"); }
DI int wswz(int row) { return ((row >> 1) & 1) | (((row >> 3) & 1) << 1); }
constexpr int kRsrc3 = 0x00020000;
typedef __attribute__((address_space(1))) unsigned gu32;
typedef __attribute__((address_space(1))) unsigned long long gu64;
typedef unsigned long long u64;
DI __amdgpu_buffer_rsrc_t rsrc(const void* p, int bytes) { return __builtin_amdgcn_make_buffer_rsrc((void*)p, 0, bytes, kRsrc3); }
constexpr int SC1 = 16;

#define CONV_MAP(sw) (sw)
#define NLD 1
#define RING_POLL 4
#define UT_WAIT F_KVPUB
#define UT_DELAY 16
#define QHOLD 99
#define QRAMP 3
#define GEN_PRIO 2
#define ATT_PRIO 0
#define KV_MODE 1
#define KV_DELAY 32
#define KV_POLL 8
#define LPOLL 1
constexpr int LDS_CV = 3 * KV_SEG;
constexpr int LDS_WKV = 0;
constexpr int LDS_UA = 23040;
constexpr int LDS_QT = 40960;
constexpr int LDS_K0 = 53248;
constexpr int GS = 20736;
constexpr int CV_CH = 31744, CV_K = 4416, CV_U = 17664, CV_UP = 7040;
constexpr int LDS_GEN = LDS_CV + 4 * GS;
constexpr int GEN_AREA = 6656, GEN_PITCH = 272, GEN_P = 21 * GEN_PITCH;
constexpr int LDS_CMB = 0;
constexpr int LDS_OT = 8 * 3328;
constexpr int LDS_FA = LDS_CV + 82944, LDS_FB = LDS_FA + 9216;
constexpr int LDS_SCR = LDS_CV + 3 * CV_CH;
constexpr int LDS_FLAG = LDS_SCR + 4 * 2048;
constexpr int K_LDS = LDS_FLAG + 256;
constexpr float FAST_BOUND = 30.f;
#define CONV_PRIO 0

constexpr int F_FULL = 0  , F_SEG0 = 3, F_TAGOK = 4, F_TAG = 5  , F_WKV = 7, F_DONE = 8  ,
              F_GF = 16  , F_GD = 32  , F_QDONE = 48, F_QRD = 49, F_KVOK = 50, F_UDONE = 51, F_KCOP = 52, F_WO = 53,
              F_KVPUB = 54, F_CFILL = 55, F_TB = 56  , F_CDONE = 62;
DI void flag_add(char* lds, int word) { __hip_atomic_fetch_add((LDSP unsigned*)((LDSP char*)lds + LDS_FLAG) + word, 1u, __ATOMIC_RELAXED, __HIP_MEMORY_SCOPE_WORKGROUP); }
DI void flag_set(char* lds, int word, unsigned v) {
  asm volatile("" ::: "memory");
  __hip_atomic_store((LDSP unsigned*)((LDSP char*)lds + LDS_FLAG) + word, v, __ATOMIC_RELAXED, __HIP_MEMORY_SCOPE_WORKGROUP);
  asm volatile("" ::: "memory");
}
DI unsigned flag_get(char* lds, int word) { return __hip_atomic_load((LDSP unsigned*)((LDSP char*)lds + LDS_FLAG) + word, __ATOMIC_RELAXED, __HIP_MEMORY_SCOPE_WORKGROUP); }
template <int SLEEP, int PRIO = -1> DI void lds_wait(char* lds, int word, unsigned need) {
  if (rfl(flag_get(lds, word)) < need) {
    if (PRIO >= 0) __builtin_amdgcn_s_setprio(0);
#pragma unroll 1
    for (unsigned spins = 0; spins < (1u << 20); ++spins) {
      __builtin_amdgcn_s_sleep(SLEEP);
      if (rfl(flag_get(lds, word)) >= need) break;
    }
    if (PRIO >= 0) __builtin_amdgcn_s_setprio(PRIO);
  }
  asm volatile("" ::: "memory");
}
template <int SLEEP> DI void done_wait(char* lds, unsigned need, int lane) {
#pragma unroll 1
  for (unsigned spins = 0; spins < (1u << 20); ++spins) {
    if (__builtin_amdgcn_ballot_w64(flag_get(lds, F_DONE + (lane & 7)) < need) == 0) break;
    __builtin_amdgcn_s_sleep(SLEEP);
  }
  asm volatile("" ::: "memory");
}
#define ONE_LANE(stmt) do { asm volatile("" ::: "memory"); if (lane == 0) { stmt; } asm volatile("" ::: "memory"); } while (0)

#define FLAG_ADD(word) ONE_LANE(flag_add(lds, word))
#define TBAR(k) do { asm volatile("s_waitcnt lgkmcnt(0)" ::: "memory"); FLAG_ADD(F_TB + (k)); lds_wait<0>(lds, F_TB + (k), 8); } while (0)
DI u64 get_tag(char* lds) {
  lds_wait<1>(lds, F_TAGOK, 1);
  return (u64)rfl(flag_get(lds, F_TAG)) | ((u64)rfl(flag_get(lds, F_TAG + 1)) << 32);
}
DI void tag_store(char* p, u64 tag) { __hip_atomic_store((gu64*)p, tag, __ATOMIC_RELAXED, __HIP_MEMORY_SCOPE_AGENT); }
template <int N, int SLEEP> DI void tags_wait(const char* p, u64 tag, int lane) {
#pragma unroll 1
  for (unsigned spins = 0; spins < (1u << 20); ++spins) {
    bool ok = true;
#pragma unroll
    for (int j = 0; j < (N + 63) / 64; ++j) {
      const int k = 64 * j + lane;
      ok &= __hip_atomic_load((gu64*)p + (k < N ? k : 0), __ATOMIC_RELAXED, __HIP_MEMORY_SCOPE_AGENT) == tag;
    }
    if (__builtin_amdgcn_ballot_w64(!ok) == 0) break;
    __builtin_amdgcn_s_sleep(SLEEP);
  }
  __builtin_amdgcn_fence(__ATOMIC_ACQUIRE, "wavefront");
  asm volatile("" ::: "memory");
}

template <int SLEEP> DI void tags256_wait(const char* p, u64 tag, int lane) {
  u64 a[4], c[4];
#define TLD(v) do { _Pragma("unroll") for (int j = 0; j < 4; ++j) v[j] = __hip_atomic_load((gu64*)p + 64 * j + lane, __ATOMIC_RELAXED, __HIP_MEMORY_SCOPE_AGENT); } while (0)
#define TOK(v) (__builtin_amdgcn_ballot_w64(!(v[0] == tag && v[1] == tag && v[2] == tag && v[3] == tag)) == 0)
  TLD(a);
#pragma unroll 1
  for (unsigned spins = 0; spins < (1u << 19); ++spins) {
    __builtin_amdgcn_s_sleep(SLEEP);
    TLD(c);
    if (TOK(a)) break;
    __builtin_amdgcn_s_sleep(SLEEP);
    TLD(a);
    if (TOK(c)) break;
  }
#undef TLD
#undef TOK
  __builtin_amdgcn_fence(__ATOMIC_ACQUIRE, "wavefront");
  asm volatile("" ::: "memory");
}

constexpr int NI = 7;
template <int IW> constexpr int q_cnt(int s) { return s > 7 ? 0 : s < 4 ? (18 - IW + NI - 1) / NI : (21 - IW + NI - 1) / NI; }
template <int IW, int S> DI void q_issue(const char* wb, const char* xb, int kvg, const unsigned (&vw)[3], const unsigned (&vx)[3], unsigned lds0, int lane) {
  const unsigned sb = lds0 + LDS_CV + (S & 3) * GS;
#pragma unroll
  for (int j = 0; j < q_cnt<IW>(S); ++j) {
    const int p = IW + NI * j;
    if constexpr (S >= 4) { if (p < 20 || lane < 16) glds16s(wb + (size_t)144 * (S - 4) * (DM * 2), vw[j], sb + 1024 * p); }
    else glds16s(xb + (size_t)16 * ((kvg + S) & 3) * (DM * 2), vx[j], sb + 1024 * p);
  }
}
constexpr int q_last(int S) { return S < 0 ? 1 : S < QRAMP ? S + 2 : (S + 3 > 7 ? 7 : S + 3); }
template <int IW> constexpr int q_pending(int S) { int n = 0; for (int k = S + 1; k <= q_last(S - 1); ++k) n += q_cnt<IW>(k); return n; }
template <int IW, int S, int K> DI void q_issue_upto(const char* wb, const char* xb, int kvg, const unsigned (&vw)[3], const unsigned (&vx)[3], char* lds, unsigned lds0, int lane) {
  if constexpr (K <= q_last(S) && K <= 7) {
    if constexpr (K == QHOLD) lds_wait<LPOLL, 3>(lds, F_KVPUB, 1);
    if constexpr (K >= 4) lds_wait<LPOLL, 3>(lds, F_GD + K - 4, K == 4 ? 2 : 1);
    q_issue<IW, K>(wb, xb, kvg, vw, vx, lds0, lane);
    q_issue_upto<IW, S, K + 1>(wb, xb, kvg, vw, vx, lds, lds0, lane);
  }
}
template <int IW, int S> DI void q_steps(const char* wb, const char* xb, int kvg, const unsigned (&vw)[3], const unsigned (&vx)[3], char* lds, unsigned lds0, int lane) {
  waitv<q_pending<IW>(S)>();
  FLAG_ADD(F_GF + S);
  q_issue_upto<IW, S, q_last(S - 1) + 1>(wb, xb, kvg, vw, vx, lds, lds0, lane);
  if constexpr (S + 1 < 8) q_steps<IW, S + 1>(wb, xb, kvg, vw, vx, lds, lds0, lane);
}
DI unsigned x_piece(int p, int lane) {
  const int n = 64 * p + lane, row = n / 72, c = n - 72 * row;
  return row * (DM * 2) + 16 * ((c & ~7) | ((c ^ (row >> 1)) & 7));
}
template <int IW> DI void q_loader(const u16* x, const u16* wq, int rb, int hg, char* lds, unsigned lds0, int lane) {
  unsigned vw[3], vx[3];
#pragma unroll
  for (int j = 0; j < 3; ++j) vx[j] = x_piece(IW + NI * j < 18 ? IW + NI * j : 0, lane);
  const char* wb = (const char*)(wq + 72 * hg);
  const char* xb = (const char*)(x + (size_t)64 * rb * DM);
  q_issue<IW, 0>(wb, xb, hg & 3, vw, vx, lds0, lane);
  q_issue<IW, 1>(wb, xb, hg & 3, vw, vx, lds0, lane);
#pragma unroll
  for (int j = 0; j < 3; ++j) { const int n = 64 * (IW + NI * j) + lane, row = n / 9, c = n - 9 * row; vw[j] = row * (DM * 2) + 16 * c; }
  q_steps<IW, 0>(wb, xb, hg & 3, vw, vx, lds, lds0, lane);
}
DI void x_bases(int lane, unsigned (&xb)[2]) {
  const int li = lane & 15, g = lane >> 4, xs = (li >> 1) & 7;
  xb[0] = li * (DM * 2) + 16 * (g ^ xs); xb[1] = li * (DM * 2) + 16 * ((4 + g) ^ xs);
}
#define XFRAG(base, ks) ldsr<u32x4>(lds, (base)[(ks) & 1] + 128 * ((ks) >> 1))

constexpr int QD0 = 5;
constexpr int QD1(int i) { return QD0 + 1 + i / 5 > 12 ? 12 : QD0 + 1 + i / 5; }
constexpr int qp_end(int i) { return i < 20 ? 20 : i < 45 ? 45 : i < 65 ? 65 : 90; }
constexpr int qp_ahead(int i) { return i + 1 + (QD0 + i / 5 > 12 ? 12 : QD0 + i / 5) < qp_end(i) ? i + 1 + (QD0 + i / 5 > 12 ? 12 : QD0 + i / 5) : qp_end(i); }
template <int FROM, int TO> DI void q_reads(const char* lds, unsigned wn, unsigned wx, u32x4 (&wa)[90]) {
  if constexpr (FROM < TO) {
    constexpr int ks = FROM / 5, nt = FROM % 5;
    const unsigned b_ = ((2 * ks) % 9 == 8 ? wx : wn) + ((2 * ks) / 9) * GS + ((2 * ks) % 9) * 2304 + 32 * nt;
    wa[FROM] = __builtin_bit_cast(u32x4, __builtin_shufflevector(ldstr(lds, b_), ldstr(lds, b_ + 4 * 144), 0, 1, 2, 3, 4, 5, 6, 7));
    q_reads<FROM + 1, TO>(lds, wn, wx, wa);
  }
}
template <int I, int NXT> DI void q_products(char* lds, unsigned wn, unsigned wx, const u32x4 (&xf)[18], u32x4 (&wa)[90], f32x4 (&acc)[5]) {
  if constexpr (I < 90) {
    constexpr bool first = I == 0 || I == 20 || I == 45 || I == 65;
    constexpr int start = first ? (I + QD1(I) < qp_end(I) ? I + QD1(I) : qp_end(I)) : NXT;
    if constexpr (first) {
      lds_wait<LPOLL, 3>(lds, F_GF + 4 + (2 * (I / 5) + 1) / 9, NI);
      q_reads<I, start>(lds, wn, wx, wa);
      SBAR();
    }
    acc[I % 5] = __builtin_amdgcn_mfma_f32_16x16x32_bf16(__builtin_bit_cast(bf16x8, wa[I]), __builtin_bit_cast(bf16x8, xf[I / 5]), acc[I % 5], 0, 0, 0);
    SBAR();
    constexpr int upto = qp_ahead(I) > start ? qp_ahead(I) : start;
    q_reads<start, upto>(lds, wn, wx, wa);
    SBAR();
    q_products<I + 1, upto>(lds, wn, wx, xf, wa, acc);
  }
}
DI void q_compute(const u16* wk, const u16* wv, int hg, char* lds, int w, int lane, char* ws) {
  {
    typedef unsigned u32x3 __attribute__((ext_vector_type(3)));
    const char* src = (const char*)(hg >> 2 ? wv : wk);
    u32x3 t[7];
#pragma unroll
    for (int j = 0; j < 7; ++j) { const int i = w + 4 * j; __builtin_memcpy(&t[j], src + 12 * (64 * (i < 27 ? i : 0) + lane), 12); }
#pragma unroll
    for (int j = 0; j < 7; ++j) {
      const int i = w + 4 * j, e = 64 * i + lane, k = e / 3, c = e - 3 * k;
      if (i < 27) { const unsigned o = LDS_WKV + 40 * k + 12 * c; ldsw<unsigned>(lds, o, t[j][0]); ldsw<unsigned>(lds, o + 4, t[j][1]); ldsw<unsigned>(lds, o + 8, t[j][2]); }
    }
    asm volatile("s_waitcnt lgkmcnt(0)" ::: "memory");
    FLAG_ADD(F_WKV);
    STAMP(1, 6);
  }
  const int li = lane & 15, g = lane >> 4, q = li >> 2, p = lane & 3, st = (w - hg) & 3;
  u32x4 xf[18];
  {
    unsigned xb[2];
    x_bases(lane, xb);
    xb[0] += LDS_CV + st * GS; xb[1] += LDS_CV + st * GS;
    lds_wait<LPOLL, 3>(lds, F_GF + st, NI);
#pragma unroll
    for (int ks = 0; ks < 18; ++ks) xf[ks] = XFRAG(xb, ks);
    asm volatile("s_waitcnt lgkmcnt(0)" ::: "memory");
    FLAG_ADD(F_GD + st);
  }
  STAMP(1, 7);
  f32x4 acc[5] = {};
  const unsigned wbase = LDS_CV + (8 * (g & 1) + q) * 144 + 8 * p;
  const unsigned wn = wbase + (g >> 1) * 2304;
  const unsigned wx = wbase + (g >> 1) * (GS - 8 * 2304);
  u32x4 wa[90];
  q_products<0, 0>(lds, wn, wx, xf, wa, acc);
#pragma unroll
  for (int nt = 0; nt < 5; ++nt) {
    const unsigned a = cvtpk(acc[nt][0], acc[nt][1]), b2 = cvtpk(acc[nt][2], acc[nt][3]);
#pragma unroll
    for (int e = 0; e < 4; ++e) {
      const int n = 16 * nt + 4 * g + e, hh = n / 18;
      const unsigned v = e < 2 ? a : b2;
      if (nt < 4 || g < 2) ldsw<u16>(lds, LDS_QT + (16 * w + li) * 192 + hh * 48 + 2 * (n - 18 * hh), (u16)((e & 1) ? v >> 16 : v));
    }
  }
  asm volatile("s_waitcnt lgkmcnt(0)" ::: "memory");
  FLAG_ADD(F_QDONE);
}

template <bool ISV> DI void kv_products(const char* lds, int lane, const u32x4 (&xf)[18], f32x4 (&acc)[2]) {
  const int li = lane & 15, g = lane >> 4, q = li >> 2, p = lane & 3;
  f32x4 a0[2] = {}, a1[2] = {};
  const unsigned wo_ = LDS_WKV + (8 * g + q) * 40 + 8 * p;
  s16x4 wr[4][4];
#define WKV_RD(ks) do { _Pragma("unroll") for (int e = 0; e < 4; ++e) wr[(ks) & 3][e] = ldstr(lds, wo_ + 32 * (ks) * 40 + (e >> 1) * 32 + (e & 1) * 160); } while (0)
  WKV_RD(0); WKV_RD(1); WKV_RD(2);
#pragma unroll
  for (int ks = 0; ks < 18; ++ks) {
    if (ks + 3 < 18) WKV_RD(ks + 3);
    SBAR();
#pragma unroll
    for (int nt = 0; nt < 2; ++nt) {
      const auto wfr = __builtin_shufflevector(wr[ks & 3][2 * nt], wr[ks & 3][2 * nt + 1], 0, 1, 2, 3, 4, 5, 6, 7);
      f32x4& a = (ks & 1) ? a1[nt] : a0[nt];
      if (ISV) a = __builtin_amdgcn_mfma_f32_16x16x32_bf16(__builtin_bit_cast(bf16x8, xf[ks]), __builtin_bit_cast(bf16x8, wfr), a, 0, 0, 0);
      else a = __builtin_amdgcn_mfma_f32_16x16x32_bf16(__builtin_bit_cast(bf16x8, wfr), __builtin_bit_cast(bf16x8, xf[ks]), a, 0, 0, 0);
    }
    SBAR();
  }
#undef WKV_RD
#pragma unroll
  for (int e = 0; e < 4; ++e) { acc[0][e] = a0[0][e] + a1[0][e]; acc[1][e] = a0[1][e] + a1[1][e]; }
}
DI void kv_wave(int b, int rb, int hg, const u16* x, char* ws, char* lds, unsigned lds0, int lane) {
  const int li = lane & 15, g = lane >> 4;
  const bool isv = hg >> 2;
  u32x4 xf[18];
  lds_wait<LPOLL, 3>(lds, F_GF + 0, NI);
  unsigned xb[2];
  x_bases(lane, xb);
  xb[0] += LDS_CV; xb[1] += LDS_CV;
#pragma unroll
  for (int ks = 0; ks < 18; ++ks) xf[ks] = XFRAG(xb, ks);
  asm volatile("s_waitcnt lgkmcnt(0)" ::: "memory");
  FLAG_ADD(F_GD + 0);
  lds_wait<LPOLL, 3>(lds, F_WKV, 4);
  STAMP(1, 2);
  f32x4 acc[2];
  if (isv) kv_products<true>(lds, lane, xf, acc); else kv_products<false>(lds, lane, xf, acc);
  STAMP(1, 4);
  const int kt = 2 * rb + ((hg & 3) >> 1), s = hg & 1, kh = kt >> 5, t = kt & 31;
  const __amdgpu_buffer_rsrc_t rs = rsrc(ws + WS_KV, 163840);
  if (!isv) {
    const int img = t ? ((t - 1) >> 2) * KV_SEG + kh * KV_HALF + ((t - 1) & 3) * KV_TILE : KV_HDR + kh * 1152;
    const float sc = 0.23570226039551584f * 1.4426950408889634f;
    const int key = 16 * s + li, e = (key & 0x13) | ((key & 4) << 1) | ((key & 8) >> 1);
    const unsigned d0 = cvtpk(acc[0][0] * sc, acc[0][1] * sc), d1 = cvtpk(acc[0][2] * sc, acc[0][3] * sc);
    const unsigned d2 = g ? 0u : cvtpk(acc[1][0] * sc, acc[1][1] * sc);
    __builtin_amdgcn_raw_buffer_store_b64(u32x2{d0, d1}, rs, img + (g >> 1) * 512 + e * 16 + 8 * (g & 1), 0, SC1);
    if (g == 0) __builtin_amdgcn_raw_buffer_store_b32(d2, rs, img + 1024 + e * 4, 0, SC1);
    float n2 = bflo(d0) * bflo(d0) + bfhi(d0) * bfhi(d0) + bflo(d1) * bflo(d1) + bfhi(d1) * bfhi(d1) + bflo(d2) * bflo(d2) + bfhi(d2) * bfhi(d2);
    n2 = wave_max(xrow16_sum(n2));
    if (lane == 0) __hip_atomic_store((gu32*)(ws + WS_KMAX) + 4 * rb + hg, __float_as_uint(n2), __ATOMIC_RELAXED, __HIP_MEMORY_SCOPE_AGENT);
  } else {
    const int img = (t >> 2) * KV_SEG + kh * KV_HALF + (t & 3) * KV_TILE + 1152 + (2 * s + (g >> 1)) * KV_VP + 8 * (g & 1);
    __builtin_amdgcn_raw_buffer_store_b64(u32x2{cvtpk(acc[0][0], acc[0][1]), cvtpk(acc[0][2], acc[0][3])}, rs, img + li * 16, 0, SC1);
    u32x2 v = {cvtpk(acc[1][0], acc[1][1]), cvtpk(acc[1][2], acc[1][3])};
    if (li == 2) v = u32x2{0x3f803f80u, 0x3f803f80u};
    if (li == 3) v = u32x2{0u, 0u};
    if (li < 4) __builtin_amdgcn_raw_buffer_store_b64(v, rs, img + (16 + li) * 16, 0, SC1);
  }
  const u64 tag = get_tag(lds);
  WAITV(0);
  if (lane < 8) tag_store(ws + WS_KVF + 2048 * lane + 8 * b, tag);
  flag_set(lds, F_KVPUB, 1);
  STAMP(1, 3);
  __builtin_amdgcn_s_sleep(KV_DELAY);
  tags_wait<256, KV_POLL>(ws + WS_KVF + 2048 * (b & 7), tag, lane);
  flag_set(lds, F_KVOK, 1);
  if (b == 0 && lane == 0) tag_store(ws + WS_EPOCH, tag);
}

DI void ut_wave(int rb, int hg, const u16* u, char* ws, char* lds, int lane) {
  u32x4 v[9];
#pragma unroll
  for (int j = 0; j < 9; ++j) { const int ch = 64 * j + lane, row = ch / 9, c = ch - 9 * row; v[j] = *(const u32x4*)(u + (size_t)(64 * rb + row) * DM + 72 * hg + 8 * c); }
#pragma unroll
  for (int j = 0; j < 9; ++j) ldsw<u32x4>(lds, LDS_UA + (64 * j + lane) * 16, v[j]);
  const __amdgpu_buffer_rsrc_t rs = rsrc(ws + WS_UT + (size_t)72 * hg * (UT_ROW * 2), 72 * UT_ROW * 2);
  const int g = lane >> 4, li = lane & 15, q = li >> 2, p = lane & 3;
  u32x4 t[10];
#pragma unroll
  for (int i = 0; i < 10; ++i) {
    const unsigned ad = LDS_UA + (8 * (4 * (i & 1) + g) + q) * 144 + 32 * (i >> 1) + 8 * p;
    t[i] = __builtin_bit_cast(u32x4, __builtin_shufflevector(ldstr(lds, ad), ldstr(lds, ad + 4 * 144), 0, 1, 2, 3, 4, 5, 6, 7));
  }
  asm volatile("s_waitcnt lgkmcnt(0)" ::: "memory");
  FLAG_ADD(F_UDONE);
  lds_wait<4>(lds, UT_WAIT, 1);
#pragma unroll
  for (int i = 0; i < 10; ++i)
    if (i < 8 || li < 8) __builtin_amdgcn_raw_buffer_store_b128(t[i], rs, (16 * (i >> 1) + li) * (UT_ROW * 2) + 16 + 128 * rb + 16 * (4 * (i & 1) + g), 0, SC1);
  if (rb == 0 || rb == 31) {
#pragma unroll
    for (int j = 0; j < 2; ++j)
      if (64 * j + lane < 72) __builtin_amdgcn_raw_buffer_store_b128(u32x4{0, 0, 0, 0}, rs, (64 * j + lane) * (UT_ROW * 2) + (rb ? 16 + SEQ * 2 : 0), 0, SC1);
  }
  const u64 tag = get_tag(lds);
  WAITV(0);
  if (lane == 0) tag_store(ws + WS_UF + 8 * (32 * hg + rb), tag);
}

DI f32x2 cmul(f32x2 a, f32x2 b) { return f32x2{__builtin_fmaf(a[0], b[0], -(a[1] * b[1])), __builtin_fmaf(a[0], b[1], a[1] * b[0])}; }
DI f32x2 cexp_l(float zr, float zi, float l) {
  const float mag = __builtin_amdgcn_exp2f((zr * l) * 1.4426950408889634f);
  const float rev = __builtin_amdgcn_fractf((zi * l) * 0.15915494309189535f);
  return f32x2{mag * __builtin_amdgcn_cosf(rev), mag * __builtin_amdgcn_sinf(rev)};
}
DI f32x2 cis2(float e2, float rev) {
  const float mag = __builtin_amdgcn_exp2f(e2), f = __builtin_amdgcn_fractf(rev);
  return f32x2{mag * __builtin_amdgcn_cosf(f), mag * __builtin_amdgcn_sinf(f)};
}
DI void gen_wave(int d, const u16* Are, const u16* Aim, const u16* Bre, const u16* Bim, const u16* Cre, const u16* Cim,
                 const u16* logdt, char* lds, int lane) {
  const int r = lane & 31, h = lane >> 5;
  u16 pr[2][6];
#pragma unroll
  for (int pass = 0; pass < 2; ++pass) {
    const int idx = d * NST + 64 * pass + lane;
    pr[pass][0] = Are[idx]; pr[pass][1] = Aim[idx]; pr[pass][2] = Bre[idx]; pr[pass][3] = Bim[idx]; pr[pass][4] = Cre[idx]; pr[pass][5] = Cim[idx];
  }
  const float dt = rbf(expf(bf2f(logdt[d])));
  const float fm = (float)r;
  f32x16 acc = {};
#pragma unroll 1
  for (int pass = 0; pass < 2; ++pass) {
    const u16 p0 = pass ? pr[1][0] : pr[0][0], p1 = pass ? pr[1][1] : pr[0][1], p2_ = pass ? pr[1][2] : pr[0][2];
    const u16 p3_ = pass ? pr[1][3] : pr[0][3], p4_ = pass ? pr[1][4] : pr[0][4], p5 = pass ? pr[1][5] : pr[0][5];
    const float zr = dt * bf2f(p0), zi = dt * bf2f(p1);
    const float dBr = dt * bf2f(p2_), dBi = dt * bf2f(p3_);
    const float cr = bf2f(p4_), ci = bf2f(p5);
    const f32x2 c = {cr * dBr - ci * dBi, cr * dBi + ci * dBr};
    ldsw<f32x2>(lds, GEN_P + 8 * lane, f32x2{zr * (8.f * 1.4426950408889634f), zi * (-8.f * 0.15915494309189535f)});
    const f32x2 w1 = cexp_l(zr, zi, 1.f), w256 = cexp_l(zr, zi, 256.f);
#define PUTY(col, v) ldsw<unsigned>(lds, (col) * GEN_PITCH + 4 * lane, cvtpk((v)[0], (v)[1]))
    f32x2 y[8];
    y[0] = c;
#pragma unroll
    for (int i = 1; i < 8; ++i) y[i] = cmul(y[i - 1], w1);
#pragma unroll
    for (int i = 0; i < 8; ++i) PUTY(i, y[i]);
#pragma unroll
    for (int i = 0; i < 4; ++i) { const f32x2 t = cmul(y[2 * i], w256); PUTY(8 + i, t); }
    const f32x2 p2 = cmul(w256, w256), p3 = cmul(p2, w256), p4 = cmul(p2, p2);
    { f32x2 t = cmul(y[0], p2); PUTY(12, t); t = cmul(y[4], p2); PUTY(13, t); }
    { f32x2 t = cmul(y[0], p3); PUTY(14, t); t = cmul(y[4], p3); PUTY(15, t); }
    { const f32x2 t4 = cmul(y[0], p4); PUTY(16, t4);
      f32x2 t = cmul(t4, w256); PUTY(17, t); t = cmul(t4, p2); PUTY(18, t); t = cmul(t4, p3); PUTY(19, t); t = cmul(t4, p4); PUTY(20, t); }
#undef PUTY
    asm volatile("" ::: "memory");
    const int yr = r <= 20 ? r : r - 16;
#pragma unroll 2
    for (int ks = 0; ks < 8; ++ks) {
      const f32x4 z0 = ldsr<f32x4>(lds, GEN_P + 64 * ks + 32 * h), z1 = ldsr<f32x4>(lds, GEN_P + 64 * ks + 32 * h + 16);
      const u32x4 fb = ldsr<u32x4>(lds, yr * GEN_PITCH + 32 * ks + 16 * h);
      const f32x2 a0 = cis2(z0[0] * fm, z0[1] * fm), a1 = cis2(z0[2] * fm, z0[3] * fm), a2 = cis2(z1[0] * fm, z1[1] * fm), a3 = cis2(z1[2] * fm, z1[3] * fm);
      const u32x4 fa = {cvtpk(a0[0], a0[1]), cvtpk(a1[0], a1[1]), cvtpk(a2[0], a2[1]), cvtpk(a3[0], a3[1])};
      acc = MFMA16(fa, fb, acc);
    }
    asm volatile("" ::: "memory");
  }
  const int c = r;
  const int hi = c < 8 ? c : c < 12 ? 256 + 2 * (c - 8) : c < 16 ? 512 + 256 * ((c - 12) >> 1) + 4 * ((c - 12) & 1) : 1024 + 256 * (c - 16);
  if (c < 20) {
#pragma unroll
    for (int i = 0; i < 16; ++i) ldsw<u16>(lds, 2 * (8 * crow(i, h) + hi), (u16)cvtpk(acc[i], acc[i]));
  } else if (c == 20 && h == 0) {
    ldsw<u16>(lds, 2 * 2048, (u16)cvtpk(acc[0], acc[0]));
  }
  asm volatile("" ::: "memory");
}
DI void build_copies(const char* kc, char* cl, int lane) {
  auto rd = [&](int l) -> unsigned { return ldsr<u16>(kc, 2 * l); };
  auto pk = [](unsigned lo, unsigned hi) -> unsigned { return lo | (hi << 16); };
  auto put2 = [&](int par, int y, unsigned v0, unsigned v1) {
    ldsw<u32x2>(cl, par * CV_K + 4 * y, u32x2{v0, v1});
    ldsw<unsigned>(cl, (par + 2) * CV_K + 4 * y - 4, v0); ldsw<unsigned>(cl, (par + 2) * CV_K + 4 * y, v1);
  };
  const int L = lane;
  {
    const unsigned e0 = rd(4 * L), e1 = rd(4 * L + 1), e2 = rd(4 * L + 2), e3 = rd(4 * L + 3);
    const unsigned pv = L ? rd(4 * L - 1) : 0u;
    put2(0, 1028 - 2 * L, pk(e3, e2), pk(e1, e0));
    put2(1, 1028 - 2 * L, pk(e2, e1), pk(e0, pv));
  }
  {
    const int j0 = 256 + 4 * L;
    const unsigned a = rd(j0), b = rd(j0 + 2), c = rd(j0 + 4), pv = L ? a : rd(255);
    put2(0, 1028 - j0 / 2, pk(c, b), pk(a, a));
    put2(1, 1028 - j0 / 2, pk(b, a), pk(a, pv));
  }
  {
    const int j0 = 512 + 8 * L, y = 1026 - j0 / 2;
    const unsigned a = rd(j0), b = rd(j0 + 4), c = rd(j0 + 8);
    const unsigned aa = pk(a, a), bb = pk(b, b), cc = pk(c, c);
    put2(0, y, cc, bb);        put2(0, y + 2, pk(b, a), aa);
    put2(1, y, pk(c, b), bb);  put2(1, y + 2, aa, aa);
  }
  {
    const int j0 = 1024 + 16 * L, y = 1022 - j0 / 2;
    const unsigned a = rd(j0), b = rd(j0 + 8), c = rd(j0 + 16);
    const unsigned aa = pk(a, a), bb = pk(b, b), cc = pk(c, c);
    put2(0, y, cc, cc);        put2(0, y + 2, bb, bb);  put2(0, y + 4, bb, pk(b, a));  put2(0, y + 6, aa, aa);
    put2(1, y, cc, pk(c, b));  put2(1, y + 2, bb, bb);  put2(1, y + 4, bb, aa);        put2(1, y + 6, aa, aa);
    if (L == 63) {
      ldsw<unsigned>(cl, CV_K + 4 * 5, pk(0u, c)); ldsw<unsigned>(cl, 3 * CV_K + 4 * 4, pk(0u, c));
    }
  }
  if (L < 6) { ldsw<unsigned>(cl, 4 * L, 0u); if (L) ldsw<unsigned>(cl, 2 * CV_K + 4 * L - 4, 0u); }
  if (L < 5) { ldsw<unsigned>(cl, CV_K + 4 * L, 0u); if (L) ldsw<unsigned>(cl, 3 * CV_K + 4 * L - 4, 0u); }
  if (L < 42) {
    ldsw<unsigned>(cl, 4 * (1030 + L), 0u); ldsw<unsigned>(cl, CV_K + 4 * (1030 + L), 0u);
    ldsw<unsigned>(cl, 2 * CV_K + 4 * (1029 + L), 0u); ldsw<unsigned>(cl, 3 * CV_K + 4 * (1029 + L), 0u);
  }
}
template <int SW> DI void u_fill(int d, const char* ws, char* cl, unsigned cl0, int lane) {
  const char* ur = ws + WS_UT + (size_t)d * (UT_ROW * 2);
#pragma unroll
  for (int item = SW; item < 10; item += 4) {
    const int bb = item / 5, part = item - 5 * bb, sg = 136 + part * 64 + lane, qc = sg - sg / 9, s0 = 8 * qc - 976 + 4 * bb;
    if (sg <= 424) glds16c(ur, sg % 9 == 8 ? 0u : 2 * (8 + s0), cl0 + CV_U + bb * CV_UP + (136 + part * 64) * 16);
  }
  if (SW < 2) {
#pragma unroll
    for (int j = 0; j < 3; ++j)
      if (64 * j + lane < 136) ldsw<u32x4>(cl, CV_U + SW * CV_UP + (64 * j + lane) * 16, u32x4{0, 0, 0, 0});
  }
}
template <int SW> DI void u_fill2(int d0, const char* ws, char* lds, unsigned lds0, int lane) {
  u_fill<SW>(d0, ws, lds + LDS_CV, lds0 + LDS_CV, lane);
  u_fill<(SW + 2) & 3>(d0 + 1, ws, lds + LDS_CV + CV_CH, lds0 + LDS_CV + CV_CH, lane);
}

struct KVAddr { unsigned k, kx, v; };
#define LDK(dst, dstx, t_off) do { dst = ldsr<u32x4>(lds, ad.k + (t_off)); dstx[0] = ldsr<unsigned>(lds, ad.kx + (t_off)); } while (0)
#define LDV(d0, d1, t_off) do { d0 = ldsr<u32x4>(lds, ad.v + (t_off)); d1 = ldsr<u32x4>(lds, ad.v + 2 * KV_VP + (t_off)); } while (0)
#define EX2(x) __builtin_amdgcn_exp2f(x)
#define GRP(MF, S, P, i) do { MF; SBAR(); \
    { const float e0 = EX2(S[4 * (i)]), e1 = EX2(S[4 * (i) + 1]), e2 = EX2(S[4 * (i) + 2]), e3 = EX2(S[4 * (i) + 3]); \
      unsigned t0_ = cvtpk(e0, e1), t1_ = cvtpk(e2, e3); PIN(t0_); PIN(t1_); P[2 * (i)] = t0_; P[2 * (i) + 1] = t1_; } \
    SBAR(); } while (0)
typedef unsigned u32x8 __attribute__((ext_vector_type(8)));
DI u32x4 lo4(const u32x8& p) { return __builtin_shufflevector(p, p, 0, 1, 2, 3); }
DI u32x4 hi4(const u32x8& p) { return __builtin_shufflevector(p, p, 4, 5, 6, 7); }

#define STEP(Kc, Kcx, Kn, Knx, Vp0, Vp1, Vc0, Vc1, off) do { \
    LDK(Kn, Knx, (off)); LDV(Vc0, Vc1, (off)); SBAR(); \
    GRP(sb = MFMA16(Kc, qbm, zero16), sa, pa, 0); \
    GRP(ob = MFMA16(Vp0, lo4(pb), ob), sa, pa, 1); \
    GRP(sb = MFMA8(Kcx, qbx, sb), sa, pa, 2); \
    GRP(ob = MFMA16(Vp1, hi4(pb), ob), sa, pa, 3); \
    GRP(sa = MFMA16(Kn, qam, zero16), sb, pb, 0); \
    GRP(oa = MFMA16(Vc0, lo4(pa), oa), sb, pb, 1); \
    GRP(sa = MFMA8(Knx, qax, sa), sb, pb, 2); \
    GRP(oa = MFMA16(Vc1, hi4(pa), oa), sb, pb, 3); \
  } while (0)

DI void slow_tile(const u32x4& kf, const u32x2& kx, const u32x4& v0, const u32x4& v1, const u32x4& qm, const u32x2& qx, f32x16& o, float& m) {
  f32x16 s = {};
  s = MFMA16(kf, qm, s);
  s = MFMA8(kx, qx, s);
  float mx = s[0];
#pragma unroll
  for (int i = 1; i < 16; ++i) mx = fmaxf(mx, s[i]);
  mx = half_max(mx);
  const float mn = fmaxf(m, mx);
  const float alpha = EX2(m - mn);
  m = mn;
#pragma unroll
  for (int i = 0; i < 16; ++i) o[i] *= alpha;
  u32x8 p;
#pragma unroll
  for (int i = 0; i < 8; ++i) p[i] = cvtpk(EX2(s[2 * i] - mn), EX2(s[2 * i + 1] - mn));
  o = MFMA16(v0, lo4(p), o);
  o = MFMA16(v1, hi4(p), o);
}

template <bool TWO, bool SKIP0> DI void conv_steps(const char* lds, unsigned pa0, unsigned pb0, unsigned pa1, unsigned pb1, f32x16& c0, f32x16& c1) {
  __builtin_amdgcn_s_setprio(CONV_PRIO);
  u32x4 a0[2], b0[2], a1[2], b1[2];
#define CLD(i, n) do { a0[i] = ldsr<u32x4>(lds, pa0 + 32 * (n)); b0[i] = ldsr<u32x4>(lds, pb0 + 32 * (n) + 16 * ((n) >> 2)); \
    if (TWO) { a1[i] = ldsr<u32x4>(lds, pa1 + 32 * (n)); b1[i] = ldsr<u32x4>(lds, pb1 + 32 * (n) + 16 * ((n) >> 2)); } } while (0)
#define CMM(i) do { c0 = MFMA16(a0[i], b0[i], c0); if (TWO) c1 = MFMA16(a1[i], b1[i], c1); } while (0)
  CLD(0, 0); CLD(1, 1);
  c0 = MFMA16(a0[0], b0[0], c0);
  if (TWO && !SKIP0) c1 = MFMA16(a1[0], b1[0], c1);
  SBAR();
  typedef const LDSP char* lptr;
  lptr qa0 = (lptr)lds + pa0, qb0 = (lptr)lds + pb0, qa1 = (lptr)lds + pa1, qb1 = (lptr)lds + pb1;
  PIN(qa0); PIN(qb0);
  if (TWO) { PIN(qa1); PIN(qb1); }
#define LRD(p, o) (*(const LDSP u32x4*)((p) + (o)))
#define CLD4(i, k) do { a0[i] = LRD(qa0, 32 * (k)); b0[i] = LRD(qb0, 32 * (k) + 16 * ((k) >> 2)); \
    if (TWO) { a1[i] = LRD(qa1, 32 * (k)); b1[i] = LRD(qb1, 32 * (k) + 16 * ((k) >> 2)); } } while (0)
#pragma unroll
  for (int n = 1; n < 65; n += 2) {
    CLD4(0, n + 1); SBAR(); CMM(1); SBAR();
    if (n + 2 < 65) CLD4(1, n + 2);
    SBAR(); CMM(0); SBAR();
  }
#undef CLD4
#undef LRD
#undef CLD
#undef CMM
  __builtin_amdgcn_s_setprio(3);
}
DI void conv_out(const f32x16& c, int d, int tl, char* ws, char* scratch, int lane) {
  const int r = lane & 31, h = lane >> 5;
#pragma unroll
  for (int g = 0; g < 4; ++g)
    ldsw<u32x2>(scratch, 2 * (16 * g + 8 * h + 4 * (r & 1) + 64 * (r >> 1)), u32x2{cvtpk(c[4 * g], c[4 * g + 1]), cvtpk(c[4 * g + 2], c[4 * g + 3])});
  const __amdgpu_buffer_rsrc_t rsY = __builtin_amdgcn_make_buffer_rsrc((void*)(ws + WS_YT + (size_t)d * (SEQ * 2) + 2048 * tl), 0, 2048, kRsrc3);
#pragma unroll
  for (int j = 0; j < 2; ++j) __builtin_amdgcn_raw_buffer_store_b128(ldsr<u32x4>(scratch, 1024 * j + 16 * lane), rsY, 1024 * j + 16 * lane, 0, 16);
}

DI void skip_tile(int rb, int hg, const u16* u, const u16* dskip, char* ws, u16* out, char* lds, int lane) {
  u32x4 y[9];
  tags_wait<32, 4>(ws + WS_YF + 8 * 32 * hg, get_tag(lds), lane);
  const __amdgpu_buffer_rsrc_t rsY = __builtin_amdgcn_make_buffer_rsrc((void*)(ws + WS_YT + (size_t)72 * hg * (SEQ * 2)), 0, 72 * SEQ * 2, kRsrc3);
#pragma unroll
  for (int j = 0; j < 9; ++j) {
    const int ch = 64 * j + lane, row = ch >> 3, c = ch & 7;
    y[j] = __builtin_amdgcn_raw_buffer_load_b128(rsY, row * (SEQ * 2) + 128 * rb + 16 * c, 0, 16);
  }
#pragma unroll
  for (int j = 0; j < 9; ++j) {
    const int ch = 64 * j + lane, row = ch >> 3, c = ch & 7;
    ldsw<u32x4>(lds, LDS_FA + row * 128 + 16 * (c ^ (wswz(row) << 1)), y[j]);
  }
  SBAR();
  u32x4 uu[9];
#pragma unroll
  for (int j = 0; j < 9; ++j) {
    const int ch = 64 * j + lane, row = ch / 9, c = ch - 9 * row;
    uu[j] = *(const u32x4*)(u + (size_t)(64 * rb + row) * DM + 72 * hg + 8 * c);
  }
  SBAR();
  const int g = lane >> 4, li = lane & 15, q = li >> 2, p = lane & 3;
#pragma unroll
  for (int j = 0; j < 9; ++j) {
    const int R0 = 8 * j + q, R1 = R0 + 4;
    const s16x4 a0 = ldstr(lds, LDS_FA + R0 * 128 + ((g ^ wswz(R0)) * 32) + 8 * p), a1 = ldstr(lds, LDS_FA + R1 * 128 + ((g ^ wswz(R1)) * 32) + 8 * p);
    ldsw<u32x4>(lds, LDS_FB + (16 * g + li) * 144 + 16 * j, __builtin_bit_cast(u32x4, __builtin_shufflevector(a0, a1, 0, 1, 2, 3, 4, 5, 6, 7)));
  }
#pragma unroll
  for (int j = 0; j < 9; ++j) {
    const int ch = 64 * j + lane, row = ch / 9, c = ch - 9 * row;
    const u32x4 yy = ldsr<u32x4>(lds, LDS_FB + ch * 16), dd = *(const u32x4*)(dskip + 72 * hg + 8 * c);
    u32x4 o;
#pragma unroll
    for (int k = 0; k < 4; ++k) {
      const unsigned sk = cvtpk(bflo(uu[j][k]) * bflo(dd[k]), bfhi(uu[j][k]) * bfhi(dd[k]));
      o[k] = cvtpk(bflo(yy[k]) + bflo(sk), bfhi(yy[k]) + bfhi(sk));
    }
    *(u32x4*)(out + (size_t)(64 * rb + row) * (2 * DM) + DM + 72 * hg + 8 * c) = o;
  }
}

DI void tail_part(const f32x16& oa, const f32x16& ob, float ma, float mb, int w, int rb, int hg, const u16* u, const u16* dskip, char* ws, u16* out, char* lds) {
  int lane;
  asm volatile("v_mbcnt_lo_u32_b32 %0, -1, 0\n\tv_mbcnt_hi_u32_b32 %0, -1, %0" : "=v"(lane));
  const int r = lane & 31, h = lane >> 5, pt = w & 1, hp = (w >> 1) & 1, kh = w >> 2;

  {
    const f32x16& give = kh ? oa : ob;
    const float gm = kh ? ma : mb;
    char* own = lds + LDS_CMB + w * 3328;
#pragma unroll
    for (int g = 0; g < 3; ++g) ldsw<f32x4>(own, g * 1024 + lane * 16, f32x4{give[4 * g], give[4 * g + 1], give[4 * g + 2], give[4 * g + 3]});
    ldsw<float>(own, 3072 + lane * 4, gm);
    TBAR(1);
    const char* oth = lds + LDS_CMB + (w ^ 4) * 3328;
    f32x16 keep = kh ? ob : oa;
    const float m1 = kh ? mb : ma, m2 = ldsr<float>(oth, 3072 + lane * 4);
    const float mm = fmaxf(m1, m2), f1 = EX2(m1 - mm), f2 = EX2(m2 - mm);
    float o[12];
#pragma unroll
    for (int g = 0; g < 3; ++g) {
      const f32x4 t = ldsr<f32x4>(oth, g * 1024 + lane * 16);
#pragma unroll
      for (int e = 0; e < 4; ++e) o[4 * g + e] = keep[4 * g + e] * f1 + t[e] * f2;
    }
    const float inv = 1.0f / half_lo(o[10]);
    const unsigned ot = LDS_OT + (32 * pt + r) * 144 + (2 * hp + kh) * 36;
    ldsw<unsigned>(lds, ot + 8 * h, cvtpk(o[0] * inv, o[1] * inv));
    ldsw<unsigned>(lds, ot + 8 * h + 4, cvtpk(o[2] * inv, o[3] * inv));
    ldsw<unsigned>(lds, ot + 16 + 8 * h, cvtpk(o[4] * inv, o[5] * inv));
    ldsw<unsigned>(lds, ot + 16 + 8 * h + 4, cvtpk(o[6] * inv, o[7] * inv));
    if (!h) ldsw<unsigned>(lds, ot + 32, cvtpk(o[8] * inv, o[9] * inv));
  }
  TBAR(2);
  const __amdgpu_buffer_rsrc_t rsO = rsrc(ws + WS_O + (size_t)64 * rb * (DM * 2), 64 * DM * 2);
#pragma unroll
  for (int n = 0; n < 2; ++n) {
    if (n == 0 || w == 0) {
      const int pair = 4 * (n ? 8 : w) + (lane >> 4), pt4 = pair / 9, c = pair - 9 * pt4, li = lane & 15, c8 = 9 * hg + c;
      __builtin_amdgcn_raw_buffer_store_b128(ldsr<u32x4>(lds, LDS_OT + (16 * pt4 + li) * 144 + 16 * c), rsO,
                                             ((pt4 * 18 + (c8 >> 2)) * 64 + (c8 & 3) * 16 + li) * 16, 0, SC1);
    }
  }
  asm volatile("s_waitcnt vmcnt(0)" ::: "memory");
  TBAR(3);
  STAMP(1, 9);
  if (w == 0) {
    const u64 tag = get_tag(lds);
    if (lane == 0) tag_store(ws + WS_OF + 8 * (8 * rb + hg), tag);
    tags_wait<8, 1>(ws + WS_OF + 64 * rb, tag, lane);
  }
  TBAR(4);
  STAMP(1, 10);
  lds_wait<1>(lds, F_WO, 3);
  const int li = lane & 15, g = lane >> 4, q = li >> 2, p = lane & 3, pt4 = w & 3, k2 = w >> 2;
  u32x4 xf[9];
#pragma unroll
  for (int ks = 0; ks < 9; ++ks) xf[ks] = __builtin_amdgcn_raw_buffer_load_b128(rsO, ((pt4 * 18 + 9 * k2 + ks) * 64 + lane) * 16, 0, SC1);
  STAMP(1, 11);
  f32x4 acc[5] = {};
  const unsigned wb = LDS_CV + (32 * 9 * k2 + 8 * g + q) * 144 + 8 * p;
  s16x4 wa[2][10];
#define WRD(buf, ks) do { _Pragma("unroll") for (int nt = 0; nt < 5; ++nt) { \
    wa[buf][2 * nt] = ldstr(lds, wb + 32 * (ks) * 144 + 32 * nt); wa[buf][2 * nt + 1] = ldstr(lds, wb + (32 * (ks) + 4) * 144 + 32 * nt); } } while (0)
  WRD(0, 0);
#pragma unroll
  for (int ks = 0; ks < 9; ++ks) {
    if (ks + 1 < 9) WRD((ks + 1) & 1, ks + 1);
    SBAR();
#pragma unroll
    for (int nt = 0; nt < 5; ++nt) {
      const auto a = __builtin_shufflevector(wa[ks & 1][2 * nt], wa[ks & 1][2 * nt + 1], 0, 1, 2, 3, 4, 5, 6, 7);
      acc[nt] = __builtin_amdgcn_mfma_f32_16x16x32_bf16(__builtin_bit_cast(bf16x8, a), __builtin_bit_cast(bf16x8, xf[ks]), acc[nt], 0, 0, 0);
    }
    SBAR();
  }
#undef WRD
  STAMP(1, 12);
  if (k2) {
#pragma unroll
    for (int nt = 0; nt < 5; ++nt) ldsw<f32x4>(lds, LDS_CMB + (pt4 * 5 + nt) * 1024 + lane * 16, acc[nt]);
  }
  TBAR(5);
  if (k2) return;
  u16* orow = out + (size_t)(64 * rb + 16 * pt4 + li) * (2 * DM) + 72 * hg + 4 * g;
#pragma unroll
  for (int nt = 0; nt < 5; ++nt) {
    const f32x4 t = ldsr<f32x4>(lds, LDS_CMB + (pt4 * 5 + nt) * 1024 + lane * 16);
    if (nt < 4 || lane < 32)
      *(u32x2*)(orow + 16 * nt) = u32x2{cvtpk(acc[nt][0] + t[0], acc[nt][1] + t[1]), cvtpk(acc[nt][2] + t[2], acc[nt][3] + t[3])};
  }
  asm volatile("fk_pf_end:");
  STAMP(1, 15);
}

DI void seg0_share(int i, const char* ws, char* lds, unsigned lds0, int lane) {
  const char* kvg = ws + WS_KV;
  lds_wait<LPOLL, 3>(lds, F_KVOK, 1);
#pragma unroll
  for (int j = 0; j < 4; ++j) {
    const int p = i + 6 * j;
    if (p < 19) glds16c(kvg + 1024 * p, 16 * lane, lds0 + 1024 * p);
    else if (p < 22) { if (p < 21 || lane < 16) glds16c(kvg + KV_HDR + 1024 * (p - 19), 16 * lane, lds0 + LDS_K0 + 1024 * (p - 19)); }
    else if (p == 22) { if (lane < 32) glds16c(ws + WS_KMAX, 16 * lane, lds0 + LDS_K0 + 3072); }
  }
  WAITV(0);
  FLAG_ADD(F_SEG0);
}
template <int K> DI void ring_loader(const char* ws, char* lds, unsigned lds0, int lane) {
  constexpr int NP = (19 - K + NLD - 1) / NLD;
  const char* kvg = ws + WS_KV;
  auto issue = [&](int s) {
#pragma unroll
    for (int j = 0; j < NP; ++j) glds16c(kvg + s * KV_SEG + 1024 * (K + NLD * j), 16 * lane, lds0 + (s % 3) * KV_SEG + 1024 * (K + NLD * j));
  };
  __builtin_amdgcn_s_setprio(3);
  lds_wait<1>(lds, F_UDONE, 1);
  issue(1);
  lds_wait<1>(lds, F_QRD, 8);
  issue(2);
  waitv<NP>();
  flag_set(lds, F_FULL + K, 2);
  WAITV(0);
  flag_set(lds, F_FULL + K, 3);
#pragma unroll 1
  for (int s = 3; s < 8; ++s) {
    done_wait<RING_POLL>(lds, s - 2, lane);
    issue(s);
    WAITV(0);
    flag_set(lds, F_FULL + K, s + 1);
  }
}
DI unsigned ring_full(char* lds) {
  if (NLD == 1) return flag_get(lds, F_FULL);
  const u32x4 v = *(const volatile LDSP u32x4*)((LDSP char*)lds + LDS_FLAG + 4 * F_FULL);
  if (NLD == 1) return v[0];
  if (NLD == 2) return v[0] < v[1] ? v[0] : v[1];
  return v[0] < v[1] ? (v[0] < v[2] ? v[0] : v[2]) : (v[1] < v[2] ? v[1] : v[2]);
}
DI void ring_wait(char* lds, unsigned need) {
#pragma unroll 1
  for (unsigned spins = 0; spins < (1u << 20); ++spins) {
    if (rfl(ring_full(lds)) >= need) break;
    __builtin_amdgcn_s_sleep(1);
  }
  asm volatile("" ::: "memory");
}
DI void l2_touch(const char* base, int line0, int nlines, unsigned lds0, int lane) {
  unsigned sv;
  if (line0 + lane < nlines)
    asm volatile("s_mov_b32 %0, m0\n\ts_mov_b32 m0, %3\n\ts_nop 0\n\tglobal_load_lds_dword %1, %2\n\ts_mov_b32 m0, %0"
                 : "=&s"(sv) : "v"(128 * (line0 + lane)), "s"(base), "s"(lds0 + LDS_GEN + 3 * GEN_AREA) : "memory");
}

__global__ void __launch_bounds__(1024)
fused_kernel(const u16* x, const u16* wq, const u16* wk, const u16* wv, const u16* wo, const u16* u, const u16* Are, const u16* Aim,
             const u16* Bre, const u16* Bim, const u16* Cre, const u16* Cim, const u16* logdt, const u16* dskip, char* ws, u16* out) {
  extern __shared__ __attribute__((aligned(16))) char lds[];
  const unsigned lds0 = (unsigned)(uintptr_t)lds;
  const char* pc0;
  asm volatile("s_getpc_b64 %0" : "=s"(pc0));
  const int b = BID, lane = TID & 63, w = rfl(TID >> 6);
  const int rb = (b & 7) + 8 * (b >> 6), hg = (b >> 3) & 7;
  STAMP(1, 0);
  if (TID < 64) ((LDSP unsigned*)((LDSP char*)lds + LDS_FLAG))[TID] = 0u;
  BAR();
  const char* kvg = ws + WS_KV;
  if (w >= 14) {
    __builtin_amdgcn_s_setprio(2);
    if (w == 15) {
      const char* pc1;
      int dend;
      asm volatile("s_getpc_b64 %0\nfk_pf_here:\n\ts_mov_b32 %1, fk_pf_end-fk_pf_here" : "=s"(pc1), "=s"(dend));
      const int len = (int)(pc1 - pc0) + (dend > 0 ? dend : 0);
      l2_touch(pc0, (b >> 3) + 31 * lane, len >> 7, lds0, lane);
    }
    if (w == 14) q_loader<4>(x, wq, rb, hg, lds, lds0, lane); else q_loader<5>(x, wq, rb, hg, lds, lds0, lane);
    seg0_share(w - 10, ws, lds, lds0, lane);
    if (NLD > 1 && w == 14) ring_loader<1>(ws, lds, lds0, lane);
    if (NLD > 2 && w == 15) ring_loader<2>(ws, lds, lds0, lane);
    return;
  }
  if (w == 13) {
    __builtin_amdgcn_s_setprio(2);
    __builtin_amdgcn_s_sleep(UT_DELAY);
    ut_wave(rb, hg, u, ws, lds, lane);
    STAMP(1, 15);
    return;
  }
  if (w == 12) {
    __builtin_amdgcn_s_setprio(3);
    const u64 tag = __hip_atomic_load((gu64*)(ws + WS_EPOCH), __ATOMIC_RELAXED, __HIP_MEMORY_SCOPE_AGENT) + 1;
    flag_set(lds, F_TAG, (unsigned)tag); flag_set(lds, F_TAG + 1, (unsigned)(tag >> 32)); flag_set(lds, F_TAGOK, 1);
    q_loader<3>(x, wq, rb, hg, lds, lds0, lane);
    STAMP(1, 3);
    seg0_share(3, ws, lds, lds0, lane);
    STAMP(1, 4);
    ring_loader<0>(ws, lds, lds0, lane);
    STAMP(1, 15);
    return;
  }
  if (w >= 8) {
    const int sw = w - 8, i = b >> 3, xc = b & 7;
    const int d0 = 72 * xc + 2 * i, dx = 72 * xc + 64 + (i >> 2), third = (i & 3) < 2;
    char* area = lds + LDS_GEN + (sw - 1) * GEN_AREA;
    if (sw == 0) {
      __builtin_amdgcn_s_setprio(3);
      kv_wave(b, rb, hg, x, ws, lds, lds0, lane);
      STAMP(1, 5);
    } else if (sw < 3 || third) {
      __builtin_amdgcn_s_setprio(GEN_PRIO);
      gen_wave(sw < 3 ? d0 + sw - 1 : dx, Are, Aim, Bre, Bim, Cre, Cim, logdt, area, lane);
      STAMP(1, 3);
    }
    __builtin_amdgcn_s_setprio(3);
    tags_wait<32, 4>(ws + WS_UF + 8 * 32 * xc, get_tag(lds), lane);
    lds_wait<2, 3>(lds, F_QDONE, 4);
    if (sw == 0) u_fill2<0>(d0, ws, lds, lds0, lane);
    else if (sw == 1) u_fill2<1>(d0, ws, lds, lds0, lane);
    else if (sw == 2) u_fill2<2>(d0, ws, lds, lds0, lane);
    else u_fill2<3>(d0, ws, lds, lds0, lane);
    if (sw != 0 && (sw < 3 || third)) build_copies(area, lds + LDS_CV + (sw - 1) * CV_CH, lane);
    if (third) {
      asm volatile("s_waitcnt lgkmcnt(0)" ::: "memory");
      FLAG_ADD(F_KCOP);
      lds_wait<LPOLL>(lds, F_KCOP, 4);
      char* c2 = lds + LDS_CV + 2 * CV_CH;
      const unsigned c20 = lds0 + LDS_CV + 2 * CV_CH;
      if (sw == 0) u_fill<0>(dx, ws, c2, c20, lane);
      else if (sw == 1) u_fill<1>(dx, ws, c2, c20, lane);
      else if (sw == 2) u_fill<2>(dx, ws, c2, c20, lane);
      else u_fill<3>(dx, ws, c2, c20, lane);
    }
    asm volatile("s_waitcnt vmcnt(0) lgkmcnt(0)" ::: "memory");
    FLAG_ADD(F_CFILL);
    STAMP(1, 6);
    {
      const int r = lane & 31, h = lane >> 5;
      const unsigned kb = LDS_CV + (3 - (r & 3)) * CV_K + 2 * (8 * h - 8 * (r >> 2));
      const unsigned ub = LDS_CV + CV_U + (r & 1) * CV_UP + 144 * (r >> 1) + 16 * h;
      const unsigned A1 = kb + 2 * 1080, A2 = kb + 2 * 56, B1 = ub, B3 = ub + 2304;
      char* scr = lds + LDS_SCR + sw * 2048;
      f32x16 c0 = {}, c1 = {};
      lds_wait<4>(lds, F_CFILL, 4);
      const int cu = CONV_MAP(sw);
      if (cu == 2) {
        conv_steps<true, false>(lds, A1, B1, A1 + CV_CH, B1 + CV_CH, c0, c1);
        conv_out(c0, d0, 0, ws, scr, lane);
        conv_out(c1, d0 + 1, 0, ws, scr, lane);
      } else if (cu < 2 || (i & 3) == 0) {
        const unsigned so = (cu < 2 ? cu : 2) * CV_CH;
        conv_steps<true, true>(lds, A2 + so, B1 + so, A1 + so, B3 + so, c0, c1);
#pragma unroll
        for (int k = 0; k < 16; ++k) c0[k] += c1[k];
        conv_out(c0, cu < 2 ? d0 + cu : dx, 1, ws, scr, lane);
      } else if (third) {
        conv_steps<false, false>(lds, A1 + 2 * CV_CH, B1 + 2 * CV_CH, 0, 0, c0, c1);
        conv_out(c0, dx, 0, ws, scr, lane);
      }
      STAMP(1, 7);
    }
    asm volatile("s_waitcnt vmcnt(0) lgkmcnt(0)" ::: "memory");
    unsigned arrived = 0;
    if (lane == 0) arrived = __hip_atomic_fetch_add((LDSP unsigned*)((LDSP char*)lds + LDS_FLAG) + F_CDONE, 1u, __ATOMIC_RELAXED, __HIP_MEMORY_SCOPE_WORKGROUP);
    if (rfl(arrived) == 3) { const u64 tag = get_tag(lds); if (lane == 0) tag_store(ws + WS_YF + 8 * (32 * xc + i), tag); }
    lds_wait<4>(lds, F_CDONE, 4);
    if (sw == 2) {
      __builtin_amdgcn_s_setprio(0);
      lds_wait<16, 3>(lds, F_TB + 0, 8);
      skip_tile(rb, hg, u, dskip, ws, out, lds, lane);
      STAMP(1, 8);
    } else {
      const int q3 = sw == 3 ? 2 : sw;
      unsigned vo[3];
#pragma unroll
      for (int k = 0; k < 3; ++k) { const int ch = 64 * (3 * q3 + k) + lane, row = ch / 9, c = ch - 9 * row; vo[k] = row * (DM * 2) + c * 16; }
      const char* wb = (const char*)(wo + 72 * hg);
#pragma unroll 1
      for (int a = 0; a < 9; ++a) {
#pragma unroll
        for (int k = 0; k < 3; ++k) glds16s(wb + (size_t)a * 64 * (DM * 2), vo[k], lds0 + LDS_CV + 1024 * (9 * a + 3 * q3 + k));
      }
      STAMP(1, 8);
      WAITV(0);
      FLAG_ADD(F_WO);
    }
    STAMP(1, 15);
    return;
  }
  __builtin_amdgcn_s_setprio(3);
  if (w < 4) {
    q_compute(wk, wv, hg, lds, w, lane, ws);
  } else if (w == 4) {
    q_loader<6>(x, wq, rb, hg, lds, lds0, lane);
  } else {
    if (w == 5) q_loader<0>(x, wq, rb, hg, lds, lds0, lane);
    else if (w == 6) q_loader<1>(x, wq, rb, hg, lds, lds0, lane);
    else q_loader<2>(x, wq, rb, hg, lds, lds0, lane);
    STAMP(1, 6);
    seg0_share(w - 5, ws, lds, lds0, lane);
  }
  STAMP(1, 3);
  const int r = lane & 31, h = lane >> 5;
  const int pt = w & 1, hp = (w >> 1) & 1, kh = w >> 2;

  lds_wait<LPOLL, 3>(lds, F_QDONE, 4);
  const unsigned qo = LDS_QT + (32 * pt + r) * 192 + 2 * hp * 48;
  const u32x4 qam = ldsr<u32x4>(lds, qo + 16 * h), qbm = ldsr<u32x4>(lds, qo + 48 + 16 * h);
  const unsigned qa16 = ldsr<unsigned>(lds, qo + 32), qb16 = ldsr<unsigned>(lds, qo + 48 + 32);
  auto sq = [](unsigned u) { return bflo(u) * bflo(u) + bfhi(u) * bfhi(u); };
  float na = half_sum(sq(qam[0]) + sq(qam[1]) + sq(qam[2]) + sq(qam[3]) + (h ? 0.f : sq(qa16)));
  float nb = half_sum(sq(qbm[0]) + sq(qbm[1]) + sq(qbm[2]) + sq(qbm[3]) + (h ? 0.f : sq(qb16)));
  PIN(na); PIN(nb);
  lds_wait<LPOLL, 3>(lds, F_SEG0, 6);
  u32x4 kA = ldsr<u32x4>(lds, LDS_K0 + kh * 1152 + h * 512 + r * 16);
  u32x2 kAx = {ldsr<unsigned>(lds, LDS_K0 + kh * 1152 + 1024 + r * 4), 0x3f80u};
  const float km0 = ldsr<float>(lds, LDS_K0 + 3072 + 4 * lane), km1 = ldsr<float>(lds, LDS_K0 + 3072 + 256 + 4 * lane);
  asm volatile("s_waitcnt lgkmcnt(0)" ::: "memory");
  FLAG_ADD(F_QRD);
  const float kmax2 = wave_max(fmaxf(km0, km1));
  const float ba = rbf(__builtin_amdgcn_sqrtf(na * kmax2) * 1.01f + 1e-6f), bb = rbf(__builtin_amdgcn_sqrtf(nb * kmax2) * 1.01f + 1e-6f);
  const bool fast = __builtin_amdgcn_ballot_w64(!(ba <= FAST_BOUND && bb <= FAST_BOUND)) == 0;

  KVAddr ad;
  {
    const int vr = r <= 18 ? r : 19;
    const unsigned tb = kh * KV_HALF;
    ad.k = tb + h * 512 + r * 16; ad.kx = tb + 1024 + r * 4; ad.v = tb + 1152 + h * KV_VP + vr * 16;
  }
  __builtin_amdgcn_s_setprio(ATT_PRIO);
  f32x16 oa = {}, ob = {};
  float ma, mb;
  if (fast) {
    ma = 0.f; mb = 0.f;
    u32x2 qax = {h ? 0u : qa16, h ? 0u : (cvtpk(-ba, 0.f))}, qbx = {h ? 0u : qb16, h ? 0u : (cvtpk(-bb, 0.f))};
    u32x4 kB, vA0, vA1, vB0, vB1;
    u32x2 kBx = {0u, 0x3f80u};
    f32x16 zero16 = {}, sa, sb;
    u32x8 pa, pb = {};
    sa = MFMA16(kA, qam, zero16); sa = MFMA8(kAx, qax, sa);
    STAMP(1, 1);
    STAMP(1, 2);
    LDV(vB0, vB1, 0);
    unsigned off = 0;
#pragma unroll 1
    for (int s = 0; s < 8; ++s) {
      STEP(kA, kAx, kB, kBx, vB0, vB1, vA0, vA1, off);
      STEP(kB, kBx, kA, kAx, vA0, vA1, vB0, vB1, off + KV_TILE);
      STEP(kA, kAx, kB, kBx, vB0, vB1, vA0, vA1, off + 2 * KV_TILE);
      const unsigned full = ring_full(lds);
      STEP(kB, kBx, kA, kAx, vA0, vA1, vB0, vB1, off + 3 * KV_TILE);
      flag_set(lds, F_DONE + w, s + 1);
      off = off == 2 * KV_SEG ? 0 : off + KV_SEG;
      if (s < 7 && rfl(full) < s + 2) ring_wait(lds, s + 2);
    }
    ob = MFMA16(vB0, lo4(pb), ob);
    ob = MFMA16(vB1, hi4(pb), ob);
  } else {
    ma = -INFINITY; mb = -INFINITY;
    const u32x2 qax = {h ? 0u : qa16, 0u}, qbx = {h ? 0u : qb16, 0u};
    kAx[1] = 0u;
#pragma unroll 1
    for (int t = 0; t < 32; ++t) {
      if (t && (t & 3) == 0) ring_wait(lds, (t >> 2) + 1);
      const unsigned off = ((t >> 2) % 3) * KV_SEG + (t & 3) * KV_TILE;
      u32x4 kn, v0, v1; u32x2 knx = {0u, 0u};
      LDK(kn, knx, off); LDV(v0, v1, off);
      if ((t & 3) == 3) flag_set(lds, F_DONE + w, (t >> 2) + 1);
      slow_tile(kA, kAx, v0, v1, qam, qax, oa, ma);
      slow_tile(kA, kAx, v0, v1, qbm, qbx, ob, mb);
      kA = kn; kAx = knx;
    }
  }
  STAMP(1, 5);
  TBAR(0);
  tail_part(oa, ob, ma, mb, w, rb, hg, u, dskip, ws, out, lds);
}

#if defined(__HIP_DEVICE_COMPILE__)
#pragma clang attribute pop
#endif
extern "C" void kernel_launch(void* const* d_in, const int* in_sizes, int n_in, void* d_out, int out_size, void* d_ws, size_t ws_size,
                              hipStream_t stream) {
  (void)hipFuncSetAttribute((const void*)fused_kernel, hipFuncAttributeMaxDynamicSharedMemorySize, K_LDS);
  fused_kernel<<<256, 1024, K_LDS, stream>>>((const u16*)d_in[0], (const u16*)d_in[1], (const u16*)d_in[2], (const u16*)d_in[3], (const u16*)d_in[4],
                                            (const u16*)d_in[5], (const u16*)d_in[6], (const u16*)d_in[7], (const u16*)d_in[8], (const u16*)d_in[9],
                                            (const u16*)d_in[10], (const u16*)d_in[11], (const u16*)d_in[12], (const u16*)d_in[13], (char*)d_ws,
                                            (u16*)d_out);
}
```

```cpp
#if defined(__HIP_DEVICE_COMPILE__)
#pragma clang attribute push(__attribute__((target("no-packed-fp32-ops"))), apply_to = function)
#endif
#include <hip/hip_runtime.h>
#include <stdint.h>

typedef __bf16 bf16x2 __attribute__((ext_vector_type(2)));
typedef __bf16 bf16x8 __attribute__((ext_vector_type(8)));
typedef short s16x4 __attribute__((ext_vector_type(4)));
typedef float f32x2 __attribute__((ext_vector_type(2)));
typedef float f32x4 __attribute__((ext_vector_type(4)));
typedef float f32x16 __attribute__((ext_vector_type(16)));
typedef unsigned u32x2 __attribute__((ext_vector_type(2)));
typedef unsigned u32x4 __attribute__((ext_vector_type(4)));
typedef unsigned short u16;
#define DI __device__ __forceinline__
#define BID ((int)__builtin_amdgcn_workgroup_id_x())
#define TID ((int)__builtin_amdgcn_workitem_id_x())
#define LDSP __attribute__((address_space(3)))
#define SBAR() __builtin_amdgcn_sched_barrier(0)
#define PIN(x) asm volatile("" : "+v"(x))
#define MFMA16(a, b, c) __builtin_amdgcn_mfma_f32_32x32x16_bf16(__builtin_bit_cast(bf16x8, a), __builtin_bit_cast(bf16x8, b), c, 0, 0, 0)
#define MFMA8(a, b, c) __builtin_amdgcn_mfma_f32_32x32x8bf16_1k(__builtin_bit_cast(s16x4, a), __builtin_bit_cast(s16x4, b), c, 0, 0, 0)

constexpr int SEQ = 2048, DM = 576, DH = 18, NST = 128;
constexpr int KV_VP = 320;
constexpr int KV_TILE = 1152 + 4 * KV_VP;
constexpr int KV_HALF = 4 * KV_TILE, KV_SEG = 2 * KV_HALF;
constexpr int KV_HDR = 8 * KV_SEG;
constexpr int UT_ROW = 2064;
constexpr size_t WS_EPOCH = 0;
constexpr size_t WS_KVF = 256;
constexpr size_t WS_OF = WS_KVF + 8 * 2048;
constexpr size_t WS_UF = WS_OF + 2048;
constexpr size_t WS_YF = WS_UF + 2048;
constexpr size_t WS_KMAX = WS_YF + 2048;
constexpr size_t WS_KV = WS_KMAX + 1024;
constexpr size_t WS_O = WS_KV + 163840;
constexpr size_t WS_UT = WS_O + (size_t)SEQ * DM * 2;
constexpr size_t WS_YT = WS_UT + (size_t)DM * UT_ROW * 2;

DI unsigned cvtpk(float a, float b) { f32x2 v = {a, b}; return __builtin_bit_cast(unsigned, __builtin_convertvector(v, bf16x2)); }
DI float bflo(unsigned u) { return __uint_as_float(u << 16); }
DI float bfhi(unsigned u) { return __uint_as_float(u & 0xffff0000u); }
DI float bf2f(u16 x) { return __uint_as_float((unsigned)x << 16); }
DI float rbf(float x) { return bflo(cvtpk(x, x)); }
DI int crow(int reg, int h) { return (reg & 3) + 8 * (reg >> 2) + 4 * h; }
DI void glds16s(const void* sbase, unsigned voff, unsigned lds_base) {
  unsigned sv;
  asm volatile("s_mov_b32 %0, m0\n\ts_mov_b32 m0, %3\n\ts_nop 0\n\tglobal_load_lds_dwordx4 %1, %2\n\ts_mov_b32 m0, %0"
               : "=&s"(sv) : "v"(voff), "s"(sbase), "s"(lds_base) : "memory");
}
DI void glds16c(const void* sbase, unsigned voff, unsigned lds_base) {
  unsigned sv;
  asm volatile("s_mov_b32 %0, m0\n\ts_mov_b32 m0, %3\n\ts_nop 0\n\tglobal_load_lds_dwordx4 %1, %2 sc1\n\ts_mov_b32 m0, %0"
               : "=&s"(sv) : "v"(voff), "s"(sbase), "s"(lds_base) : "memory");
}
#define WAITV(n) asm volatile("s_waitcnt vmcnt(" #n ")" ::: "memory")
#define WAITV_BAR(n) asm volatile("s_waitcnt vmcnt(" #n ") lgkmcnt(0)\n\ts_barrier" ::: "memory")
#define SB() asm volatile("s_barrier" ::: "memory")
#define BAR() asm volatile("s_waitcnt lgkmcnt(0)\n\ts_barrier" ::: "memory")
#define STAMP(kern, i)
DI unsigned rfl(unsigned x) { return __builtin_amdgcn_readfirstlane(x); }
template <typename T> DI T ldsr(const char* lds, unsigned off) { return *(const LDSP T*)((const LDSP char*)lds + off); }
template <typename T> DI void ldsw(char* lds, unsigned off, T v) { *(LDSP T*)((LDSP char*)lds + off) = v; }
DI s16x4 ldstr(const char* lds, unsigned off) {
  typedef short v4i16_t __attribute__((ext_vector_type(4)));
  return __builtin_bit_cast(s16x4, __builtin_amdgcn_ds_read_tr16_b64_v4i16((LDSP v4i16_t*)((LDSP char*)lds + off)));
}
DI float half_sum(float v) {
  const auto t = __builtin_amdgcn_permlane32_swap(__float_as_uint(v), __float_as_uint(v), false, false);
  return __uint_as_float(t[0]) + __uint_as_float(t[1]);
}
DI float half_max(float v) {
  const auto t = __builtin_amdgcn_permlane32_swap(__float_as_uint(v), __float_as_uint(v), false, false);
  return fmaxf(__uint_as_float(t[0]), __uint_as_float(t[1]));
}
DI float half_lo(float v) {
  const auto t = __builtin_amdgcn_permlane32_swap(__float_as_uint(v), __float_as_uint(v), false, false);
  return __uint_as_float(t[0]);
}
template <int CTRL> DI float dpp_max(float v) {
  return fmaxf(v, __uint_as_float(__builtin_amdgcn_update_dpp(__float_as_uint(v), __float_as_uint(v), CTRL, 0xf, 0xf, false)));
}
DI float wave_max(float v) {
  v = dpp_max<0xb1>(v);
  v = dpp_max<0x4e>(v);
  v = dpp_max<0x141>(v);
  v = dpp_max<0x140>(v);
  const unsigned u = __float_as_uint(v);
  const float a = __uint_as_float(__builtin_amdgcn_readlane(u, 0)), b = __uint_as_float(__builtin_amdgcn_readlane(u, 16));
  const float c = __uint_as_float(__builtin_amdgcn_readlane(u, 32)), d = __uint_as_float(__builtin_amdgcn_readlane(u, 48));
  return fmaxf(fmaxf(a, b), fmaxf(c, d));
}

DI float xrow16_sum(float x) {
  const auto s = __builtin_amdgcn_permlane16_swap(__float_as_uint(x), __float_as_uint(x), false, false);
  return half_sum(__uint_as_float(s[0]) + __uint_as_float(s[1]));
}
template <int N> DI void waitv() { asm volatile("s_waitcnt vmcnt(%0)" ::"i"(N) : "memory"); }
DI int wswz(int row) { return ((row >> 1) & 1) | (((row >> 3) & 1) << 1); }
constexpr int kRsrc3 = 0x00020000;
typedef __attribute__((address_space(1))) unsigned gu32;
typedef __attribute__((address_space(1))) unsigned long long gu64;
typedef unsigned long long u64;
DI __amdgpu_buffer_rsrc_t rsrc(const void* p, int bytes) { return __builtin_amdgcn_make_buffer_rsrc((void*)p, 0, bytes, kRsrc3); }
constexpr int SC1 = 16;

#define CONV_MAP(sw) (sw)
#define NLD 1
#define RING_POLL 4
#define UT_WAIT F_KVPUB
#define UT_DELAY 16
#define QHOLD 99
#define QRAMP 3
#define GEN_PRIO 2
#define ATT_PRIO 0
#define KV_MODE 1
#define KV_DELAY 32
#define KV_POLL 8
#define LPOLL 1
constexpr int LDS_CV = 3 * KV_SEG;
constexpr int LDS_WKV = 0;
constexpr int LDS_UA = 23040;
constexpr int LDS_QT = 40960;
constexpr int LDS_K0 = 53248;
constexpr int GS = 20736;
constexpr int CV_CH = 31744, CV_K = 4416, CV_U = 17664, CV_UP = 7040;
constexpr int LDS_GEN = LDS_CV + 4 * GS;
constexpr int GEN_AREA = 6656, GEN_PITCH = 272, GEN_P = 21 * GEN_PITCH;
constexpr int LDS_CMB = 0;
constexpr int LDS_OT = 8 * 3328;
constexpr int LDS_FA = LDS_CV + 82944, LDS_FB = LDS_FA + 9216;
constexpr int LDS_SCR = LDS_CV + 3 * CV_CH;
constexpr int LDS_FLAG = LDS_SCR + 4 * 2048;
constexpr int K_LDS = LDS_FLAG + 256;
constexpr float FAST_BOUND = 30.f;
#define CONV_PRIO 0

constexpr int F_FULL = 0  , F_SEG0 = 3, F_TAGOK = 4, F_TAG = 5  , F_WKV = 7, F_DONE = 8  ,
              F_GF = 16  , F_GD = 32  , F_QDONE = 48, F_QRD = 49, F_KVOK = 50, F_UDONE = 51, F_KCOP = 52, F_WO = 53,
              F_KVPUB = 54, F_CFILL = 55, F_KMAX = 41  , F_KMOK = 42, F_TB = 56  , F_CDONE = 62;
DI void flag_add(char* lds, int word) { __hip_atomic_fetch_add((LDSP unsigned*)((LDSP char*)lds + LDS_FLAG) + word, 1u, __ATOMIC_RELAXED, __HIP_MEMORY_SCOPE_WORKGROUP); }
DI void flag_set(char* lds, int word, unsigned v) {
  asm volatile("" ::: "memory");
  __hip_atomic_store((LDSP unsigned*)((LDSP char*)lds + LDS_FLAG) + word, v, __ATOMIC_RELAXED, __HIP_MEMORY_SCOPE_WORKGROUP);
  asm volatile("" ::: "memory");
}
DI unsigned flag_get(char* lds, int word) { return __hip_atomic_load((LDSP unsigned*)((LDSP char*)lds + LDS_FLAG) + word, __ATOMIC_RELAXED, __HIP_MEMORY_SCOPE_WORKGROUP); }
template <int SLEEP, int PRIO = -1> DI void lds_wait(char* lds, int word, unsigned need) {
  if (rfl(flag_get(lds, word)) < need) {
    if (PRIO >= 0) __builtin_amdgcn_s_setprio(0);
#pragma unroll 1
    for (unsigned spins = 0; spins < (1u << 20); ++spins) {
      __builtin_amdgcn_s_sleep(SLEEP);
      if (rfl(flag_get(lds, word)) >= need) break;
    }
    if (PRIO >= 0) __builtin_amdgcn_s_setprio(PRIO);
  }
  asm volatile("" ::: "memory");
}
template <int SLEEP> DI void done_wait(char* lds, unsigned need, int lane) {
#pragma unroll 1
  for (unsigned spins = 0; spins < (1u << 20); ++spins) {
    if (__builtin_amdgcn_ballot_w64(flag_get(lds, F_DONE + (lane & 7)) < need) == 0) break;
    __builtin_amdgcn_s_sleep(SLEEP);
  }
  asm volatile("" ::: "memory");
}
#define ONE_LANE(stmt) do { asm volatile("" ::: "memory"); if (lane == 0) { stmt; } asm volatile("" ::: "memory"); } while (0)

#define FLAG_ADD(word) ONE_LANE(flag_add(lds, word))
#define TBAR(k) do { asm volatile("s_waitcnt lgkmcnt(0)" ::: "memory"); FLAG_ADD(F_TB + (k)); lds_wait<0>(lds, F_TB + (k), 8); } while (0)
DI u64 get_tag(char* lds) {
  lds_wait<1>(lds, F_TAGOK, 1);
  return (u64)rfl(flag_get(lds, F_TAG)) | ((u64)rfl(flag_get(lds, F_TAG + 1)) << 32);
}
DI void tag_store(char* p, u64 tag) { __hip_atomic_store((gu64*)p, tag, __ATOMIC_RELAXED, __HIP_MEMORY_SCOPE_AGENT); }
template <int N, int SLEEP> DI void tags_wait(const char* p, u64 tag, int lane) {
#pragma unroll 1
  for (unsigned spins = 0; spins < (1u << 20); ++spins) {
    bool ok = true;
#pragma unroll
    for (int j = 0; j < (N + 63) / 64; ++j) {
      const int k = 64 * j + lane;
      ok &= __hip_atomic_load((gu64*)p + (k < N ? k : 0), __ATOMIC_RELAXED, __HIP_MEMORY_SCOPE_AGENT) == tag;
    }
    if (__builtin_amdgcn_ballot_w64(!ok) == 0) break;
    __builtin_amdgcn_s_sleep(SLEEP);
  }
  __builtin_amdgcn_fence(__ATOMIC_ACQUIRE, "wavefront");
  asm volatile("" ::: "memory");
}

template <int SLEEP> DI void tags256_wait(const char* p, u64 tag, int lane) {
  u64 a[4], c[4];
#define TLD(v) do { _Pragma("unroll") for (int j = 0; j < 4; ++j) v[j] = __hip_atomic_load((gu64*)p + 64 * j + lane, __ATOMIC_RELAXED, __HIP_MEMORY_SCOPE_AGENT); } while (0)
#define TOK(v) (__builtin_amdgcn_ballot_w64(!(v[0] == tag && v[1] == tag && v[2] == tag && v[3] == tag)) == 0)
  TLD(a);
#pragma unroll 1
  for (unsigned spins = 0; spins < (1u << 19); ++spins) {
    __builtin_amdgcn_s_sleep(SLEEP);
    TLD(c);
    if (TOK(a)) break;
    __builtin_amdgcn_s_sleep(SLEEP);
    TLD(a);
    if (TOK(c)) break;
  }
#undef TLD
#undef TOK
  __builtin_amdgcn_fence(__ATOMIC_ACQUIRE, "wavefront");
  asm volatile("" ::: "memory");
}

constexpr int NI = 7;
template <int IW> constexpr int q_cnt(int s) { return s > 7 ? 0 : s < 4 ? (18 - IW + NI - 1) / NI : (21 - IW + NI - 1) / NI; }
template <int IW, int S> DI void q_issue(const char* wb, const char* xb, int kvg, const unsigned (&vw)[3], const unsigned (&vx)[3], unsigned lds0, int lane) {
  const unsigned sb = lds0 + LDS_CV + (S & 3) * GS;
#pragma unroll
  for (int j = 0; j < q_cnt<IW>(S); ++j) {
    const int p = IW + NI * j;
    if constexpr (S >= 4) { if (p < 20 || lane < 16) glds16s(wb + (size_t)144 * (S - 4) * (DM * 2), vw[j], sb + 1024 * p); }
    else glds16s(xb + (size_t)16 * ((kvg + S) & 3) * (DM * 2), vx[j], sb + 1024 * p);
  }
}
constexpr int q_last(int S) { return S < 0 ? 1 : S < QRAMP ? S + 2 : (S + 3 > 7 ? 7 : S + 3); }
template <int IW> constexpr int q_pending(int S) { int n = 0; for (int k = S + 1; k <= q_last(S - 1); ++k) n += q_cnt<IW>(k); return n; }
template <int IW, int S, int K> DI void q_issue_upto(const char* wb, const char* xb, int kvg, const unsigned (&vw)[3], const unsigned (&vx)[3], char* lds, unsigned lds0, int lane) {
  if constexpr (K <= q_last(S) && K <= 7) {
    if constexpr (K == QHOLD) lds_wait<LPOLL, 3>(lds, F_KVPUB, 1);
    if constexpr (K >= 4) lds_wait<LPOLL, 3>(lds, F_GD + K - 4, K == 4 ? 2 : 1);
    q_issue<IW, K>(wb, xb, kvg, vw, vx, lds0, lane);
    q_issue_upto<IW, S, K + 1>(wb, xb, kvg, vw, vx, lds, lds0, lane);
  }
}
template <int IW, int S> DI void q_steps(const char* wb, const char* xb, int kvg, const unsigned (&vw)[3], const unsigned (&vx)[3], char* lds, unsigned lds0, int lane) {
  waitv<q_pending<IW>(S)>();
  FLAG_ADD(F_GF + S);
  q_issue_upto<IW, S, q_last(S - 1) + 1>(wb, xb, kvg, vw, vx, lds, lds0, lane);
  if constexpr (S + 1 < 8) q_steps<IW, S + 1>(wb, xb, kvg, vw, vx, lds, lds0, lane);
}
DI unsigned x_piece(int p, int lane) {
  const int n = 64 * p + lane, row = n / 72, c = n - 72 * row;
  return row * (DM * 2) + 16 * ((c & ~7) | ((c ^ (row >> 1)) & 7));
}
template <int IW> DI void q_loader(const u16* x, const u16* wq, int rb, int hg, char* lds, unsigned lds0, int lane) {
  unsigned vw[3], vx[3];
#pragma unroll
  for (int j = 0; j < 3; ++j) vx[j] = x_piece(IW + NI * j < 18 ? IW + NI * j : 0, lane);
  const char* wb = (const char*)(wq + 72 * hg);
  const char* xb = (const char*)(x + (size_t)64 * rb * DM);
  q_issue<IW, 0>(wb, xb, hg & 3, vw, vx, lds0, lane);
  q_issue<IW, 1>(wb, xb, hg & 3, vw, vx, lds0, lane);
#pragma unroll
  for (int j = 0; j < 3; ++j) { const int n = 64 * (IW + NI * j) + lane, row = n / 9, c = n - 9 * row; vw[j] = row * (DM * 2) + 16 * c; }
  q_steps<IW, 0>(wb, xb, hg & 3, vw, vx, lds, lds0, lane);
}
DI void x_bases(int lane, unsigned (&xb)[2]) {
  const int li = lane & 15, g = lane >> 4, xs = (li >> 1) & 7;
  xb[0] = li * (DM * 2) + 16 * (g ^ xs); xb[1] = li * (DM * 2) + 16 * ((4 + g) ^ xs);
}
#define XFRAG(base, ks) ldsr<u32x4>(lds, (base)[(ks) & 1] + 128 * ((ks) >> 1))

constexpr int QD0 = 5;
constexpr int QD1(int i) { return QD0 + 1 + i / 5 > 12 ? 12 : QD0 + 1 + i / 5; }
constexpr int qp_end(int i) { return i < 20 ? 20 : i < 45 ? 45 : i < 65 ? 65 : 90; }
constexpr int qp_ahead(int i) { return i + 1 + (QD0 + i / 5 > 12 ? 12 : QD0 + i / 5) < qp_end(i) ? i + 1 + (QD0 + i / 5 > 12 ? 12 : QD0 + i / 5) : qp_end(i); }
template <int FROM, int TO> DI void q_reads(const char* lds, unsigned wn, unsigned wx, u32x4 (&wa)[90]) {
  if constexpr (FROM < TO) {
    constexpr int ks = FROM / 5, nt = FROM % 5;
    const unsigned b_ = ((2 * ks) % 9 == 8 ? wx : wn) + ((2 * ks) / 9) * GS + ((2 * ks) % 9) * 2304 + 32 * nt;
    wa[FROM] = __builtin_bit_cast(u32x4, __builtin_shufflevector(ldstr(lds, b_), ldstr(lds, b_ + 4 * 144), 0, 1, 2, 3, 4, 5, 6, 7));
    q_reads<FROM + 1, TO>(lds, wn, wx, wa);
  }
}
template <int I, int NXT> DI void q_products(char* lds, unsigned wn, unsigned wx, const u32x4 (&xf)[18], u32x4 (&wa)[90], f32x4 (&acc)[5]) {
  if constexpr (I < 90) {
    constexpr bool first = I == 0 || I == 20 || I == 45 || I == 65;
    constexpr int start = first ? (I + QD1(I) < qp_end(I) ? I + QD1(I) : qp_end(I)) : NXT;
    if constexpr (first) {
      lds_wait<LPOLL, 3>(lds, F_GF + 4 + (2 * (I / 5) + 1) / 9, NI);
      q_reads<I, start>(lds, wn, wx, wa);
      SBAR();
    }
    acc[I % 5] = __builtin_amdgcn_mfma_f32_16x16x32_bf16(__builtin_bit_cast(bf16x8, wa[I]), __builtin_bit_cast(bf16x8, xf[I / 5]), acc[I % 5], 0, 0, 0);
    SBAR();
    constexpr int upto = qp_ahead(I) > start ? qp_ahead(I) : start;
    q_reads<start, upto>(lds, wn, wx, wa);
    SBAR();
    q_products<I + 1, upto>(lds, wn, wx, xf, wa, acc);
  }
}
DI void q_compute(const u16* wk, const u16* wv, int hg, char* lds, int w, int lane, char* ws) {
  {
    typedef unsigned u32x3 __attribute__((ext_vector_type(3)));
    const char* src = (const char*)(hg >> 2 ? wv : wk);
    u32x3 t[7];
#pragma unroll
    for (int j = 0; j < 7; ++j) { const int i = w + 4 * j; __builtin_memcpy(&t[j], src + 12 * (64 * (i < 27 ? i : 0) + lane), 12); }
#pragma unroll
    for (int j = 0; j < 7; ++j) {
      const int i = w + 4 * j, e = 64 * i + lane, k = e / 3, c = e - 3 * k;
      if (i < 27) { const unsigned o = LDS_WKV + 40 * k + 12 * c; ldsw<unsigned>(lds, o, t[j][0]); ldsw<unsigned>(lds, o + 4, t[j][1]); ldsw<unsigned>(lds, o + 8, t[j][2]); }
    }
    asm volatile("s_waitcnt lgkmcnt(0)" ::: "memory");
    FLAG_ADD(F_WKV);
    STAMP(1, 6);
  }
  const int li = lane & 15, g = lane >> 4, q = li >> 2, p = lane & 3, st = (w - hg) & 3;
  u32x4 xf[18];
  {
    unsigned xb[2];
    x_bases(lane, xb);
    xb[0] += LDS_CV + st * GS; xb[1] += LDS_CV + st * GS;
    lds_wait<LPOLL, 3>(lds, F_GF + st, NI);
#pragma unroll
    for (int ks = 0; ks < 18; ++ks) xf[ks] = XFRAG(xb, ks);
    asm volatile("s_waitcnt lgkmcnt(0)" ::: "memory");
    FLAG_ADD(F_GD + st);
  }
  STAMP(1, 7);
  f32x4 acc[5] = {};
  const unsigned wbase = LDS_CV + (8 * (g & 1) + q) * 144 + 8 * p;
  const unsigned wn = wbase + (g >> 1) * 2304;
  const unsigned wx = wbase + (g >> 1) * (GS - 8 * 2304);
  u32x4 wa[90];
  q_products<0, 0>(lds, wn, wx, xf, wa, acc);
#pragma unroll
  for (int nt = 0; nt < 5; ++nt) {
    const unsigned a = cvtpk(acc[nt][0], acc[nt][1]), b2 = cvtpk(acc[nt][2], acc[nt][3]);
#pragma unroll
    for (int e = 0; e < 4; ++e) {
      const int n = 16 * nt + 4 * g + e, hh = n / 18;
      const unsigned v = e < 2 ? a : b2;
      if (nt < 4 || g < 2) ldsw<u16>(lds, LDS_QT + (16 * w + li) * 192 + hh * 48 + 2 * (n - 18 * hh), (u16)((e & 1) ? v >> 16 : v));
    }
  }
  asm volatile("s_waitcnt lgkmcnt(0)" ::: "memory");
  FLAG_ADD(F_QDONE);
}

template <bool ISV> DI void kv_products(const char* lds, int lane, const u32x4 (&xf)[18], f32x4 (&acc)[2]) {
  const int li = lane & 15, g = lane >> 4, q = li >> 2, p = lane & 3;
  f32x4 a0[2] = {}, a1[2] = {};
  const unsigned wo_ = LDS_WKV + (8 * g + q) * 40 + 8 * p;
  s16x4 wr[4][4];
#define WKV_RD(ks) do { _Pragma("unroll") for (int e = 0; e < 4; ++e) wr[(ks) & 3][e] = ldstr(lds, wo_ + 32 * (ks) * 40 + (e >> 1) * 32 + (e & 1) * 160); } while (0)
  WKV_RD(0); WKV_RD(1); WKV_RD(2);
#pragma unroll
  for (int ks = 0; ks < 18; ++ks) {
    if (ks + 3 < 18) WKV_RD(ks + 3);
    SBAR();
#pragma unroll
    for (int nt = 0; nt < 2; ++nt) {
      const auto wfr = __builtin_shufflevector(wr[ks & 3][2 * nt], wr[ks & 3][2 * nt + 1], 0, 1, 2, 3, 4, 5, 6, 7);
      f32x4& a = (ks & 1) ? a1[nt] : a0[nt];
      if (ISV) a = __builtin_amdgcn_mfma_f32_16x16x32_bf16(__builtin_bit_cast(bf16x8, xf[ks]), __builtin_bit_cast(bf16x8, wfr), a, 0, 0, 0);
      else a = __builtin_amdgcn_mfma_f32_16x16x32_bf16(__builtin_bit_cast(bf16x8, wfr), __builtin_bit_cast(bf16x8, xf[ks]), a, 0, 0, 0);
    }
    SBAR();
  }
#undef WKV_RD
#pragma unroll
  for (int e = 0; e < 4; ++e) { acc[0][e] = a0[0][e] + a1[0][e]; acc[1][e] = a0[1][e] + a1[1][e]; }
}
DI void kv_wave(int b, int rb, int hg, const u16* x, char* ws, char* lds, unsigned lds0, int lane) {
  const int li = lane & 15, g = lane >> 4;
  const bool isv = hg >> 2;
  u32x4 xf[18];
  lds_wait<LPOLL, 3>(lds, F_GF + 0, NI);
  unsigned xb[2];
  x_bases(lane, xb);
  xb[0] += LDS_CV; xb[1] += LDS_CV;
#pragma unroll
  for (int ks = 0; ks < 18; ++ks) xf[ks] = XFRAG(xb, ks);
  asm volatile("s_waitcnt lgkmcnt(0)" ::: "memory");
  FLAG_ADD(F_GD + 0);
  lds_wait<LPOLL, 3>(lds, F_WKV, 4);
  STAMP(1, 2);
  f32x4 acc[2];
  if (isv) kv_products<true>(lds, lane, xf, acc); else kv_products<false>(lds, lane, xf, acc);
  STAMP(1, 4);
  const int kt = 2 * rb + ((hg & 3) >> 1), s = hg & 1, kh = kt >> 5, t = kt & 31;
  const __amdgpu_buffer_rsrc_t rs = rsrc(ws + WS_KV, 163840);
  if (!isv) {
    const int img = t ? ((t - 1) >> 2) * KV_SEG + kh * KV_HALF + ((t - 1) & 3) * KV_TILE : KV_HDR + kh * 1152;
    const float sc = 0.23570226039551584f * 1.4426950408889634f;
    const int key = 16 * s + li, e = (key & 0x13) | ((key & 4) << 1) | ((key & 8) >> 1);
    const unsigned d0 = cvtpk(acc[0][0] * sc, acc[0][1] * sc), d1 = cvtpk(acc[0][2] * sc, acc[0][3] * sc);
    const unsigned d2 = g ? 0u : cvtpk(acc[1][0] * sc, acc[1][1] * sc);
    __builtin_amdgcn_raw_buffer_store_b64(u32x2{d0, d1}, rs, img + (g >> 1) * 512 + e * 16 + 8 * (g & 1), 0, SC1);
    if (g == 0) __builtin_amdgcn_raw_buffer_store_b32(d2, rs, img + 1024 + e * 4, 0, SC1);
    float n2 = bflo(d0) * bflo(d0) + bfhi(d0) * bfhi(d0) + bflo(d1) * bflo(d1) + bfhi(d1) * bfhi(d1) + bflo(d2) * bflo(d2) + bfhi(d2) * bfhi(d2);
    n2 = wave_max(xrow16_sum(n2));
    if (lane == 0) __hip_atomic_store((gu32*)(ws + WS_KMAX) + 4 * rb + hg, __float_as_uint(n2), __ATOMIC_RELAXED, __HIP_MEMORY_SCOPE_AGENT);
  } else {
    const int img = (t >> 2) * KV_SEG + kh * KV_HALF + (t & 3) * KV_TILE + 1152 + (2 * s + (g >> 1)) * KV_VP + 8 * (g & 1);
    __builtin_amdgcn_raw_buffer_store_b64(u32x2{cvtpk(acc[0][0], acc[0][1]), cvtpk(acc[0][2], acc[0][3])}, rs, img + li * 16, 0, SC1);
    u32x2 v = {cvtpk(acc[1][0], acc[1][1]), cvtpk(acc[1][2], acc[1][3])};
    if (li == 2) v = u32x2{0x3f803f80u, 0x3f803f80u};
    if (li == 3) v = u32x2{0u, 0u};
    if (li < 4) __builtin_amdgcn_raw_buffer_store_b64(v, rs, img + (16 + li) * 16, 0, SC1);
  }
  const u64 tag = get_tag(lds);
  WAITV(0);
  if (lane < 8) tag_store(ws + WS_KVF + 2048 * lane + 8 * b, tag);
  flag_set(lds, F_KVPUB, 1);
  STAMP(1, 3);
  __builtin_amdgcn_s_sleep(KV_DELAY);
  tags_wait<256, KV_POLL>(ws + WS_KVF + 2048 * (b & 7), tag, lane);
  flag_set(lds, F_KVOK, 1);
  if (b == 0 && lane == 0) tag_store(ws + WS_EPOCH, tag);
  const __amdgpu_buffer_rsrc_t rsM = rsrc(ws + WS_KMAX, 512);
  const float km = wave_max(fmaxf(__uint_as_float(__builtin_amdgcn_raw_buffer_load_b32(rsM, 4 * lane, 0, SC1)),
                                  __uint_as_float(__builtin_amdgcn_raw_buffer_load_b32(rsM, 256 + 4 * lane, 0, SC1))));
  flag_set(lds, F_KMAX, __float_as_uint(km));
  flag_set(lds, F_KMOK, 1);
}

DI void ut_wave(int rb, int hg, const u16* u, char* ws, char* lds, int lane) {
  u32x4 v[9];
#pragma unroll
  for (int j = 0; j < 9; ++j) { const int ch = 64 * j + lane, row = ch / 9, c = ch - 9 * row; v[j] = *(const u32x4*)(u + (size_t)(64 * rb + row) * DM + 72 * hg + 8 * c); }
#pragma unroll
  for (int j = 0; j < 9; ++j) ldsw<u32x4>(lds, LDS_UA + (64 * j + lane) * 16, v[j]);
  const __amdgpu_buffer_rsrc_t rs = rsrc(ws + WS_UT + (size_t)72 * hg * (UT_ROW * 2), 72 * UT_ROW * 2);
  const int g = lane >> 4, li = lane & 15, q = li >> 2, p = lane & 3;
  u32x4 t[10];
#pragma unroll
  for (int i = 0; i < 10; ++i) {
    const unsigned ad = LDS_UA + (8 * (4 * (i & 1) + g) + q) * 144 + 32 * (i >> 1) + 8 * p;
    t[i] = __builtin_bit_cast(u32x4, __builtin_shufflevector(ldstr(lds, ad), ldstr(lds, ad + 4 * 144), 0, 1, 2, 3, 4, 5, 6, 7));
  }
  asm volatile("s_waitcnt lgkmcnt(0)" ::: "memory");
  FLAG_ADD(F_UDONE);
  lds_wait<4>(lds, UT_WAIT, 1);
#pragma unroll
  for (int i = 0; i < 10; ++i)
    if (i < 8 || li < 8) __builtin_amdgcn_raw_buffer_store_b128(t[i], rs, (16 * (i >> 1) + li) * (UT_ROW * 2) + 16 + 128 * rb + 16 * (4 * (i & 1) + g), 0, SC1);
  if (rb == 0 || rb == 31) {
#pragma unroll
    for (int j = 0; j < 2; ++j)
      if (64 * j + lane < 72) __builtin_amdgcn_raw_buffer_store_b128(u32x4{0, 0, 0, 0}, rs, (64 * j + lane) * (UT_ROW * 2) + (rb ? 16 + SEQ * 2 : 0), 0, SC1);
  }
  const u64 tag = get_tag(lds);
  WAITV(0);
  if (lane == 0) tag_store(ws + WS_UF + 8 * (32 * hg + rb), tag);
}

DI f32x2 cmul(f32x2 a, f32x2 b) { return f32x2{__builtin_fmaf(a[0], b[0], -(a[1] * b[1])), __builtin_fmaf(a[0], b[1], a[1] * b[0])}; }
DI f32x2 cexp_l(float zr, float zi, float l) {
  const float mag = __builtin_amdgcn_exp2f((zr * l) * 1.4426950408889634f);
  const float rev = __builtin_amdgcn_fractf((zi * l) * 0.15915494309189535f);
  return f32x2{mag * __builtin_amdgcn_cosf(rev), mag * __builtin_amdgcn_sinf(rev)};
}
DI f32x2 cis2(float e2, float rev) {
  const float mag = __builtin_amdgcn_exp2f(e2), f = __builtin_amdgcn_fractf(rev);
  return f32x2{mag * __builtin_amdgcn_cosf(f), mag * __builtin_amdgcn_sinf(f)};
}
DI void gen_wave(int d, const u16* Are, const u16* Aim, const u16* Bre, const u16* Bim, const u16* Cre, const u16* Cim,
                 const u16* logdt, char* lds, int lane) {
  const int r = lane & 31, h = lane >> 5;
  u16 pr[2][6];
#pragma unroll
  for (int pass = 0; pass < 2; ++pass) {
    const int idx = d * NST + 64 * pass + lane;
    pr[pass][0] = Are[idx]; pr[pass][1] = Aim[idx]; pr[pass][2] = Bre[idx]; pr[pass][3] = Bim[idx]; pr[pass][4] = Cre[idx]; pr[pass][5] = Cim[idx];
  }
  const float dt = rbf(expf(bf2f(logdt[d])));
  const float fm = (float)r;
  f32x16 acc = {};
#pragma unroll 1
  for (int pass = 0; pass < 2; ++pass) {
    const u16 p0 = pass ? pr[1][0] : pr[0][0], p1 = pass ? pr[1][1] : pr[0][1], p2_ = pass ? pr[1][2] : pr[0][2];
    const u16 p3_ = pass ? pr[1][3] : pr[0][3], p4_ = pass ? pr[1][4] : pr[0][4], p5 = pass ? pr[1][5] : pr[0][5];
    const float zr = dt * bf2f(p0), zi = dt * bf2f(p1);
    const float dBr = dt * bf2f(p2_), dBi = dt * bf2f(p3_);
    const float cr = bf2f(p4_), ci = bf2f(p5);
    const f32x2 c = {cr * dBr - ci * dBi, cr * dBi + ci * dBr};
    ldsw<f32x2>(lds, GEN_P + 8 * lane, f32x2{zr * (8.f * 1.4426950408889634f), zi * (-8.f * 0.15915494309189535f)});
    const f32x2 w1 = cexp_l(zr, zi, 1.f), w256 = cexp_l(zr, zi, 256.f);
#define PUTY(col, v) ldsw<unsigned>(lds, (col) * GEN_PITCH + 4 * lane, cvtpk((v)[0], (v)[1]))
    f32x2 y[8];
    y[0] = c;
#pragma unroll
    for (int i = 1; i < 8; ++i) y[i] = cmul(y[i - 1], w1);
#pragma unroll
    for (int i = 0; i < 8; ++i) PUTY(i, y[i]);
#pragma unroll
    for (int i = 0; i < 4; ++i) { const f32x2 t = cmul(y[2 * i], w256); PUTY(8 + i, t); }
    const f32x2 p2 = cmul(w256, w256), p3 = cmul(p2, w256), p4 = cmul(p2, p2);
    { f32x2 t = cmul(y[0], p2); PUTY(12, t); t = cmul(y[4], p2); PUTY(13, t); }
    { f32x2 t = cmul(y[0], p3); PUTY(14, t); t = cmul(y[4], p3); PUTY(15, t); }
    { const f32x2 t4 = cmul(y[0], p4); PUTY(16, t4);
      f32x2 t = cmul(t4, w256); PUTY(17, t); t = cmul(t4, p2); PUTY(18, t); t = cmul(t4, p3); PUTY(19, t); t = cmul(t4, p4); PUTY(20, t); }
#undef PUTY
    asm volatile("" ::: "memory");
    const int yr = r <= 20 ? r : r - 16;
#pragma unroll 2
    for (int ks = 0; ks < 8; ++ks) {
      const f32x4 z0 = ldsr<f32x4>(lds, GEN_P + 64 * ks + 32 * h), z1 = ldsr<f32x4>(lds, GEN_P + 64 * ks + 32 * h + 16);
      const u32x4 fb = ldsr<u32x4>(lds, yr * GEN_PITCH + 32 * ks + 16 * h);
      const f32x2 a0 = cis2(z0[0] * fm, z0[1] * fm), a1 = cis2(z0[2] * fm, z0[3] * fm), a2 = cis2(z1[0] * fm, z1[1] * fm), a3 = cis2(z1[2] * fm, z1[3] * fm);
      const u32x4 fa = {cvtpk(a0[0], a0[1]), cvtpk(a1[0], a1[1]), cvtpk(a2[0], a2[1]), cvtpk(a3[0], a3[1])};
      acc = MFMA16(fa, fb, acc);
    }
    asm volatile("" ::: "memory");
  }
  const int c = r;
  const int hi = c < 8 ? c : c < 12 ? 256 + 2 * (c - 8) : c < 16 ? 512 + 256 * ((c - 12) >> 1) + 4 * ((c - 12) & 1) : 1024 + 256 * (c - 16);
  if (c < 20) {
#pragma unroll
    for (int i = 0; i < 16; ++i) ldsw<u16>(lds, 2 * (8 * crow(i, h) + hi), (u16)cvtpk(acc[i], acc[i]));
  } else if (c == 20 && h == 0) {
    ldsw<u16>(lds, 2 * 2048, (u16)cvtpk(acc[0], acc[0]));
  }
  asm volatile("" ::: "memory");
}
DI void build_copies(const char* kc, char* cl, int lane) {
  auto rd = [&](int l) -> unsigned { return ldsr<u16>(kc, 2 * l); };
  auto pk = [](unsigned lo, unsigned hi) -> unsigned { return lo | (hi << 16); };
  auto put2 = [&](int par, int y, unsigned v0, unsigned v1) {
    ldsw<u32x2>(cl, par * CV_K + 4 * y, u32x2{v0, v1});
    ldsw<unsigned>(cl, (par + 2) * CV_K + 4 * y - 4, v0); ldsw<unsigned>(cl, (par + 2) * CV_K + 4 * y, v1);
  };
  const int L = lane;
  {
    const unsigned e0 = rd(4 * L), e1 = rd(4 * L + 1), e2 = rd(4 * L + 2), e3 = rd(4 * L + 3);
    const unsigned pv = L ? rd(4 * L - 1) : 0u;
    put2(0, 1028 - 2 * L, pk(e3, e2), pk(e1, e0));
    put2(1, 1028 - 2 * L, pk(e2, e1), pk(e0, pv));
  }
  {
    const int j0 = 256 + 4 * L;
    const unsigned a = rd(j0), b = rd(j0 + 2), c = rd(j0 + 4), pv = L ? a : rd(255);
    put2(0, 1028 - j0 / 2, pk(c, b), pk(a, a));
    put2(1, 1028 - j0 / 2, pk(b, a), pk(a, pv));
  }
  {
    const int j0 = 512 + 8 * L, y = 1026 - j0 / 2;
    const unsigned a = rd(j0), b = rd(j0 + 4), c = rd(j0 + 8);
    const unsigned aa = pk(a, a), bb = pk(b, b), cc = pk(c, c);
    put2(0, y, cc, bb);        put2(0, y + 2, pk(b, a), aa);
    put2(1, y, pk(c, b), bb);  put2(1, y + 2, aa, aa);
  }
  {
    const int j0 = 1024 + 16 * L, y = 1022 - j0 / 2;
    const unsigned a = rd(j0), b = rd(j0 + 8), c = rd(j0 + 16);
    const unsigned aa = pk(a, a), bb = pk(b, b), cc = pk(c, c);
    put2(0, y, cc, cc);        put2(0, y + 2, bb, bb);  put2(0, y + 4, bb, pk(b, a));  put2(0, y + 6, aa, aa);
    put2(1, y, cc, pk(c, b));  put2(1, y + 2, bb, bb);  put2(1, y + 4, bb, aa);        put2(1, y + 6, aa, aa);
    if (L == 63) {
      ldsw<unsigned>(cl, CV_K + 4 * 5, pk(0u, c)); ldsw<unsigned>(cl, 3 * CV_K + 4 * 4, pk(0u, c));
    }
  }
  if (L < 6) { ldsw<unsigned>(cl, 4 * L, 0u); if (L) ldsw<unsigned>(cl, 2 * CV_K + 4 * L - 4, 0u); }
  if (L < 5) { ldsw<unsigned>(cl, CV_K + 4 * L, 0u); if (L) ldsw<unsigned>(cl, 3 * CV_K + 4 * L - 4, 0u); }
  if (L < 42) {
    ldsw<unsigned>(cl, 4 * (1030 + L), 0u); ldsw<unsigned>(cl, CV_K + 4 * (1030 + L), 0u);
    ldsw<unsigned>(cl, 2 * CV_K + 4 * (1029 + L), 0u); ldsw<unsigned>(cl, 3 * CV_K + 4 * (1029 + L), 0u);
  }
}
template <int SW> DI void u_fill(int d, const char* ws, char* cl, unsigned cl0, int lane) {
  const char* ur = ws + WS_UT + (size_t)d * (UT_ROW * 2);
#pragma unroll
  for (int item = SW; item < 10; item += 4) {
    const int bb = item / 5, part = item - 5 * bb, sg = 136 + part * 64 + lane, qc = sg - sg / 9, s0 = 8 * qc - 976 + 4 * bb;
    if (sg <= 424) glds16c(ur, sg % 9 == 8 ? 0u : 2 * (8 + s0), cl0 + CV_U + bb * CV_UP + (136 + part * 64) * 16);
  }
  if (SW < 2) {
#pragma unroll
    for (int j = 0; j < 3; ++j)
      if (64 * j + lane < 136) ldsw<u32x4>(cl, CV_U + SW * CV_UP + (64 * j + lane) * 16, u32x4{0, 0, 0, 0});
  }
}
template <int SW> DI void u_fill2(int d0, const char* ws, char* lds, unsigned lds0, int lane) {
  u_fill<SW>(d0, ws, lds + LDS_CV, lds0 + LDS_CV, lane);
  u_fill<(SW + 2) & 3>(d0 + 1, ws, lds + LDS_CV + CV_CH, lds0 + LDS_CV + CV_CH, lane);
}

struct KVAddr { unsigned k, kx, v; };
#define LDK(dst, dstx, t_off) do { dst = ldsr<u32x4>(lds, ad.k + (t_off)); dstx[0] = ldsr<unsigned>(lds, ad.kx + (t_off)); } while (0)
#define LDV(d0, d1, t_off) do { d0 = ldsr<u32x4>(lds, ad.v + (t_off)); d1 = ldsr<u32x4>(lds, ad.v + 2 * KV_VP + (t_off)); } while (0)
#define EX2(x) __builtin_amdgcn_exp2f(x)
#define GRP(MF, S, P, i) do { MF; SBAR(); \
    { const float e0 = EX2(S[4 * (i)]), e1 = EX2(S[4 * (i) + 1]), e2 = EX2(S[4 * (i) + 2]), e3 = EX2(S[4 * (i) + 3]); \
      unsigned t0_ = cvtpk(e0, e1), t1_ = cvtpk(e2, e3); PIN(t0_); PIN(t1_); P[2 * (i)] = t0_; P[2 * (i) + 1] = t1_; } \
    SBAR(); } while (0)
typedef unsigned u32x8 __attribute__((ext_vector_type(8)));
DI u32x4 lo4(const u32x8& p) { return __builtin_shufflevector(p, p, 0, 1, 2, 3); }
DI u32x4 hi4(const u32x8& p) { return __builtin_shufflevector(p, p, 4, 5, 6, 7); }

#define STEP(Kc, Kcx, Kn, Knx, Vp0, Vp1, Vc0, Vc1, off) do { \
    LDK(Kn, Knx, (off)); LDV(Vc0, Vc1, (off)); SBAR(); \
    GRP(sb = MFMA16(Kc, qbm, zero16), sa, pa, 0); \
    GRP(ob = MFMA16(Vp0, lo4(pb), ob), sa, pa, 1); \
    GRP(sb = MFMA8(Kcx, qbx, sb), sa, pa, 2); \
    GRP(ob = MFMA16(Vp1, hi4(pb), ob), sa, pa, 3); \
    GRP(sa = MFMA16(Kn, qam, zero16), sb, pb, 0); \
    GRP(oa = MFMA16(Vc0, lo4(pa), oa), sb, pb, 1); \
    GRP(sa = MFMA8(Knx, qax, sa), sb, pb, 2); \
    GRP(oa = MFMA16(Vc1, hi4(pa), oa), sb, pb, 3); \
  } while (0)

DI void slow_tile(const u32x4& kf, const u32x2& kx, const u32x4& v0, const u32x4& v1, const u32x4& qm, const u32x2& qx, f32x16& o, float& m) {
  f32x16 s = {};
  s = MFMA16(kf, qm, s);
  s = MFMA8(kx, qx, s);
  float mx = s[0];
#pragma unroll
  for (int i = 1; i < 16; ++i) mx = fmaxf(mx, s[i]);
  mx = half_max(mx);
  const float mn = fmaxf(m, mx);
  const float alpha = EX2(m - mn);
  m = mn;
#pragma unroll
  for (int i = 0; i < 16; ++i) o[i] *= alpha;
  u32x8 p;
#pragma unroll
  for (int i = 0; i < 8; ++i) p[i] = cvtpk(EX2(s[2 * i] - mn), EX2(s[2 * i + 1] - mn));
  o = MFMA16(v0, lo4(p), o);
  o = MFMA16(v1, hi4(p), o);
}

template <bool TWO, bool SKIP0> DI void conv_steps(const char* lds, unsigned pa0, unsigned pb0, unsigned pa1, unsigned pb1, f32x16& c0, f32x16& c1) {
  __builtin_amdgcn_s_setprio(CONV_PRIO);
  u32x4 a0[2], b0[2], a1[2], b1[2];
#define CLD(i, n) do { a0[i] = ldsr<u32x4>(lds, pa0 + 32 * (n)); b0[i] = ldsr<u32x4>(lds, pb0 + 32 * (n) + 16 * ((n) >> 2)); \
    if (TWO) { a1[i] = ldsr<u32x4>(lds, pa1 + 32 * (n)); b1[i] = ldsr<u32x4>(lds, pb1 + 32 * (n) + 16 * ((n) >> 2)); } } while (0)
#define CMM(i) do { c0 = MFMA16(a0[i], b0[i], c0); if (TWO) c1 = MFMA16(a1[i], b1[i], c1); } while (0)
  CLD(0, 0); CLD(1, 1);
  c0 = MFMA16(a0[0], b0[0], c0);
  if (TWO && !SKIP0) c1 = MFMA16(a1[0], b1[0], c1);
  SBAR();
  typedef const LDSP char* lptr;
  lptr qa0 = (lptr)lds + pa0, qb0 = (lptr)lds + pb0, qa1 = (lptr)lds + pa1, qb1 = (lptr)lds + pb1;
  PIN(qa0); PIN(qb0);
  if (TWO) { PIN(qa1); PIN(qb1); }
#define LRD(p, o) (*(const LDSP u32x4*)((p) + (o)))
#define CLD4(i, k) do { a0[i] = LRD(qa0, 32 * (k)); b0[i] = LRD(qb0, 32 * (k) + 16 * ((k) >> 2)); \
    if (TWO) { a1[i] = LRD(qa1, 32 * (k)); b1[i] = LRD(qb1, 32 * (k) + 16 * ((k) >> 2)); } } while (0)
#pragma unroll
  for (int n = 1; n < 65; n += 2) {
    CLD4(0, n + 1); SBAR(); CMM(1); SBAR();
    if (n + 2 < 65) CLD4(1, n + 2);
    SBAR(); CMM(0); SBAR();
  }
#undef CLD4
#undef LRD
#undef CLD
#undef CMM
  __builtin_amdgcn_s_setprio(3);
}
DI void conv_out(const f32x16& c, int d, int tl, char* ws, char* scratch, int lane) {
  const int r = lane & 31, h = lane >> 5;
#pragma unroll
  for (int g = 0; g < 4; ++g)
    ldsw<u32x2>(scratch, 2 * (16 * g + 8 * h + 4 * (r & 1) + 64 * (r >> 1)), u32x2{cvtpk(c[4 * g], c[4 * g + 1]), cvtpk(c[4 * g + 2], c[4 * g + 3])});
  const __amdgpu_buffer_rsrc_t rsY = __builtin_amdgcn_make_buffer_rsrc((void*)(ws + WS_YT + (size_t)d * (SEQ * 2) + 2048 * tl), 0, 2048, kRsrc3);
#pragma unroll
  for (int j = 0; j < 2; ++j) __builtin_amdgcn_raw_buffer_store_b128(ldsr<u32x4>(scratch, 1024 * j + 16 * lane), rsY, 1024 * j + 16 * lane, 0, 16);
}

struct SkipRegs { u32x4 y[3], u[3], d[3]; };
DI void skip_load(SkipRegs& s, int rb, int hg, int tq, const u16* u, const u16* dskip, char* ws, char* lds, int lane) {
  tags_wait<32, 2>(ws + WS_YF + 8 * 32 * hg, get_tag(lds), lane);
  const __amdgpu_buffer_rsrc_t rsY = rsrc(ws + WS_YT + (size_t)72 * hg * (SEQ * 2), 72 * SEQ * 2);
#pragma unroll
  for (int j = 0; j < 3; ++j) {
    const int ch = 64 * j + lane < 144 ? 64 * j + lane : 0, row = ch / 9, c = ch - 9 * row;
    s.y[j] = __builtin_amdgcn_raw_buffer_load_b128(rsY, (ch >> 1) * (SEQ * 2) + 128 * rb + 32 * tq + 16 * (ch & 1), 0, SC1);
    s.u[j] = *(const u32x4*)(u + (size_t)(64 * rb + 16 * tq + row) * DM + 72 * hg + 8 * c);
    s.d[j] = *(const u32x4*)(dskip + 72 * hg + 8 * c);
  }
}
DI void skip_finish(const SkipRegs& s, int rb, int hg, int tq, u16* out, char* lds, int lane) {
  char* ia = lds + LDS_FA + tq * 4608;
  char* ib = ia + 2304;
#pragma unroll
  for (int j = 0; j < 3; ++j)
    if (64 * j + lane < 144) ldsw<u32x4>(ia, (64 * j + lane) * 16, s.y[j]);
  const int g = lane >> 4, li = lane & 15, q = li >> 2, p = lane & 3;
#pragma unroll
  for (int j = 0; j < 3; ++j) {
    const int oct = 4 * j + g < 9 ? 4 * j + g : 8;
    const unsigned ad = (8 * oct + q) * 32 + 8 * p;
    const u32x4 t = __builtin_bit_cast(u32x4, __builtin_shufflevector(ldstr(ia, ad), ldstr(ia, ad + 4 * 32), 0, 1, 2, 3, 4, 5, 6, 7));
    if (4 * j + g < 9) ldsw<u32x4>(ib, li * 144 + 16 * oct, t);
  }
#pragma unroll
  for (int j = 0; j < 3; ++j) {
    const int ch = 64 * j + lane < 144 ? 64 * j + lane : 0, row = ch / 9, c = ch - 9 * row;
    const u32x4 yy = ldsr<u32x4>(ib, ch * 16), dd = s.d[j];
    u32x4 o;
#pragma unroll
    for (int k = 0; k < 4; ++k) {
      const unsigned sk = cvtpk(bflo(s.u[j][k]) * bflo(dd[k]), bfhi(s.u[j][k]) * bfhi(dd[k]));
      o[k] = cvtpk(bflo(yy[k]) + bflo(sk), bfhi(yy[k]) + bfhi(sk));
    }
    if (64 * j + lane < 144) *(u32x4*)(out + (size_t)(64 * rb + 16 * tq + row) * (2 * DM) + DM + 72 * hg + 8 * c) = o;
  }
}

DI void tail_part(const f32x16& oa, const f32x16& ob, float ma, float mb, int w, int rb, int hg, const u16* u, const u16* dskip, char* ws, u16* out, char* lds) {
  int lane;
  asm volatile("v_mbcnt_lo_u32_b32 %0, -1, 0\n\tv_mbcnt_hi_u32_b32 %0, -1, %0" : "=v"(lane));
  const int r = lane & 31, h = lane >> 5, pt = w & 1, hp = (w >> 1) & 1, kh = w >> 2;
  SkipRegs sk;
  if (w < 4) skip_load(sk, rb, hg, w, u, dskip, ws, lds, lane);
  BAR();

  {
    const f32x16& give = kh ? oa : ob;
    const float gm = kh ? ma : mb;
    char* own = lds + LDS_CMB + w * 3328;
#pragma unroll
    for (int g = 0; g < 3; ++g) ldsw<f32x4>(own, g * 1024 + lane * 16, f32x4{give[4 * g], give[4 * g + 1], give[4 * g + 2], give[4 * g + 3]});
    ldsw<float>(own, 3072 + lane * 4, gm);
    BAR();
    const char* oth = lds + LDS_CMB + (w ^ 4) * 3328;
    f32x16 keep = kh ? ob : oa;
    const float m1 = kh ? mb : ma, m2 = ldsr<float>(oth, 3072 + lane * 4);
    const float mm = fmaxf(m1, m2), f1 = EX2(m1 - mm), f2 = EX2(m2 - mm);
    float o[12];
#pragma unroll
    for (int g = 0; g < 3; ++g) {
      const f32x4 t = ldsr<f32x4>(oth, g * 1024 + lane * 16);
#pragma unroll
      for (int e = 0; e < 4; ++e) o[4 * g + e] = keep[4 * g + e] * f1 + t[e] * f2;
    }
    const float inv = 1.0f / half_lo(o[10]);
    const unsigned ot = LDS_OT + (32 * pt + r) * 144 + (2 * hp + kh) * 36;
    ldsw<unsigned>(lds, ot + 8 * h, cvtpk(o[0] * inv, o[1] * inv));
    ldsw<unsigned>(lds, ot + 8 * h + 4, cvtpk(o[2] * inv, o[3] * inv));
    ldsw<unsigned>(lds, ot + 16 + 8 * h, cvtpk(o[4] * inv, o[5] * inv));
    ldsw<unsigned>(lds, ot + 16 + 8 * h + 4, cvtpk(o[6] * inv, o[7] * inv));
    if (!h) ldsw<unsigned>(lds, ot + 32, cvtpk(o[8] * inv, o[9] * inv));
  }
  BAR();
  const __amdgpu_buffer_rsrc_t rsO = rsrc(ws + WS_O + (size_t)64 * rb * (DM * 2), 64 * DM * 2);
#pragma unroll
  for (int n = 0; n < 2; ++n) {
    if (n == 0 || w == 0) {
      const int pair = 4 * (n ? 8 : w) + (lane >> 4), pt4 = pair / 9, c = pair - 9 * pt4, li = lane & 15, c8 = 9 * hg + c;
      __builtin_amdgcn_raw_buffer_store_b128(ldsr<u32x4>(lds, LDS_OT + (16 * pt4 + li) * 144 + 16 * c), rsO,
                                             ((pt4 * 18 + (c8 >> 2)) * 64 + (c8 & 3) * 16 + li) * 16, 0, SC1);
    }
  }
  asm volatile("s_waitcnt vmcnt(0)" ::: "memory");
  BAR();
  STAMP(1, 9);
  if (w < 4) skip_finish(sk, rb, hg, w, out, lds, lane);
  if (w == 4) {
    const u64 tag = get_tag(lds);
    if (lane == 0) tag_store(ws + WS_OF + 8 * (8 * rb + hg), tag);
    tags_wait<8, 1>(ws + WS_OF + 64 * rb, tag, lane);
  }
  BAR();
  STAMP(1, 10);
  const int li = lane & 15, g = lane >> 4, q = li >> 2, p = lane & 3, pt4 = w & 3, k2 = w >> 2;
  u32x4 xf[9];
#pragma unroll
  for (int ks = 0; ks < 9; ++ks) xf[ks] = __builtin_amdgcn_raw_buffer_load_b128(rsO, ((pt4 * 18 + 9 * k2 + ks) * 64 + lane) * 16, 0, SC1);
  STAMP(1, 11);
  f32x4 acc[5] = {};
  const unsigned wb = LDS_CV + (32 * 9 * k2 + 8 * g + q) * 144 + 8 * p;
  s16x4 wa[2][10];
#define WRD(buf, ks) do { _Pragma("unroll") for (int nt = 0; nt < 5; ++nt) { \
    wa[buf][2 * nt] = ldstr(lds, wb + 32 * (ks) * 144 + 32 * nt); wa[buf][2 * nt + 1] = ldstr(lds, wb + (32 * (ks) + 4) * 144 + 32 * nt); } } while (0)
  WRD(0, 0);
#pragma unroll
  for (int ks = 0; ks < 9; ++ks) {
    if (ks + 1 < 9) WRD((ks + 1) & 1, ks + 1);
    SBAR();
#pragma unroll
    for (int nt = 0; nt < 5; ++nt) {
      const auto a = __builtin_shufflevector(wa[ks & 1][2 * nt], wa[ks & 1][2 * nt + 1], 0, 1, 2, 3, 4, 5, 6, 7);
      acc[nt] = __builtin_amdgcn_mfma_f32_16x16x32_bf16(__builtin_bit_cast(bf16x8, a), __builtin_bit_cast(bf16x8, xf[ks]), acc[nt], 0, 0, 0);
    }
    SBAR();
  }
#undef WRD
  STAMP(1, 12);
  if (k2) {
#pragma unroll
    for (int nt = 0; nt < 5; ++nt) ldsw<f32x4>(lds, LDS_CMB + (pt4 * 5 + nt) * 1024 + lane * 16, acc[nt]);
  }
  BAR();
  if (k2) return;
  u16* orow = out + (size_t)(64 * rb + 16 * pt4 + li) * (2 * DM) + 72 * hg + 4 * g;
#pragma unroll
  for (int nt = 0; nt < 5; ++nt) {
    const f32x4 t = ldsr<f32x4>(lds, LDS_CMB + (pt4 * 5 + nt) * 1024 + lane * 16);
    if (nt < 4 || lane < 32)
      *(u32x2*)(orow + 16 * nt) = u32x2{cvtpk(acc[nt][0] + t[0], acc[nt][1] + t[1]), cvtpk(acc[nt][2] + t[2], acc[nt][3] + t[3])};
  }
  asm volatile("fk_pf_end:");
  STAMP(1, 15);
}

DI void seg0_share(int i, const char* ws, char* lds, unsigned lds0, int lane) {
  const char* kvg = ws + WS_KV;
  lds_wait<LPOLL, 3>(lds, F_KVOK, 1);
#pragma unroll
  for (int j = 0; j < 6; ++j) {
    const int p = i + 4 * j;
    if (p < 19) glds16c(kvg + 1024 * p, 16 * lane, lds0 + 1024 * p);
    else if (p < 22) { if (p < 21 || lane < 16) glds16c(kvg + KV_HDR + 1024 * (p - 19), 16 * lane, lds0 + LDS_K0 + 1024 * (p - 19)); }
  }
  WAITV(0);
  FLAG_ADD(F_SEG0);
}
template <int K> DI void ring_loader(const char* ws, char* lds, unsigned lds0, int lane) {
  constexpr int NP = (19 - K + NLD - 1) / NLD;
  const char* kvg = ws + WS_KV;
  auto issue = [&](int s) {
#pragma unroll
    for (int j = 0; j < NP; ++j) glds16c(kvg + s * KV_SEG + 1024 * (K + NLD * j), 16 * lane, lds0 + (s % 3) * KV_SEG + 1024 * (K + NLD * j));
  };
  __builtin_amdgcn_s_setprio(3);
  lds_wait<1>(lds, F_UDONE, 1);
  issue(1);
  lds_wait<1>(lds, F_QRD, 8);
  issue(2);
  waitv<NP>();
  flag_set(lds, F_FULL + K, 2);
  WAITV(0);
  flag_set(lds, F_FULL + K, 3);
#pragma unroll 1
  for (int s = 3; s < 8; ++s) {
    done_wait<RING_POLL>(lds, s - 2, lane);
    issue(s);
    WAITV(0);
    flag_set(lds, F_FULL + K, s + 1);
  }
}
DI unsigned ring_full(char* lds) {
  if (NLD == 1) return flag_get(lds, F_FULL);
  const u32x4 v = *(const volatile LDSP u32x4*)((LDSP char*)lds + LDS_FLAG + 4 * F_FULL);
  if (NLD == 1) return v[0];
  if (NLD == 2) return v[0] < v[1] ? v[0] : v[1];
  return v[0] < v[1] ? (v[0] < v[2] ? v[0] : v[2]) : (v[1] < v[2] ? v[1] : v[2]);
}
DI void ring_wait(char* lds, unsigned need) {
#pragma unroll 1
  for (unsigned spins = 0; spins < (1u << 20); ++spins) {
    if (rfl(ring_full(lds)) >= need) break;
    __builtin_amdgcn_s_sleep(1);
  }
  asm volatile("" ::: "memory");
}
DI void l2_touch(const char* base, int line0, int nlines, unsigned lds0, int lane) {
  unsigned sv;
  if (line0 + lane < nlines)
    asm volatile("s_mov_b32 %0, m0\n\ts_mov_b32 m0, %3\n\ts_nop 0\n\tglobal_load_lds_dword %1, %2\n\ts_mov_b32 m0, %0"
                 : "=&s"(sv) : "v"(128 * (line0 + lane)), "s"(base), "s"(lds0 + LDS_GEN + 3 * GEN_AREA) : "memory");
}

__global__ void __launch_bounds__(1024)
fused_kernel(const u16* x, const u16* wq, const u16* wk, const u16* wv, const u16* wo, const u16* u, const u16* Are, const u16* Aim,
             const u16* Bre, const u16* Bim, const u16* Cre, const u16* Cim, const u16* logdt, const u16* dskip, char* ws, u16* out) {
  extern __shared__ __attribute__((aligned(16))) char lds[];
  const unsigned lds0 = (unsigned)(uintptr_t)lds;
  const char* pc0;
  asm volatile("s_getpc_b64 %0" : "=s"(pc0));
  const int b = BID, lane = TID & 63, w = rfl(TID >> 6);
  const int rb = (b & 7) + 8 * (b >> 6), hg = (b >> 3) & 7;
  STAMP(1, 0);
  if (TID < 64) ((LDSP unsigned*)((LDSP char*)lds + LDS_FLAG))[TID] = 0u;
  BAR();
  const char* kvg = ws + WS_KV;
  if (w >= 14) {
    __builtin_amdgcn_s_setprio(2);
    if (w == 15) {
      const char* pc1;
      int dend;
      asm volatile("s_getpc_b64 %0\nfk_pf_here:\n\ts_mov_b32 %1, fk_pf_end-fk_pf_here" : "=s"(pc1), "=s"(dend));
      const int len = (int)(pc1 - pc0) + (dend > 0 ? dend : 0);
      l2_touch(pc0, (b >> 3) + 31 * lane, len >> 7, lds0, lane);
    }
    if (w == 14) q_loader<4>(x, wq, rb, hg, lds, lds0, lane); else q_loader<5>(x, wq, rb, hg, lds, lds0, lane);
    seg0_share(w - 12, ws, lds, lds0, lane);
    if (NLD > 1 && w == 14) ring_loader<1>(ws, lds, lds0, lane);
    if (NLD > 2 && w == 15) ring_loader<2>(ws, lds, lds0, lane);
    return;
  }
  if (w == 13) {
    __builtin_amdgcn_s_setprio(2);
    __builtin_amdgcn_s_sleep(UT_DELAY);
    ut_wave(rb, hg, u, ws, lds, lane);
    seg0_share(1, ws, lds, lds0, lane);
    STAMP(1, 15);
    return;
  }
  if (w == 12) {
    __builtin_amdgcn_s_setprio(3);
    const u64 tag = __hip_atomic_load((gu64*)(ws + WS_EPOCH), __ATOMIC_RELAXED, __HIP_MEMORY_SCOPE_AGENT) + 1;
    flag_set(lds, F_TAG, (unsigned)tag); flag_set(lds, F_TAG + 1, (unsigned)(tag >> 32)); flag_set(lds, F_TAGOK, 1);
    q_loader<3>(x, wq, rb, hg, lds, lds0, lane);
    STAMP(1, 3);
    seg0_share(0, ws, lds, lds0, lane);
    STAMP(1, 4);
    ring_loader<0>(ws, lds, lds0, lane);
    STAMP(1, 15);
    return;
  }
  if (w >= 8) {
    const int sw = w - 8, i = b >> 3, xc = b & 7;
    const int d0 = 72 * xc + 2 * i, dx = 72 * xc + 64 + (i >> 2), third = (i & 3) < 2;
    char* area = lds + LDS_GEN + (sw - 1) * GEN_AREA;
    if (sw == 0) {
      __builtin_amdgcn_s_setprio(3);
      kv_wave(b, rb, hg, x, ws, lds, lds0, lane);
      STAMP(1, 5);
    } else if (sw < 3 || third) {
      __builtin_amdgcn_s_setprio(GEN_PRIO);
      gen_wave(sw < 3 ? d0 + sw - 1 : dx, Are, Aim, Bre, Bim, Cre, Cim, logdt, area, lane);
      STAMP(1, 3);
    }
    __builtin_amdgcn_s_setprio(3);
    tags_wait<32, 4>(ws + WS_UF + 8 * 32 * xc, get_tag(lds), lane);
    lds_wait<2, 3>(lds, F_QDONE, 4);
    if (sw == 0) u_fill2<0>(d0, ws, lds, lds0, lane);
    else if (sw == 1) u_fill2<1>(d0, ws, lds, lds0, lane);
    else if (sw == 2) u_fill2<2>(d0, ws, lds, lds0, lane);
    else u_fill2<3>(d0, ws, lds, lds0, lane);
    if (sw != 0 && (sw < 3 || third)) build_copies(area, lds + LDS_CV + (sw - 1) * CV_CH, lane);
    if (third) {
      asm volatile("s_waitcnt lgkmcnt(0)" ::: "memory");
      FLAG_ADD(F_KCOP);
      lds_wait<LPOLL>(lds, F_KCOP, 4);
      char* c2 = lds + LDS_CV + 2 * CV_CH;
      const unsigned c20 = lds0 + LDS_CV + 2 * CV_CH;
      if (sw == 0) u_fill<0>(dx, ws, c2, c20, lane);
      else if (sw == 1) u_fill<1>(dx, ws, c2, c20, lane);
      else if (sw == 2) u_fill<2>(dx, ws, c2, c20, lane);
      else u_fill<3>(dx, ws, c2, c20, lane);
    }
    asm volatile("s_waitcnt vmcnt(0) lgkmcnt(0)" ::: "memory");
    FLAG_ADD(F_CFILL);
    STAMP(1, 6);
    {
      const int r = lane & 31, h = lane >> 5;
      const unsigned kb = LDS_CV + (3 - (r & 3)) * CV_K + 2 * (8 * h - 8 * (r >> 2));
      const unsigned ub = LDS_CV + CV_U + (r & 1) * CV_UP + 144 * (r >> 1) + 16 * h;
      const unsigned A1 = kb + 2 * 1080, A2 = kb + 2 * 56, B1 = ub, B3 = ub + 2304;
      char* scr = lds + LDS_SCR + sw * 2048;
      f32x16 c0 = {}, c1 = {};
      lds_wait<4>(lds, F_CFILL, 4);
      const int cu = CONV_MAP(sw);
      if (cu == 2) {
        conv_steps<true, false>(lds, A1, B1, A1 + CV_CH, B1 + CV_CH, c0, c1);
        conv_out(c0, d0, 0, ws, scr, lane);
        conv_out(c1, d0 + 1, 0, ws, scr, lane);
      } else if (cu < 2 || (i & 3) == 0) {
        const unsigned so = (cu < 2 ? cu : 2) * CV_CH;
        conv_steps<true, true>(lds, A2 + so, B1 + so, A1 + so, B3 + so, c0, c1);
#pragma unroll
        for (int k = 0; k < 16; ++k) c0[k] += c1[k];
        conv_out(c0, cu < 2 ? d0 + cu : dx, 1, ws, scr, lane);
      } else if (third) {
        conv_steps<false, false>(lds, A1 + 2 * CV_CH, B1 + 2 * CV_CH, 0, 0, c0, c1);
        conv_out(c0, dx, 0, ws, scr, lane);
      }
      STAMP(1, 7);
    }
    asm volatile("s_waitcnt vmcnt(0) lgkmcnt(0)" ::: "memory");
    unsigned arrived = 0;
    if (lane == 0) arrived = __hip_atomic_fetch_add((LDSP unsigned*)((LDSP char*)lds + LDS_FLAG) + F_CDONE, 1u, __ATOMIC_RELAXED, __HIP_MEMORY_SCOPE_WORKGROUP);
    if (rfl(arrived) == 3) { const u64 tag = get_tag(lds); if (lane == 0) tag_store(ws + WS_YF + 8 * (32 * xc + i), tag); }
    lds_wait<4>(lds, F_CDONE, 4);
    {
      unsigned vo[3];
#pragma unroll
      for (int k = 0; k < 3; ++k) { const int ch = 64 * (sw + 4 * k) + lane, row = ch / 9, c = ch - 9 * row; vo[k] = row * (DM * 2) + c * 16; }
      const char* wb = (const char*)(wo + 72 * hg);
#pragma unroll 1
      for (int a = 0; a < 9; ++a) {
#pragma unroll
        for (int k = 0; k < 3; ++k)
          if (k < 2 || sw == 0) glds16s(wb + (size_t)a * 64 * (DM * 2), vo[k], lds0 + LDS_CV + 1024 * (9 * a + sw + 4 * k));
      }
      STAMP(1, 8);
      WAITV(0);
    }
    STAMP(1, 15);
    return;
  }
  __builtin_amdgcn_s_setprio(3);
  if (w < 4) {
    q_compute(wk, wv, hg, lds, w, lane, ws);
  } else if (w == 4) {
    q_loader<6>(x, wq, rb, hg, lds, lds0, lane);
  } else {
    if (w == 5) q_loader<0>(x, wq, rb, hg, lds, lds0, lane);
    else if (w == 6) q_loader<1>(x, wq, rb, hg, lds, lds0, lane);
    else q_loader<2>(x, wq, rb, hg, lds, lds0, lane);
    STAMP(1, 6);
  }
  STAMP(1, 3);
  const int r = lane & 31, h = lane >> 5;
  const int pt = w & 1, hp = (w >> 1) & 1, kh = w >> 2;

  lds_wait<LPOLL, 3>(lds, F_QDONE, 4);
  const unsigned qo = LDS_QT + (32 * pt + r) * 192 + 2 * hp * 48;
  const u32x4 qam = ldsr<u32x4>(lds, qo + 16 * h), qbm = ldsr<u32x4>(lds, qo + 48 + 16 * h);
  const unsigned qa16 = ldsr<unsigned>(lds, qo + 32), qb16 = ldsr<unsigned>(lds, qo + 48 + 32);
  auto sq = [](unsigned u) { return bflo(u) * bflo(u) + bfhi(u) * bfhi(u); };
  float na = half_sum(sq(qam[0]) + sq(qam[1]) + sq(qam[2]) + sq(qam[3]) + (h ? 0.f : sq(qa16)));
  float nb = half_sum(sq(qbm[0]) + sq(qbm[1]) + sq(qbm[2]) + sq(qbm[3]) + (h ? 0.f : sq(qb16)));
  PIN(na); PIN(nb);
  KVAddr ad;
  {
    const int vr = r <= 18 ? r : 19;
    const unsigned tb = kh * KV_HALF;
    ad.k = tb + h * 512 + r * 16; ad.kx = tb + 1024 + r * 4; ad.v = tb + 1152 + h * KV_VP + vr * 16;
  }
  PIN(ad.k); PIN(ad.kx); PIN(ad.v);
  f32x16 oa = {}, ob = {};
  PIN(oa); PIN(ob);
  lds_wait<LPOLL, 3>(lds, F_KMOK, 1);
  const float kmax2 = __uint_as_float(flag_get(lds, F_KMAX));
  const float ba = rbf(__builtin_amdgcn_sqrtf(na * kmax2) * 1.01f + 1e-6f), bb = rbf(__builtin_amdgcn_sqrtf(nb * kmax2) * 1.01f + 1e-6f);
  const bool fast = __builtin_amdgcn_ballot_w64(!(ba <= FAST_BOUND && bb <= FAST_BOUND)) == 0;
  lds_wait<LPOLL, 3>(lds, F_SEG0, 4);
  u32x4 kA = ldsr<u32x4>(lds, LDS_K0 + kh * 1152 + h * 512 + r * 16);
  u32x2 kAx = {ldsr<unsigned>(lds, LDS_K0 + kh * 1152 + 1024 + r * 4), 0x3f80u};
  asm volatile("s_waitcnt lgkmcnt(0)" ::: "memory");
  FLAG_ADD(F_QRD);
  __builtin_amdgcn_s_setprio(ATT_PRIO);
  float ma, mb;
  if (fast) {
    ma = 0.f; mb = 0.f;
    u32x2 qax = {h ? 0u : qa16, h ? 0u : (cvtpk(-ba, 0.f))}, qbx = {h ? 0u : qb16, h ? 0u : (cvtpk(-bb, 0.f))};
    u32x4 kB, vA0, vA1, vB0, vB1;
    u32x2 kBx = {0u, 0x3f80u};
    f32x16 zero16 = {}, sa, sb;
    u32x8 pa, pb = {};
    sa = MFMA16(kA, qam, zero16); sa = MFMA8(kAx, qax, sa);
    STAMP(1, 1);
    STAMP(1, 2);
    LDV(vB0, vB1, 0);
    unsigned off = 0;
#pragma unroll 1
    for (int s = 0; s < 8; ++s) {
      STEP(kA, kAx, kB, kBx, vB0, vB1, vA0, vA1, off);
      STEP(kB, kBx, kA, kAx, vA0, vA1, vB0, vB1, off + KV_TILE);
      STEP(kA, kAx, kB, kBx, vB0, vB1, vA0, vA1, off + 2 * KV_TILE);
      const unsigned full = ring_full(lds);
      STEP(kB, kBx, kA, kAx, vA0, vA1, vB0, vB1, off + 3 * KV_TILE);
      flag_set(lds, F_DONE + w, s + 1);
      off = off == 2 * KV_SEG ? 0 : off + KV_SEG;
      if (s < 7 && rfl(full) < s + 2) ring_wait(lds, s + 2);
    }
    ob = MFMA16(vB0, lo4(pb), ob);
    ob = MFMA16(vB1, hi4(pb), ob);
  } else {
    ma = -INFINITY; mb = -INFINITY;
    const u32x2 qax = {h ? 0u : qa16, 0u}, qbx = {h ? 0u : qb16, 0u};
    kAx[1] = 0u;
#pragma unroll 1
    for (int t = 0; t < 32; ++t) {
      if (t && (t & 3) == 0) ring_wait(lds, (t >> 2) + 1);
      const unsigned off = ((t >> 2) % 3) * KV_SEG + (t & 3) * KV_TILE;
      u32x4 kn, v0, v1; u32x2 knx = {0u, 0u};
      LDK(kn, knx, off); LDV(v0, v1, off);
      if ((t & 3) == 3) flag_set(lds, F_DONE + w, (t >> 2) + 1);
      slow_tile(kA, kAx, v0, v1, qam, qax, oa, ma);
      slow_tile(kA, kAx, v0, v1, qbm, qbx, ob, mb);
      kA = kn; kAx = knx;
    }
  }
  STAMP(1, 5);
  tail_part(oa, ob, ma, mb, w, rb, hg, u, dskip, ws, out, lds);
}

#if defined(__HIP_DEVICE_COMPILE__)
#pragma clang attribute pop
#endif
extern "C" void kernel_launch(void* const* d_in, const int* in_sizes, int n_in, void* d_out, int out_size, void* d_ws, size_t ws_size,
                              hipStream_t stream) {
  (void)hipFuncSetAttribute((const void*)fused_kernel, hipFuncAttributeMaxDynamicSharedMemorySize, K_LDS);
  fused_kernel<<<256, 1024, K_LDS, stream>>>((const u16*)d_in[0], (const u16*)d_in[1], (const u16*)d_in[2], (const u16*)d_in[3], (const u16*)d_in[4],
                                            (const u16*)d_in[5], (const u16*)d_in[6], (const u16*)d_in[7], (const u16*)d_in[8], (const u16*)d_in[9],
                                            (const u16*)d_in[10], (const u16*)d_in[11], (const u16*)d_in[12], (const u16*)d_in[13], (char*)d_ws,
                                            (u16*)d_out);
}
```

```cpp
#if defined(__HIP_DEVICE_COMPILE__)
#pragma clang attribute push(__attribute__((target("no-packed-fp32-ops"))), apply_to = function)
#endif
#include <hip/hip_runtime.h>
#include <stdint.h>

typedef __bf16 bf16x2 __attribute__((ext_vector_type(2)));
typedef __bf16 bf16x8 __attribute__((ext_vector_type(8)));
typedef short s16x4 __attribute__((ext_vector_type(4)));
typedef float f32x2 __attribute__((ext_vector_type(2)));
typedef float f32x4 __attribute__((ext_vector_type(4)));
typedef float f32x16 __attribute__((ext_vector_type(16)));
typedef unsigned u32x2 __attribute__((ext_vector_type(2)));
typedef unsigned u32x4 __attribute__((ext_vector_type(4)));
typedef unsigned short u16;
#define DI __device__ __forceinline__
#define BID ((int)__builtin_amdgcn_workgroup_id_x())
#define TID ((int)__builtin_amdgcn_workitem_id_x())
#define LDSP __attribute__((address_space(3)))
#define SBAR() __builtin_amdgcn_sched_barrier(0)
#define PIN(x) asm volatile("" : "+v"(x))
#define MFMA16(a, b, c) __builtin_amdgcn_mfma_f32_32x32x16_bf16(__builtin_bit_cast(bf16x8, a), __builtin_bit_cast(bf16x8, b), c, 0, 0, 0)
#define MFMA8(a, b, c) __builtin_amdgcn_mfma_f32_32x32x8bf16_1k(__builtin_bit_cast(s16x4, a), __builtin_bit_cast(s16x4, b), c, 0, 0, 0)

constexpr int SEQ = 2048, DM = 576, DH = 18, NST = 128;
constexpr int KV_VP = 320;
constexpr int KV_TILE = 1152 + 4 * KV_VP;
constexpr int KV_HALF = 4 * KV_TILE, KV_SEG = 2 * KV_HALF;
constexpr int KV_HDR = 8 * KV_SEG;
constexpr int UT_ROW = 2064;
constexpr size_t WS_EPOCH = 0;
constexpr size_t WS_KVF = 256;
constexpr size_t WS_OF = WS_KVF + 8 * 2048;
constexpr size_t WS_UF = WS_OF + 2048;
constexpr size_t WS_YF = WS_UF + 2048;
constexpr size_t WS_KMAX = WS_YF + 2048;
constexpr size_t WS_KV = WS_KMAX + 1024;
constexpr size_t WS_O = WS_KV + 163840;
constexpr size_t WS_UT = WS_O + (size_t)SEQ * DM * 2;
constexpr size_t WS_YT = WS_UT + (size_t)DM * UT_ROW * 2;

DI unsigned cvtpk(float a, float b) { f32x2 v = {a, b}; return __builtin_bit_cast(unsigned, __builtin_convertvector(v, bf16x2)); }
DI float bflo(unsigned u) { return __uint_as_float(u << 16); }
DI float bfhi(unsigned u) { return __uint_as_float(u & 0xffff0000u); }
DI float bf2f(u16 x) { return __uint_as_float((unsigned)x << 16); }
DI float rbf(float x) { return bflo(cvtpk(x, x)); }
DI int crow(int reg, int h) { return (reg & 3) + 8 * (reg >> 2) + 4 * h; }
DI void glds16s(const void* sbase, unsigned voff, unsigned lds_base) {
  unsigned sv;
  asm volatile("s_mov_b32 %0, m0\n\ts_mov_b32 m0, %3\n\ts_nop 0\n\tglobal_load_lds_dwordx4 %1, %2\n\ts_mov_b32 m0, %0"
               : "=&s"(sv) : "v"(voff), "s"(sbase), "s"(lds_base) : "memory");
}
DI void glds16c(const void* sbase, unsigned voff, unsigned lds_base) {
  unsigned sv;
  asm volatile("s_mov_b32 %0, m0\n\ts_mov_b32 m0, %3\n\ts_nop 0\n\tglobal_load_lds_dwordx4 %1, %2 sc1\n\ts_mov_b32 m0, %0"
               : "=&s"(sv) : "v"(voff), "s"(sbase), "s"(lds_base) : "memory");
}
#define WAITV(n) asm volatile("s_waitcnt vmcnt(" #n ")" ::: "memory")
#define WAITV_BAR(n) asm volatile("s_waitcnt vmcnt(" #n ") lgkmcnt(0)\n\ts_barrier" ::: "memory")
#define SB() asm volatile("s_barrier" ::: "memory")
#define BAR() asm volatile("s_waitcnt lgkmcnt(0)\n\ts_barrier" ::: "memory")
#define STAMP(kern, i)
DI unsigned rfl(unsigned x) { return __builtin_amdgcn_readfirstlane(x); }
template <typename T> DI T ldsr(const char* lds, unsigned off) { return *(const LDSP T*)((const LDSP char*)lds + off); }
template <typename T> DI void ldsw(char* lds, unsigned off, T v) { *(LDSP T*)((LDSP char*)lds + off) = v; }
DI s16x4 ldstr(const char* lds, unsigned off) {
  typedef short v4i16_t __attribute__((ext_vector_type(4)));
  return __builtin_bit_cast(s16x4, __builtin_amdgcn_ds_read_tr16_b64_v4i16((LDSP v4i16_t*)((LDSP char*)lds + off)));
}
DI float half_sum(float v) {
  const auto t = __builtin_amdgcn_permlane32_swap(__float_as_uint(v), __float_as_uint(v), false, false);
  return __uint_as_float(t[0]) + __uint_as_float(t[1]);
}
DI float half_max(float v) {
  const auto t = __builtin_amdgcn_permlane32_swap(__float_as_uint(v), __float_as_uint(v), false, false);
  return fmaxf(__uint_as_float(t[0]), __uint_as_float(t[1]));
}
DI float half_lo(float v) {
  const auto t = __builtin_amdgcn_permlane32_swap(__float_as_uint(v), __float_as_uint(v), false, false);
  return __uint_as_float(t[0]);
}
template <int CTRL> DI float dpp_max(float v) {
  return fmaxf(v, __uint_as_float(__builtin_amdgcn_update_dpp(__float_as_uint(v), __float_as_uint(v), CTRL, 0xf, 0xf, false)));
}
DI float wave_max(float v) {
  v = dpp_max<0xb1>(v);
  v = dpp_max<0x4e>(v);
  v = dpp_max<0x141>(v);
  v = dpp_max<0x140>(v);
  const unsigned u = __float_as_uint(v);
  const float a = __uint_as_float(__builtin_amdgcn_readlane(u, 0)), b = __uint_as_float(__builtin_amdgcn_readlane(u, 16));
  const float c = __uint_as_float(__builtin_amdgcn_readlane(u, 32)), d = __uint_as_float(__builtin_amdgcn_readlane(u, 48));
  return fmaxf(fmaxf(a, b), fmaxf(c, d));
}

DI float xrow16_sum(float x) {
  const auto s = __builtin_amdgcn_permlane16_swap(__float_as_uint(x), __float_as_uint(x), false, false);
  return half_sum(__uint_as_float(s[0]) + __uint_as_float(s[1]));
}
template <int N> DI void waitv() { asm volatile("s_waitcnt vmcnt(%0)" ::"i"(N) : "memory"); }
DI int wswz(int row) { return ((row >> 1) & 1) | (((row >> 3) & 1) << 1); }
constexpr int kRsrc3 = 0x00020000;
typedef __attribute__((address_space(1))) unsigned gu32;
typedef __attribute__((address_space(1))) unsigned long long gu64;
typedef unsigned long long u64;
DI __amdgpu_buffer_rsrc_t rsrc(const void* p, int bytes) { return __builtin_amdgcn_make_buffer_rsrc((void*)p, 0, bytes, kRsrc3); }
constexpr int SC1 = 16;

#define CONV_MAP(sw) (sw)
#define NLD 1
#define RING_POLL 4
#define UT_WAIT F_KVPUB
#define UT_DELAY 16
#define QHOLD 99
#define QRAMP 3
#define GEN_PRIO 2
#define ATT_PRIO 0
#define KV_MODE 1
#define KV_DELAY 32
#define KV_POLL 8
#define LPOLL 1
constexpr int LDS_CV = 3 * KV_SEG;
constexpr int LDS_WKV = 0;
constexpr int LDS_UA = 23040;
constexpr int LDS_QT = 40960;
constexpr int LDS_K0 = 53248;
constexpr int GS = 20736;
constexpr int CV_CH = 31744, CV_K = 4416, CV_U = 17664, CV_UP = 7040;
constexpr int LDS_GEN = LDS_CV + 4 * GS;
constexpr int GEN_AREA = 6656, GEN_PITCH = 272, GEN_P = 21 * GEN_PITCH;
constexpr int LDS_CMB = 0;
constexpr int LDS_OT = 8 * 3328;
constexpr int LDS_FA = LDS_CV + 82944, LDS_FB = LDS_FA + 9216;
constexpr int LDS_SCR = LDS_CV + 3 * CV_CH;
constexpr int LDS_FLAG = LDS_SCR + 4 * 2048;
constexpr int K_LDS = LDS_FLAG + 256;
constexpr float FAST_BOUND = 30.f;
#define CONV_PRIO 0

constexpr int F_FULL = 0  , F_SEG0 = 3, F_TAGOK = 4, F_TAG = 5  , F_WKV = 7, F_DONE = 8  ,
              F_GF = 16  , F_GD = 32  , F_QDONE = 48, F_QRD = 49, F_KVOK = 50, F_UDONE = 51, F_KCOP = 52, F_WO = 53,
              F_KVPUB = 54, F_CFILL = 55, F_KMAX = 41  , F_KMOK = 42, F_CFILL3 = 43, F_KC01 = 44, F_TB = 56  , F_CDONE = 62;
DI void flag_add(char* lds, int word) { __hip_atomic_fetch_add((LDSP unsigned*)((LDSP char*)lds + LDS_FLAG) + word, 1u, __ATOMIC_RELAXED, __HIP_MEMORY_SCOPE_WORKGROUP); }
DI void flag_set(char* lds, int word, unsigned v) {
  asm volatile("" ::: "memory");
  __hip_atomic_store((LDSP unsigned*)((LDSP char*)lds + LDS_FLAG) + word, v, __ATOMIC_RELAXED, __HIP_MEMORY_SCOPE_WORKGROUP);
  asm volatile("" ::: "memory");
}
DI unsigned flag_get(char* lds, int word) { return __hip_atomic_load((LDSP unsigned*)((LDSP char*)lds + LDS_FLAG) + word, __ATOMIC_RELAXED, __HIP_MEMORY_SCOPE_WORKGROUP); }
template <int SLEEP, int PRIO = -1> DI void lds_wait(char* lds, int word, unsigned need) {
  if (rfl(flag_get(lds, word)) < need) {
    if (PRIO >= 0) __builtin_amdgcn_s_setprio(0);
#pragma unroll 1
    for (unsigned spins = 0; spins < (1u << 20); ++spins) {
      __builtin_amdgcn_s_sleep(SLEEP);
      if (rfl(flag_get(lds, word)) >= need) break;
    }
    if (PRIO >= 0) __builtin_amdgcn_s_setprio(PRIO);
  }
  asm volatile("" ::: "memory");
}
template <int SLEEP> DI void done_wait(char* lds, unsigned need, int lane) {
#pragma unroll 1
  for (unsigned spins = 0; spins < (1u << 20); ++spins) {
    if (__builtin_amdgcn_ballot_w64(flag_get(lds, F_DONE + (lane & 7)) < need) == 0) break;
    __builtin_amdgcn_s_sleep(SLEEP);
  }
  asm volatile("" ::: "memory");
}
#define ONE_LANE(stmt) do { asm volatile("" ::: "memory"); if (lane == 0) { stmt; } asm volatile("" ::: "memory"); } while (0)

#define FLAG_ADD(word) ONE_LANE(flag_add(lds, word))
#define TBAR(k) do { asm volatile("s_waitcnt lgkmcnt(0)" ::: "memory"); FLAG_ADD(F_TB + (k)); lds_wait<0>(lds, F_TB + (k), 8); } while (0)
DI u64 get_tag(char* lds) {
  lds_wait<1>(lds, F_TAGOK, 1);
  return (u64)rfl(flag_get(lds, F_TAG)) | ((u64)rfl(flag_get(lds, F_TAG + 1)) << 32);
}
DI void tag_store(char* p, u64 tag) { __hip_atomic_store((gu64*)p, tag, __ATOMIC_RELAXED, __HIP_MEMORY_SCOPE_AGENT); }
template <int N, int SLEEP> DI void tags_wait(const char* p, u64 tag, int lane) {
#pragma unroll 1
  for (unsigned spins = 0; spins < (1u << 20); ++spins) {
    bool ok = true;
#pragma unroll
    for (int j = 0; j < (N + 63) / 64; ++j) {
      const int k = 64 * j + lane;
      ok &= __hip_atomic_load((gu64*)p + (k < N ? k : 0), __ATOMIC_RELAXED, __HIP_MEMORY_SCOPE_AGENT) == tag;
    }
    if (__builtin_amdgcn_ballot_w64(!ok) == 0) break;
    __builtin_amdgcn_s_sleep(SLEEP);
  }
  __builtin_amdgcn_fence(__ATOMIC_ACQUIRE, "wavefront");
  asm volatile("" ::: "memory");
}

template <int SLEEP> DI void tags256_wait(const char* p, u64 tag, int lane) {
  u64 a[4], c[4];
#define TLD(v) do { _Pragma("unroll") for (int j = 0; j < 4; ++j) v[j] = __hip_atomic_load((gu64*)p + 64 * j + lane, __ATOMIC_RELAXED, __HIP_MEMORY_SCOPE_AGENT); } while (0)
#define TOK(v) (__builtin_amdgcn_ballot_w64(!(v[0] == tag && v[1] == tag && v[2] == tag && v[3] == tag)) == 0)
  TLD(a);
#pragma unroll 1
  for (unsigned spins = 0; spins < (1u << 19); ++spins) {
    __builtin_amdgcn_s_sleep(SLEEP);
    TLD(c);
    if (TOK(a)) break;
    __builtin_amdgcn_s_sleep(SLEEP);
    TLD(a);
    if (TOK(c)) break;
  }
#undef TLD
#undef TOK
  __builtin_amdgcn_fence(__ATOMIC_ACQUIRE, "wavefront");
  asm volatile("" ::: "memory");
}

constexpr int NI = 7;
template <int IW> constexpr int q_cnt(int s) { return s > 7 ? 0 : s < 4 ? (18 - IW + NI - 1) / NI : (21 - IW + NI - 1) / NI; }
template <int IW, int S> DI void q_issue(const char* wb, const char* xb, int kvg, const unsigned (&vw)[3], const unsigned (&vx)[3], unsigned lds0, int lane) {
  const unsigned sb = lds0 + LDS_CV + (S & 3) * GS;
#pragma unroll
  for (int j = 0; j < q_cnt<IW>(S); ++j) {
    const int p = IW + NI * j;
    if constexpr (S >= 4) { if (p < 20 || lane < 16) glds16s(wb + (size_t)144 * (S - 4) * (DM * 2), vw[j], sb + 1024 * p); }
    else glds16s(xb + (size_t)16 * ((kvg + S) & 3) * (DM * 2), vx[j], sb + 1024 * p);
  }
}
constexpr int q_last(int S) { return S < 0 ? 1 : S < QRAMP ? S + 2 : (S + 3 > 7 ? 7 : S + 3); }
template <int IW> constexpr int q_pending(int S) { int n = 0; for (int k = S + 1; k <= q_last(S - 1); ++k) n += q_cnt<IW>(k); return n; }
template <int IW, int S, int K> DI void q_issue_upto(const char* wb, const char* xb, int kvg, const unsigned (&vw)[3], const unsigned (&vx)[3], char* lds, unsigned lds0, int lane) {
  if constexpr (K <= q_last(S) && K <= 7) {
    if constexpr (K == QHOLD) lds_wait<LPOLL, 3>(lds, F_KVPUB, 1);
    if constexpr (K >= 4) lds_wait<LPOLL, 3>(lds, F_GD + K - 4, K == 4 ? 2 : 1);
    q_issue<IW, K>(wb, xb, kvg, vw, vx, lds0, lane);
    q_issue_upto<IW, S, K + 1>(wb, xb, kvg, vw, vx, lds, lds0, lane);
  }
}
template <int IW, int S> DI void q_steps(const char* wb, const char* xb, int kvg, const unsigned (&vw)[3], const unsigned (&vx)[3], char* lds, unsigned lds0, int lane) {
  waitv<q_pending<IW>(S)>();
  FLAG_ADD(F_GF + S);
  q_issue_upto<IW, S, q_last(S - 1) + 1>(wb, xb, kvg, vw, vx, lds, lds0, lane);
  if constexpr (S + 1 < 8) q_steps<IW, S + 1>(wb, xb, kvg, vw, vx, lds, lds0, lane);
}
DI unsigned x_piece(int p, int lane) {
  const int n = 64 * p + lane, row = n / 72, c = n - 72 * row;
  return row * (DM * 2) + 16 * ((c & ~7) | ((c ^ (row >> 1)) & 7));
}
template <int IW> DI void q_loader(const u16* x, const u16* wq, int rb, int hg, char* lds, unsigned lds0, int lane) {
  unsigned vw[3], vx[3];
#pragma unroll
  for (int j = 0; j < 3; ++j) vx[j] = x_piece(IW + NI * j < 18 ? IW + NI * j : 0, lane);
  const char* wb = (const char*)(wq + 72 * hg);
  const char* xb = (const char*)(x + (size_t)64 * rb * DM);
  q_issue<IW, 0>(wb, xb, hg & 3, vw, vx, lds0, lane);
  q_issue<IW, 1>(wb, xb, hg & 3, vw, vx, lds0, lane);
#pragma unroll
  for (int j = 0; j < 3; ++j) { const int n = 64 * (IW + NI * j) + lane, row = n / 9, c = n - 9 * row; vw[j] = row * (DM * 2) + 16 * c; }
  q_steps<IW, 0>(wb, xb, hg & 3, vw, vx, lds, lds0, lane);
}
DI void x_bases(int lane, unsigned (&xb)[2]) {
  const int li = lane & 15, g = lane >> 4, xs = (li >> 1) & 7;
  xb[0] = li * (DM * 2) + 16 * (g ^ xs); xb[1] = li * (DM * 2) + 16 * ((4 + g) ^ xs);
}
#define XFRAG(base, ks) ldsr<u32x4>(lds, (base)[(ks) & 1] + 128 * ((ks) >> 1))

constexpr int QD0 = 5;
constexpr int QD1(int i) { return QD0 + 1 + i / 5 > 12 ? 12 : QD0 + 1 + i / 5; }
constexpr int qp_end(int i) { return i < 20 ? 20 : i < 45 ? 45 : i < 65 ? 65 : 90; }
constexpr int qp_ahead(int i) { return i + 1 + (QD0 + i / 5 > 12 ? 12 : QD0 + i / 5) < qp_end(i) ? i + 1 + (QD0 + i / 5 > 12 ? 12 : QD0 + i / 5) : qp_end(i); }
template <int FROM, int TO> DI void q_reads(const char* lds, unsigned wn, unsigned wx, u32x4 (&wa)[90]) {
  if constexpr (FROM < TO) {
    constexpr int ks = FROM / 5, nt = FROM % 5;
    const unsigned b_ = ((2 * ks) % 9 == 8 ? wx : wn) + ((2 * ks) / 9) * GS + ((2 * ks) % 9) * 2304 + 32 * nt;
    wa[FROM] = __builtin_bit_cast(u32x4, __builtin_shufflevector(ldstr(lds, b_), ldstr(lds, b_ + 4 * 144), 0, 1, 2, 3, 4, 5, 6, 7));
    q_reads<FROM + 1, TO>(lds, wn, wx, wa);
  }
}
template <int I, int NXT> DI void q_products(char* lds, unsigned wn, unsigned wx, const u32x4 (&xf)[18], u32x4 (&wa)[90], f32x4 (&acc)[5]) {
  if constexpr (I < 90) {
    constexpr bool first = I == 0 || I == 20 || I == 45 || I == 65;
    constexpr int start = first ? (I + QD1(I) < qp_end(I) ? I + QD1(I) : qp_end(I)) : NXT;
    if constexpr (first) {
      lds_wait<LPOLL, 3>(lds, F_GF + 4 + (2 * (I / 5) + 1) / 9, NI);
      q_reads<I, start>(lds, wn, wx, wa);
      SBAR();
    }
    acc[I % 5] = __builtin_amdgcn_mfma_f32_16x16x32_bf16(__builtin_bit_cast(bf16x8, wa[I]), __builtin_bit_cast(bf16x8, xf[I / 5]), acc[I % 5], 0, 0, 0);
    SBAR();
    constexpr int upto = qp_ahead(I) > start ? qp_ahead(I) : start;
    q_reads<start, upto>(lds, wn, wx, wa);
    SBAR();
    q_products<I + 1, upto>(lds, wn, wx, xf, wa, acc);
  }
}
DI void q_compute(const u16* wk, const u16* wv, int hg, char* lds, int w, int lane, char* ws) {
  {
    typedef unsigned u32x3 __attribute__((ext_vector_type(3)));
    const char* src = (const char*)(hg >> 2 ? wv : wk);
    u32x3 t[7];
#pragma unroll
    for (int j = 0; j < 7; ++j) { const int i = w + 4 * j; __builtin_memcpy(&t[j], src + 12 * (64 * (i < 27 ? i : 0) + lane), 12); }
#pragma unroll
    for (int j = 0; j < 7; ++j) {
      const int i = w + 4 * j, e = 64 * i + lane, k = e / 3, c = e - 3 * k;
      if (i < 27) { const unsigned o = LDS_WKV + 40 * k + 12 * c; ldsw<unsigned>(lds, o, t[j][0]); ldsw<unsigned>(lds, o + 4, t[j][1]); ldsw<unsigned>(lds, o + 8, t[j][2]); }
    }
    asm volatile("s_waitcnt lgkmcnt(0)" ::: "memory");
    FLAG_ADD(F_WKV);
    STAMP(1, 6);
  }
  const int li = lane & 15, g = lane >> 4, q = li >> 2, p = lane & 3, st = (w - hg) & 3;
  u32x4 xf[18];
  {
    unsigned xb[2];
    x_bases(lane, xb);
    xb[0] += LDS_CV + st * GS; xb[1] += LDS_CV + st * GS;
    lds_wait<LPOLL, 3>(lds, F_GF + st, NI);
#pragma unroll
    for (int ks = 0; ks < 18; ++ks) xf[ks] = XFRAG(xb, ks);
    asm volatile("s_waitcnt lgkmcnt(0)" ::: "memory");
    FLAG_ADD(F_GD + st);
  }
  STAMP(1, 7);
  f32x4 acc[5] = {};
  const unsigned wbase = LDS_CV + (8 * (g & 1) + q) * 144 + 8 * p;
  const unsigned wn = wbase + (g >> 1) * 2304;
  const unsigned wx = wbase + (g >> 1) * (GS - 8 * 2304);
  u32x4 wa[90];
  q_products<0, 0>(lds, wn, wx, xf, wa, acc);
#pragma unroll
  for (int nt = 0; nt < 5; ++nt) {
    const unsigned a = cvtpk(acc[nt][0], acc[nt][1]), b2 = cvtpk(acc[nt][2], acc[nt][3]);
#pragma unroll
    for (int e = 0; e < 4; ++e) {
      const int n = 16 * nt + 4 * g + e, hh = n / 18;
      const unsigned v = e < 2 ? a : b2;
      if (nt < 4 || g < 2) ldsw<u16>(lds, LDS_QT + (16 * w + li) * 192 + hh * 48 + 2 * (n - 18 * hh), (u16)((e & 1) ? v >> 16 : v));
    }
  }
  asm volatile("s_waitcnt lgkmcnt(0)" ::: "memory");
  FLAG_ADD(F_QDONE);
}

template <bool ISV> DI void kv_products(const char* lds, int lane, const u32x4 (&xf)[18], f32x4 (&acc)[2]) {
  const int li = lane & 15, g = lane >> 4, q = li >> 2, p = lane & 3;
  f32x4 a0[2] = {}, a1[2] = {};
  const unsigned wo_ = LDS_WKV + (8 * g + q) * 40 + 8 * p;
  s16x4 wr[4][4];
#define WKV_RD(ks) do { _Pragma("unroll") for (int e = 0; e < 4; ++e) wr[(ks) & 3][e] = ldstr(lds, wo_ + 32 * (ks) * 40 + (e >> 1) * 32 + (e & 1) * 160); } while (0)
  WKV_RD(0); WKV_RD(1); WKV_RD(2);
#pragma unroll
  for (int ks = 0; ks < 18; ++ks) {
    if (ks + 3 < 18) WKV_RD(ks + 3);
    SBAR();
#pragma unroll
    for (int nt = 0; nt < 2; ++nt) {
      const auto wfr = __builtin_shufflevector(wr[ks & 3][2 * nt], wr[ks & 3][2 * nt + 1], 0, 1, 2, 3, 4, 5, 6, 7);
      f32x4& a = (ks & 1) ? a1[nt] : a0[nt];
      if (ISV) a = __builtin_amdgcn_mfma_f32_16x16x32_bf16(__builtin_bit_cast(bf16x8, xf[ks]), __builtin_bit_cast(bf16x8, wfr), a, 0, 0, 0);
      else a = __builtin_amdgcn_mfma_f32_16x16x32_bf16(__builtin_bit_cast(bf16x8, wfr), __builtin_bit_cast(bf16x8, xf[ks]), a, 0, 0, 0);
    }
    SBAR();
  }
#undef WKV_RD
#pragma unroll
  for (int e = 0; e < 4; ++e) { acc[0][e] = a0[0][e] + a1[0][e]; acc[1][e] = a0[1][e] + a1[1][e]; }
}
DI void kv_wave(int b, int rb, int hg, const u16* x, char* ws, char* lds, unsigned lds0, int lane) {
  const int li = lane & 15, g = lane >> 4;
  const bool isv = hg >> 2;
  u32x4 xf[18];
  lds_wait<LPOLL, 3>(lds, F_GF + 0, NI);
  unsigned xb[2];
  x_bases(lane, xb);
  xb[0] += LDS_CV; xb[1] += LDS_CV;
#pragma unroll
  for (int ks = 0; ks < 18; ++ks) xf[ks] = XFRAG(xb, ks);
  asm volatile("s_waitcnt lgkmcnt(0)" ::: "memory");
  FLAG_ADD(F_GD + 0);
  lds_wait<LPOLL, 3>(lds, F_WKV, 4);
  STAMP(1, 2);
  f32x4 acc[2];
  if (isv) kv_products<true>(lds, lane, xf, acc); else kv_products<false>(lds, lane, xf, acc);
  STAMP(1, 4);
  const int kt = 2 * rb + ((hg & 3) >> 1), s = hg & 1, kh = kt >> 5, t = kt & 31;
  const __amdgpu_buffer_rsrc_t rs = rsrc(ws + WS_KV, 163840);
  if (!isv) {
    const int img = t ? ((t - 1) >> 2) * KV_SEG + kh * KV_HALF + ((t - 1) & 3) * KV_TILE : KV_HDR + kh * 1152;
    const float sc = 0.23570226039551584f * 1.4426950408889634f;
    const int key = 16 * s + li, e = (key & 0x13) | ((key & 4) << 1) | ((key & 8) >> 1);
    const unsigned d0 = cvtpk(acc[0][0] * sc, acc[0][1] * sc), d1 = cvtpk(acc[0][2] * sc, acc[0][3] * sc);
    const unsigned d2 = g ? 0u : cvtpk(acc[1][0] * sc, acc[1][1] * sc);
    __builtin_amdgcn_raw_buffer_store_b64(u32x2{d0, d1}, rs, img + (g >> 1) * 512 + e * 16 + 8 * (g & 1), 0, SC1);
    if (g == 0) __builtin_amdgcn_raw_buffer_store_b32(d2, rs, img + 1024 + e * 4, 0, SC1);
    float n2 = bflo(d0) * bflo(d0) + bfhi(d0) * bfhi(d0) + bflo(d1) * bflo(d1) + bfhi(d1) * bfhi(d1) + bflo(d2) * bflo(d2) + bfhi(d2) * bfhi(d2);
    n2 = wave_max(xrow16_sum(n2));
    if (lane == 0) __hip_atomic_store((gu32*)(ws + WS_KMAX) + 4 * rb + hg, __float_as_uint(n2), __ATOMIC_RELAXED, __HIP_MEMORY_SCOPE_AGENT);
  } else {
    const int img = (t >> 2) * KV_SEG + kh * KV_HALF + (t & 3) * KV_TILE + 1152 + (2 * s + (g >> 1)) * KV_VP + 8 * (g & 1);
    __builtin_amdgcn_raw_buffer_store_b64(u32x2{cvtpk(acc[0][0], acc[0][1]), cvtpk(acc[0][2], acc[0][3])}, rs, img + li * 16, 0, SC1);
    u32x2 v = {cvtpk(acc[1][0], acc[1][1]), cvtpk(acc[1][2], acc[1][3])};
    if (li == 2) v = u32x2{0x3f803f80u, 0x3f803f80u};
    if (li == 3) v = u32x2{0u, 0u};
    if (li < 4) __builtin_amdgcn_raw_buffer_store_b64(v, rs, img + (16 + li) * 16, 0, SC1);
  }
  const u64 tag = get_tag(lds);
  WAITV(0);
  if (lane < 8) tag_store(ws + WS_KVF + 2048 * lane + 8 * b, tag);
  flag_set(lds, F_KVPUB, 1);
  STAMP(1, 3);
  __builtin_amdgcn_s_sleep(KV_DELAY);
  tags_wait<256, KV_POLL>(ws + WS_KVF + 2048 * (b & 7), tag, lane);
  flag_set(lds, F_KVOK, 1);
  if (b == 0 && lane == 0) tag_store(ws + WS_EPOCH, tag);
  const __amdgpu_buffer_rsrc_t rsM = rsrc(ws + WS_KMAX, 512);
  const float km = wave_max(fmaxf(__uint_as_float(__builtin_amdgcn_raw_buffer_load_b32(rsM, 4 * lane, 0, SC1)),
                                  __uint_as_float(__builtin_amdgcn_raw_buffer_load_b32(rsM, 256 + 4 * lane, 0, SC1))));
  flag_set(lds, F_KMAX, __float_as_uint(km));
  flag_set(lds, F_KMOK, 1);
}

DI void ut_wave(int rb, int hg, const u16* u, char* ws, char* lds, int lane) {
  u32x4 v[9];
#pragma unroll
  for (int j = 0; j < 9; ++j) { const int ch = 64 * j + lane, row = ch / 9, c = ch - 9 * row; v[j] = *(const u32x4*)(u + (size_t)(64 * rb + row) * DM + 72 * hg + 8 * c); }
#pragma unroll
  for (int j = 0; j < 9; ++j) ldsw<u32x4>(lds, LDS_UA + (64 * j + lane) * 16, v[j]);
  const __amdgpu_buffer_rsrc_t rs = rsrc(ws + WS_UT + (size_t)72 * hg * (UT_ROW * 2), 72 * UT_ROW * 2);
  const int g = lane >> 4, li = lane & 15, q = li >> 2, p = lane & 3;
  u32x4 t[10];
#pragma unroll
  for (int i = 0; i < 10; ++i) {
    const unsigned ad = LDS_UA + (8 * (4 * (i & 1) + g) + q) * 144 + 32 * (i >> 1) + 8 * p;
    t[i] = __builtin_bit_cast(u32x4, __builtin_shufflevector(ldstr(lds, ad), ldstr(lds, ad + 4 * 144), 0, 1, 2, 3, 4, 5, 6, 7));
  }
  asm volatile("s_waitcnt lgkmcnt(0)" ::: "memory");
  FLAG_ADD(F_UDONE);
  lds_wait<4>(lds, UT_WAIT, 1);
#pragma unroll
  for (int i = 0; i < 10; ++i)
    if (i < 8 || li < 8) __builtin_amdgcn_raw_buffer_store_b128(t[i], rs, (16 * (i >> 1) + li) * (UT_ROW * 2) + 16 + 128 * rb + 16 * (4 * (i & 1) + g), 0, SC1);
  if (rb == 0 || rb == 31) {
#pragma unroll
    for (int j = 0; j < 2; ++j)
      if (64 * j + lane < 72) __builtin_amdgcn_raw_buffer_store_b128(u32x4{0, 0, 0, 0}, rs, (64 * j + lane) * (UT_ROW * 2) + (rb ? 16 + SEQ * 2 : 0), 0, SC1);
  }
  const u64 tag = get_tag(lds);
  WAITV(0);
  if (lane == 0) tag_store(ws + WS_UF + 8 * (32 * hg + rb), tag);
}

DI f32x2 cmul(f32x2 a, f32x2 b) { return f32x2{__builtin_fmaf(a[0], b[0], -(a[1] * b[1])), __builtin_fmaf(a[0], b[1], a[1] * b[0])}; }
DI f32x2 cexp_l(float zr, float zi, float l) {
  const float mag = __builtin_amdgcn_exp2f((zr * l) * 1.4426950408889634f);
  const float rev = __builtin_amdgcn_fractf((zi * l) * 0.15915494309189535f);
  return f32x2{mag * __builtin_amdgcn_cosf(rev), mag * __builtin_amdgcn_sinf(rev)};
}
DI f32x2 cis2(float e2, float rev) {
  const float mag = __builtin_amdgcn_exp2f(e2), f = __builtin_amdgcn_fractf(rev);
  return f32x2{mag * __builtin_amdgcn_cosf(f), mag * __builtin_amdgcn_sinf(f)};
}
struct GenCol { unsigned y[21]; f32x2 z; };
DI void gen_columns(GenCol& o, float dt, unsigned are, unsigned aim, unsigned bre, unsigned bim, unsigned cre, unsigned cim) {
  const float zr = dt * __uint_as_float(are), zi = dt * __uint_as_float(aim);
  const float dBr = dt * __uint_as_float(bre), dBi = dt * __uint_as_float(bim);
  const float cr = __uint_as_float(cre), ci = __uint_as_float(cim);
  const f32x2 c = {cr * dBr - ci * dBi, cr * dBi + ci * dBr};
  o.z = f32x2{zr * (8.f * 1.4426950408889634f), zi * (-8.f * 0.15915494309189535f)};
  const f32x2 w1 = cexp_l(zr, zi, 1.f), w256 = cexp_l(zr, zi, 256.f);
#define PUTY(col, v) o.y[col] = cvtpk((v)[0], (v)[1])
  f32x2 y[8];
  y[0] = c;
#pragma unroll
  for (int i = 1; i < 8; ++i) y[i] = cmul(y[i - 1], w1);
#pragma unroll
  for (int i = 0; i < 8; ++i) PUTY(i, y[i]);
#pragma unroll
  for (int i = 0; i < 4; ++i) { const f32x2 t = cmul(y[2 * i], w256); PUTY(8 + i, t); }
  const f32x2 p2 = cmul(w256, w256), p3 = cmul(p2, w256), p4 = cmul(p2, p2);
  { f32x2 t = cmul(y[0], p2); PUTY(12, t); t = cmul(y[4], p2); PUTY(13, t); }
  { f32x2 t = cmul(y[0], p3); PUTY(14, t); t = cmul(y[4], p3); PUTY(15, t); }
  { const f32x2 t4 = cmul(y[0], p4); PUTY(16, t4);
    f32x2 t = cmul(t4, w256); PUTY(17, t); t = cmul(t4, p2); PUTY(18, t); t = cmul(t4, p3); PUTY(19, t); t = cmul(t4, p4); PUTY(20, t); }
#undef PUTY
}
DI void gen_pass(const GenCol& col, char* lds, int lane, f32x16& acc) {
  const int r = lane & 31, h = lane >> 5, yr = r <= 20 ? r : r - 16;
  const float fm = (float)r;
#pragma unroll
  for (int k = 0; k < 21; ++k) ldsw<unsigned>(lds, k * GEN_PITCH + 4 * lane, col.y[k]);
  ldsw<f32x2>(lds, GEN_P + 8 * lane, col.z);
  asm volatile("" ::: "memory");
#pragma unroll
  for (int k4 = 0; k4 < 2; ++k4) {
    f32x4 z0[4], z1[4];
    u32x4 fb[4];
#pragma unroll
    for (int j = 0; j < 4; ++j) {
      const int ks = 4 * k4 + j;
      z0[j] = ldsr<f32x4>(lds, GEN_P + 64 * ks + 32 * h); z1[j] = ldsr<f32x4>(lds, GEN_P + 64 * ks + 32 * h + 16);
      fb[j] = ldsr<u32x4>(lds, yr * GEN_PITCH + 32 * ks + 16 * h);
    }
#pragma unroll
    for (int j = 0; j < 4; ++j) {
      const f32x2 a0 = cis2(z0[j][0] * fm, z0[j][1] * fm), a1 = cis2(z0[j][2] * fm, z0[j][3] * fm);
      const f32x2 a2 = cis2(z1[j][0] * fm, z1[j][1] * fm), a3 = cis2(z1[j][2] * fm, z1[j][3] * fm);
      const u32x4 fa = {cvtpk(a0[0], a0[1]), cvtpk(a1[0], a1[1]), cvtpk(a2[0], a2[1]), cvtpk(a3[0], a3[1])};
      acc = MFMA16(fa, fb[j], acc);
    }
  }
  asm volatile("" ::: "memory");
}
DI void gen_wave(int d, const u16* Are, const u16* Aim, const u16* Bre, const u16* Bim, const u16* Cre, const u16* Cim,
                 const u16* logdt, char* lds, int lane) {
  const int r = lane & 31, h = lane >> 5;
  const int idx = d * (NST / 2) + lane;
  const unsigned are = ((const unsigned*)Are)[idx], aim = ((const unsigned*)Aim)[idx], bre = ((const unsigned*)Bre)[idx];
  const unsigned bim = ((const unsigned*)Bim)[idx], cre = ((const unsigned*)Cre)[idx], cim = ((const unsigned*)Cim)[idx];
  const float dt = rbf(expf(bf2f(logdt[d])));
  GenCol c0, c1;
  gen_columns(c0, dt, are << 16, aim << 16, bre << 16, bim << 16, cre << 16, cim << 16);
  gen_columns(c1, dt, are & 0xffff0000u, aim & 0xffff0000u, bre & 0xffff0000u, bim & 0xffff0000u, cre & 0xffff0000u, cim & 0xffff0000u);
  f32x16 acc = {};
  gen_pass(c0, lds, lane, acc);
  gen_pass(c1, lds, lane, acc);
  const int c = r;
  const int hi = c < 8 ? c : c < 12 ? 256 + 2 * (c - 8) : c < 16 ? 512 + 256 * ((c - 12) >> 1) + 4 * ((c - 12) & 1) : 1024 + 256 * (c - 16);
  if (c < 20) {
#pragma unroll
    for (int i = 0; i < 16; ++i) ldsw<u16>(lds, 2 * (8 * crow(i, h) + hi), (u16)cvtpk(acc[i], acc[i]));
  } else if (c == 20 && h == 0) {
    ldsw<u16>(lds, 2 * 2048, (u16)cvtpk(acc[0], acc[0]));
  }
  asm volatile("" ::: "memory");
}
DI void build_copies(const char* kc, char* cl, int lane) {
  auto rd = [&](int l) -> unsigned { return ldsr<u16>(kc, 2 * l); };
  auto pk = [](unsigned lo, unsigned hi) -> unsigned { return lo | (hi << 16); };
  auto put2 = [&](int par, int y, unsigned v0, unsigned v1) {
    ldsw<u32x2>(cl, par * CV_K + 4 * y, u32x2{v0, v1});
    ldsw<unsigned>(cl, (par + 2) * CV_K + 4 * y - 4, v0); ldsw<unsigned>(cl, (par + 2) * CV_K + 4 * y, v1);
  };
  const int L = lane;
  {
    const unsigned e0 = rd(4 * L), e1 = rd(4 * L + 1), e2 = rd(4 * L + 2), e3 = rd(4 * L + 3);
    const unsigned pv = L ? rd(4 * L - 1) : 0u;
    put2(0, 1028 - 2 * L, pk(e3, e2), pk(e1, e0));
    put2(1, 1028 - 2 * L, pk(e2, e1), pk(e0, pv));
  }
  {
    const int j0 = 256 + 4 * L;
    const unsigned a = rd(j0), b = rd(j0 + 2), c = rd(j0 + 4), pv = L ? a : rd(255);
    put2(0, 1028 - j0 / 2, pk(c, b), pk(a, a));
    put2(1, 1028 - j0 / 2, pk(b, a), pk(a, pv));
  }
  {
    const int j0 = 512 + 8 * L, y = 1026 - j0 / 2;
    const unsigned a = rd(j0), b = rd(j0 + 4), c = rd(j0 + 8);
    const unsigned aa = pk(a, a), bb = pk(b, b), cc = pk(c, c);
    put2(0, y, cc, bb);        put2(0, y + 2, pk(b, a), aa);
    put2(1, y, pk(c, b), bb);  put2(1, y + 2, aa, aa);
  }
  {
    const int j0 = 1024 + 16 * L, y = 1022 - j0 / 2;
    const unsigned a = rd(j0), b = rd(j0 + 8), c = rd(j0 + 16);
    const unsigned aa = pk(a, a), bb = pk(b, b), cc = pk(c, c);
    put2(0, y, cc, cc);        put2(0, y + 2, bb, bb);  put2(0, y + 4, bb, pk(b, a));  put2(0, y + 6, aa, aa);
    put2(1, y, cc, pk(c, b));  put2(1, y + 2, bb, bb);  put2(1, y + 4, bb, aa);        put2(1, y + 6, aa, aa);
    if (L == 63) {
      ldsw<unsigned>(cl, CV_K + 4 * 5, pk(0u, c)); ldsw<unsigned>(cl, 3 * CV_K + 4 * 4, pk(0u, c));
    }
  }
  if (L < 6) { ldsw<unsigned>(cl, 4 * L, 0u); if (L) ldsw<unsigned>(cl, 2 * CV_K + 4 * L - 4, 0u); }
  if (L < 5) { ldsw<unsigned>(cl, CV_K + 4 * L, 0u); if (L) ldsw<unsigned>(cl, 3 * CV_K + 4 * L - 4, 0u); }
  if (L < 42) {
    ldsw<unsigned>(cl, 4 * (1030 + L), 0u); ldsw<unsigned>(cl, CV_K + 4 * (1030 + L), 0u);
    ldsw<unsigned>(cl, 2 * CV_K + 4 * (1029 + L), 0u); ldsw<unsigned>(cl, 3 * CV_K + 4 * (1029 + L), 0u);
  }
}
DI void u_fill(int d, const char* ws, char* cl, unsigned cl0, int lane) {
  const char* ur = ws + WS_UT + (size_t)d * (UT_ROW * 2);
#pragma unroll
  for (int part = 0; part < 5; ++part) {
    const int sg = 136 + part * 64 + lane, qc = sg - sg / 9, s0 = 8 * qc - 976;
#pragma unroll
    for (int bb = 0; bb < 2; ++bb)
      if (sg <= 424) glds16c(ur, sg % 9 == 8 ? 0u : 2 * (8 + s0 + 4 * bb), cl0 + CV_U + bb * CV_UP + (136 + part * 64) * 16);
  }
#pragma unroll
  for (int bb = 0; bb < 2; ++bb)
#pragma unroll
    for (int j = 0; j < 3; ++j)
      if (64 * j + lane < 136) ldsw<u32x4>(cl, CV_U + bb * CV_UP + (64 * j + lane) * 16, u32x4{0, 0, 0, 0});
}

struct KVAddr { unsigned k, kx, v; };
#define LDK(dst, dstx, t_off) do { dst = ldsr<u32x4>(lds, ad.k + (t_off)); dstx[0] = ldsr<unsigned>(lds, ad.kx + (t_off)); } while (0)
#define LDV(d0, d1, t_off) do { d0 = ldsr<u32x4>(lds, ad.v + (t_off)); d1 = ldsr<u32x4>(lds, ad.v + 2 * KV_VP + (t_off)); } while (0)
#define EX2(x) __builtin_amdgcn_exp2f(x)
#define GRP(MF, S, P, i) do { MF; SBAR(); \
    { const float e0 = EX2(S[4 * (i)]), e1 = EX2(S[4 * (i) + 1]), e2 = EX2(S[4 * (i) + 2]), e3 = EX2(S[4 * (i) + 3]); \
      unsigned t0_ = cvtpk(e0, e1), t1_ = cvtpk(e2, e3); PIN(t0_); PIN(t1_); P[2 * (i)] = t0_; P[2 * (i) + 1] = t1_; } \
    SBAR(); } while (0)
typedef unsigned u32x8 __attribute__((ext_vector_type(8)));
DI u32x4 lo4(const u32x8& p) { return __builtin_shufflevector(p, p, 0, 1, 2, 3); }
DI u32x4 hi4(const u32x8& p) { return __builtin_shufflevector(p, p, 4, 5, 6, 7); }

#define STEP(Kc, Kcx, Kn, Knx, Vp0, Vp1, Vc0, Vc1, off) do { \
    LDK(Kn, Knx, (off)); LDV(Vc0, Vc1, (off)); SBAR(); \
    GRP(sb = MFMA16(Kc, qbm, zero16), sa, pa, 0); \
    GRP(ob = MFMA16(Vp0, lo4(pb), ob), sa, pa, 1); \
    GRP(sb = MFMA8(Kcx, qbx, sb), sa, pa, 2); \
    GRP(ob = MFMA16(Vp1, hi4(pb), ob), sa, pa, 3); \
    GRP(sa = MFMA16(Kn, qam, zero16), sb, pb, 0); \
    GRP(oa = MFMA16(Vc0, lo4(pa), oa), sb, pb, 1); \
    GRP(sa = MFMA8(Knx, qax, sa), sb, pb, 2); \
    GRP(oa = MFMA16(Vc1, hi4(pa), oa), sb, pb, 3); \
  } while (0)

DI void slow_tile(const u32x4& kf, const u32x2& kx, const u32x4& v0, const u32x4& v1, const u32x4& qm, const u32x2& qx, f32x16& o, float& m) {
  f32x16 s = {};
  s = MFMA16(kf, qm, s);
  s = MFMA8(kx, qx, s);
  float mx = s[0];
#pragma unroll
  for (int i = 1; i < 16; ++i) mx = fmaxf(mx, s[i]);
  mx = half_max(mx);
  const float mn = fmaxf(m, mx);
  const float alpha = EX2(m - mn);
  m = mn;
#pragma unroll
  for (int i = 0; i < 16; ++i) o[i] *= alpha;
  u32x8 p;
#pragma unroll
  for (int i = 0; i < 8; ++i) p[i] = cvtpk(EX2(s[2 * i] - mn), EX2(s[2 * i + 1] - mn));
  o = MFMA16(v0, lo4(p), o);
  o = MFMA16(v1, hi4(p), o);
}

template <bool TWO, bool SKIP0> DI void conv_steps(const char* lds, unsigned pa0, unsigned pb0, unsigned pa1, unsigned pb1, f32x16& c0, f32x16& c1) {
  __builtin_amdgcn_s_setprio(CONV_PRIO);
  u32x4 a0[2], b0[2], a1[2], b1[2];
#define CLD(i, n) do { a0[i] = ldsr<u32x4>(lds, pa0 + 32 * (n)); b0[i] = ldsr<u32x4>(lds, pb0 + 32 * (n) + 16 * ((n) >> 2)); \
    if (TWO) { a1[i] = ldsr<u32x4>(lds, pa1 + 32 * (n)); b1[i] = ldsr<u32x4>(lds, pb1 + 32 * (n) + 16 * ((n) >> 2)); } } while (0)
#define CMM(i) do { c0 = MFMA16(a0[i], b0[i], c0); if (TWO) c1 = MFMA16(a1[i], b1[i], c1); } while (0)
  CLD(0, 0); CLD(1, 1);
  c0 = MFMA16(a0[0], b0[0], c0);
  if (TWO && !SKIP0) c1 = MFMA16(a1[0], b1[0], c1);
  SBAR();
  typedef const LDSP char* lptr;
  lptr qa0 = (lptr)lds + pa0, qb0 = (lptr)lds + pb0, qa1 = (lptr)lds + pa1, qb1 = (lptr)lds + pb1;
  PIN(qa0); PIN(qb0);
  if (TWO) { PIN(qa1); PIN(qb1); }
#define LRD(p, o) (*(const LDSP u32x4*)((p) + (o)))
#define CLD4(i, k) do { a0[i] = LRD(qa0, 32 * (k)); b0[i] = LRD(qb0, 32 * (k) + 16 * ((k) >> 2)); \
    if (TWO) { a1[i] = LRD(qa1, 32 * (k)); b1[i] = LRD(qb1, 32 * (k) + 16 * ((k) >> 2)); } } while (0)
#pragma unroll
  for (int n = 1; n < 65; n += 2) {
    CLD4(0, n + 1); SBAR(); CMM(1); SBAR();
    if (n + 2 < 65) CLD4(1, n + 2);
    SBAR(); CMM(0); SBAR();
  }
#undef CLD4
#undef LRD
#undef CLD
#undef CMM
  __builtin_amdgcn_s_setprio(3);
}
DI void conv_out(const f32x16& c, int d, int tl, char* ws, char* scratch, int lane) {
  const int r = lane & 31, h = lane >> 5;
#pragma unroll
  for (int g = 0; g < 4; ++g)
    ldsw<u32x2>(scratch, 2 * (16 * g + 8 * h + 4 * (r & 1) + 64 * (r >> 1)), u32x2{cvtpk(c[4 * g], c[4 * g + 1]), cvtpk(c[4 * g + 2], c[4 * g + 3])});
  const __amdgpu_buffer_rsrc_t rsY = __builtin_amdgcn_make_buffer_rsrc((void*)(ws + WS_YT + (size_t)d * (SEQ * 2) + 2048 * tl), 0, 2048, kRsrc3);
#pragma unroll
  for (int j = 0; j < 2; ++j) __builtin_amdgcn_raw_buffer_store_b128(ldsr<u32x4>(scratch, 1024 * j + 16 * lane), rsY, 1024 * j + 16 * lane, 0, 16);
}

struct SkipRegs { u32x4 y[3], u[3], d[3]; };
DI void skip_load(SkipRegs& s, int rb, int hg, int tq, const u16* u, const u16* dskip, char* ws, char* lds, int lane) {
  tags_wait<32, 2>(ws + WS_YF + 8 * 32 * hg, get_tag(lds), lane);
  const __amdgpu_buffer_rsrc_t rsY = rsrc(ws + WS_YT + (size_t)72 * hg * (SEQ * 2), 72 * SEQ * 2);
#pragma unroll
  for (int j = 0; j < 3; ++j) {
    const int ch = 64 * j + lane < 144 ? 64 * j + lane : 0, row = ch / 9, c = ch - 9 * row;
    s.y[j] = __builtin_amdgcn_raw_buffer_load_b128(rsY, (ch >> 1) * (SEQ * 2) + 128 * rb + 32 * tq + 16 * (ch & 1), 0, SC1);
    s.u[j] = *(const u32x4*)(u + (size_t)(64 * rb + 16 * tq + row) * DM + 72 * hg + 8 * c);
    s.d[j] = *(const u32x4*)(dskip + 72 * hg + 8 * c);
  }
}
DI void skip_finish(const SkipRegs& s, int rb, int hg, int tq, u16* out, char* lds, int lane) {
  char* ia = lds + LDS_FA + tq * 4608;
  char* ib = ia + 2304;
#pragma unroll
  for (int j = 0; j < 3; ++j)
    if (64 * j + lane < 144) ldsw<u32x4>(ia, (64 * j + lane) * 16, s.y[j]);
  const int g = lane >> 4, li = lane & 15, q = li >> 2, p = lane & 3;
#pragma unroll
  for (int j = 0; j < 3; ++j) {
    const int oct = 4 * j + g < 9 ? 4 * j + g : 8;
    const unsigned ad = (8 * oct + q) * 32 + 8 * p;
    const u32x4 t = __builtin_bit_cast(u32x4, __builtin_shufflevector(ldstr(ia, ad), ldstr(ia, ad + 4 * 32), 0, 1, 2, 3, 4, 5, 6, 7));
    if (4 * j + g < 9) ldsw<u32x4>(ib, li * 144 + 16 * oct, t);
  }
#pragma unroll
  for (int j = 0; j < 3; ++j) {
    const int ch = 64 * j + lane < 144 ? 64 * j + lane : 0, row = ch / 9, c = ch - 9 * row;
    const u32x4 yy = ldsr<u32x4>(ib, ch * 16), dd = s.d[j];
    u32x4 o;
#pragma unroll
    for (int k = 0; k < 4; ++k) {
      const unsigned sk = cvtpk(bflo(s.u[j][k]) * bflo(dd[k]), bfhi(s.u[j][k]) * bfhi(dd[k]));
      o[k] = cvtpk(bflo(yy[k]) + bflo(sk), bfhi(yy[k]) + bfhi(sk));
    }
    if (64 * j + lane < 144) *(u32x4*)(out + (size_t)(64 * rb + 16 * tq + row) * (2 * DM) + DM + 72 * hg + 8 * c) = o;
  }
}

DI void tail_part(const f32x16& oa, const f32x16& ob, float ma, float mb, int w, int rb, int hg, const u16* u, const u16* dskip, char* ws, u16* out, char* lds) {
  int lane;
  asm volatile("v_mbcnt_lo_u32_b32 %0, -1, 0\n\tv_mbcnt_hi_u32_b32 %0, -1, %0" : "=v"(lane));
  const int r = lane & 31, h = lane >> 5, pt = w & 1, hp = (w >> 1) & 1, kh = w >> 2;
  SkipRegs sk;
  if (w < 4) skip_load(sk, rb, hg, w, u, dskip, ws, lds, lane);
  BAR();

  {
    const f32x16& give = kh ? oa : ob;
    const float gm = kh ? ma : mb;
    char* own = lds + LDS_CMB + w * 3328;
#pragma unroll
    for (int g = 0; g < 3; ++g) ldsw<f32x4>(own, g * 1024 + lane * 16, f32x4{give[4 * g], give[4 * g + 1], give[4 * g + 2], give[4 * g + 3]});
    ldsw<float>(own, 3072 + lane * 4, gm);
    BAR();
    const char* oth = lds + LDS_CMB + (w ^ 4) * 3328;
    f32x16 keep = kh ? ob : oa;
    const float m1 = kh ? mb : ma, m2 = ldsr<float>(oth, 3072 + lane * 4);
    const float mm = fmaxf(m1, m2), f1 = EX2(m1 - mm), f2 = EX2(m2 - mm);
    float o[12];
#pragma unroll
    for (int g = 0; g < 3; ++g) {
      const f32x4 t = ldsr<f32x4>(oth, g * 1024 + lane * 16);
#pragma unroll
      for (int e = 0; e < 4; ++e) o[4 * g + e] = keep[4 * g + e] * f1 + t[e] * f2;
    }
    const float inv = 1.0f / half_lo(o[10]);
    const unsigned ot = LDS_OT + (32 * pt + r) * 144 + (2 * hp + kh) * 36;
    ldsw<unsigned>(lds, ot + 8 * h, cvtpk(o[0] * inv, o[1] * inv));
    ldsw<unsigned>(lds, ot + 8 * h + 4, cvtpk(o[2] * inv, o[3] * inv));
    ldsw<unsigned>(lds, ot + 16 + 8 * h, cvtpk(o[4] * inv, o[5] * inv));
    ldsw<unsigned>(lds, ot + 16 + 8 * h + 4, cvtpk(o[6] * inv, o[7] * inv));
    if (!h) ldsw<unsigned>(lds, ot + 32, cvtpk(o[8] * inv, o[9] * inv));
  }
  BAR();
  const __amdgpu_buffer_rsrc_t rsO = rsrc(ws + WS_O + (size_t)64 * rb * (DM * 2), 64 * DM * 2);
#pragma unroll
  for (int n = 0; n < 2; ++n) {
    if (n == 0 || w == 0) {
      const int pair = 4 * (n ? 8 : w) + (lane >> 4), pt4 = pair / 9, c = pair - 9 * pt4, li = lane & 15, c8 = 9 * hg + c;
      __builtin_amdgcn_raw_buffer_store_b128(ldsr<u32x4>(lds, LDS_OT + (16 * pt4 + li) * 144 + 16 * c), rsO,
                                             ((pt4 * 18 + (c8 >> 2)) * 64 + (c8 & 3) * 16 + li) * 16, 0, SC1);
    }
  }
  asm volatile("s_waitcnt vmcnt(0)" ::: "memory");
  BAR();
  STAMP(1, 9);
  if (w < 4) skip_finish(sk, rb, hg, w, out, lds, lane);
  if (w == 4) {
    const u64 tag = get_tag(lds);
    if (lane == 0) tag_store(ws + WS_OF + 8 * (8 * rb + hg), tag);
    tags_wait<8, 1>(ws + WS_OF + 64 * rb, tag, lane);
  }
  BAR();
  STAMP(1, 10);
  const int li = lane & 15, g = lane >> 4, q = li >> 2, p = lane & 3, pt4 = w & 3, k2 = w >> 2;
  u32x4 xf[9];
#pragma unroll
  for (int ks = 0; ks < 9; ++ks) xf[ks] = __builtin_amdgcn_raw_buffer_load_b128(rsO, ((pt4 * 18 + 9 * k2 + ks) * 64 + lane) * 16, 0, SC1);
  STAMP(1, 11);
  f32x4 acc[5] = {};
  const unsigned wb = LDS_CV + (32 * 9 * k2 + 8 * g + q) * 144 + 8 * p;
  s16x4 wa[2][10];
#define WRD(buf, ks) do { _Pragma("unroll") for (int nt = 0; nt < 5; ++nt) { \
    wa[buf][2 * nt] = ldstr(lds, wb + 32 * (ks) * 144 + 32 * nt); wa[buf][2 * nt + 1] = ldstr(lds, wb + (32 * (ks) + 4) * 144 + 32 * nt); } } while (0)
  WRD(0, 0);
#pragma unroll
  for (int ks = 0; ks < 9; ++ks) {
    if (ks + 1 < 9) WRD((ks + 1) & 1, ks + 1);
    SBAR();
#pragma unroll
    for (int nt = 0; nt < 5; ++nt) {
      const auto a = __builtin_shufflevector(wa[ks & 1][2 * nt], wa[ks & 1][2 * nt + 1], 0, 1, 2, 3, 4, 5, 6, 7);
      acc[nt] = __builtin_amdgcn_mfma_f32_16x16x32_bf16(__builtin_bit_cast(bf16x8, a), __builtin_bit_cast(bf16x8, xf[ks]), acc[nt], 0, 0, 0);
    }
    SBAR();
  }
#undef WRD
  STAMP(1, 12);
  if (k2) {
#pragma unroll
    for (int nt = 0; nt < 5; ++nt) ldsw<f32x4>(lds, LDS_CMB + (pt4 * 5 + nt) * 1024 + lane * 16, acc[nt]);
  }
  BAR();
  if (k2) return;
  u16* orow = out + (size_t)(64 * rb + 16 * pt4 + li) * (2 * DM) + 72 * hg + 4 * g;
#pragma unroll
  for (int nt = 0; nt < 5; ++nt) {
    const f32x4 t = ldsr<f32x4>(lds, LDS_CMB + (pt4 * 5 + nt) * 1024 + lane * 16);
    if (nt < 4 || lane < 32)
      *(u32x2*)(orow + 16 * nt) = u32x2{cvtpk(acc[nt][0] + t[0], acc[nt][1] + t[1]), cvtpk(acc[nt][2] + t[2], acc[nt][3] + t[3])};
  }
  asm volatile("fk_pf_end:");
  STAMP(1, 15);
}

DI void seg0_share(int i, const char* ws, char* lds, unsigned lds0, int lane) {
  const char* kvg = ws + WS_KV;
  lds_wait<LPOLL, 3>(lds, F_KVOK, 1);
#pragma unroll
  for (int j = 0; j < 6; ++j) {
    const int p = i + 4 * j;
    if (p < 19) glds16c(kvg + 1024 * p, 16 * lane, lds0 + 1024 * p);
    else if (p < 22) { if (p < 21 || lane < 16) glds16c(kvg + KV_HDR + 1024 * (p - 19), 16 * lane, lds0 + LDS_K0 + 1024 * (p - 19)); }
  }
  WAITV(0);
  FLAG_ADD(F_SEG0);
}
template <int K> DI void ring_loader(const char* ws, char* lds, unsigned lds0, int lane) {
  constexpr int NP = (19 - K + NLD - 1) / NLD;
  const char* kvg = ws + WS_KV;
  auto issue = [&](int s) {
#pragma unroll
    for (int j = 0; j < NP; ++j) glds16c(kvg + s * KV_SEG + 1024 * (K + NLD * j), 16 * lane, lds0 + (s % 3) * KV_SEG + 1024 * (K + NLD * j));
  };
  __builtin_amdgcn_s_setprio(3);
  lds_wait<1>(lds, F_UDONE, 1);
  issue(1);
  lds_wait<1>(lds, F_QRD, 8);
  issue(2);
  waitv<NP>();
  flag_set(lds, F_FULL + K, 2);
  WAITV(0);
  flag_set(lds, F_FULL + K, 3);
#pragma unroll 1
  for (int s = 3; s < 8; ++s) {
    done_wait<RING_POLL>(lds, s - 2, lane);
    issue(s);
    WAITV(0);
    flag_set(lds, F_FULL + K, s + 1);
  }
}
DI unsigned ring_full(char* lds) {
  if (NLD == 1) return flag_get(lds, F_FULL);
  const u32x4 v = *(const volatile LDSP u32x4*)((LDSP char*)lds + LDS_FLAG + 4 * F_FULL);
  if (NLD == 1) return v[0];
  if (NLD == 2) return v[0] < v[1] ? v[0] : v[1];
  return v[0] < v[1] ? (v[0] < v[2] ? v[0] : v[2]) : (v[1] < v[2] ? v[1] : v[2]);
}
DI void ring_wait(char* lds, unsigned need) {
#pragma unroll 1
  for (unsigned spins = 0; spins < (1u << 20); ++spins) {
    if (rfl(ring_full(lds)) >= need) break;
    __builtin_amdgcn_s_sleep(1);
  }
  asm volatile("" ::: "memory");
}
DI void l2_touch(const char* base, int line0, int nlines, unsigned lds0, int lane) {
  unsigned sv;
  if (line0 + lane < nlines)
    asm volatile("s_mov_b32 %0, m0\n\ts_mov_b32 m0, %3\n\ts_nop 0\n\tglobal_load_lds_dword %1, %2\n\ts_mov_b32 m0, %0"
                 : "=&s"(sv) : "v"(128 * (line0 + lane)), "s"(base), "s"(lds0 + LDS_GEN + 3 * GEN_AREA) : "memory");
}

__global__ void __launch_bounds__(1024)
fused_kernel(const u16* x, const u16* wq, const u16* wk, const u16* wv, const u16* wo, const u16* u, const u16* Are, const u16* Aim,
             const u16* Bre, const u16* Bim, const u16* Cre, const u16* Cim, const u16* logdt, const u16* dskip, char* ws, u16* out) {
  extern __shared__ __attribute__((aligned(16))) char lds[];
  const unsigned lds0 = (unsigned)(uintptr_t)lds;
  const char* pc0;
  asm volatile("s_getpc_b64 %0" : "=s"(pc0));
  const int b = BID, lane = TID & 63, w = rfl(TID >> 6);
  const int rb = (b & 7) + 8 * (b >> 6), hg = (b >> 3) & 7;
  STAMP(1, 0);
  if (TID < 64) ((LDSP unsigned*)((LDSP char*)lds + LDS_FLAG))[TID] = 0u;
  BAR();
  const char* kvg = ws + WS_KV;
  if (w >= 14) {
    __builtin_amdgcn_s_setprio(2);
    if (w == 15) {
      const char* pc1;
      int dend;
      asm volatile("s_getpc_b64 %0\nfk_pf_here:\n\ts_mov_b32 %1, fk_pf_end-fk_pf_here" : "=s"(pc1), "=s"(dend));
      const int len = (int)(pc1 - pc0) + (dend > 0 ? dend : 0);
      l2_touch(pc0, (b >> 3) + 31 * lane, len >> 7, lds0, lane);
    }
    if (w == 14) {
      q_loader<4>(x, wq, rb, hg, lds, lds0, lane);
      seg0_share(2, ws, lds, lds0, lane);
      if (NLD > 1) ring_loader<1>(ws, lds, lds0, lane);
      return;
    }
    q_loader<5>(x, wq, rb, hg, lds, lds0, lane);
    seg0_share(3, ws, lds, lds0, lane);
    if (NLD > 2) ring_loader<2>(ws, lds, lds0, lane);
    return;
  }
  if (w == 13) {
    __builtin_amdgcn_s_setprio(2);
    __builtin_amdgcn_s_sleep(UT_DELAY);
    ut_wave(rb, hg, u, ws, lds, lane);
    seg0_share(1, ws, lds, lds0, lane);
    STAMP(1, 15);
    return;
  }
  if (w == 12) {
    __builtin_amdgcn_s_setprio(3);
    const u64 tag = __hip_atomic_load((gu64*)(ws + WS_EPOCH), __ATOMIC_RELAXED, __HIP_MEMORY_SCOPE_AGENT) + 1;
    flag_set(lds, F_TAG, (unsigned)tag); flag_set(lds, F_TAG + 1, (unsigned)(tag >> 32)); flag_set(lds, F_TAGOK, 1);
    q_loader<3>(x, wq, rb, hg, lds, lds0, lane);
    STAMP(1, 3);
    seg0_share(0, ws, lds, lds0, lane);
    STAMP(1, 4);
    ring_loader<0>(ws, lds, lds0, lane);
    STAMP(1, 15);
    return;
  }
  if (w >= 8) {
    const int sw = w - 8, i = b >> 3, xc = b & 7;
    const int d0 = 72 * xc + 2 * i, dx = 72 * xc + 64 + (i >> 2), third = (i & 3) < 2;
    char* area = lds + LDS_GEN + (sw - 1) * GEN_AREA;
    if (sw == 0) {
      __builtin_amdgcn_s_setprio(3);
      kv_wave(b, rb, hg, x, ws, lds, lds0, lane);
      STAMP(1, 5);
      tags_wait<32, 4>(ws + WS_UF + 8 * 32 * xc, get_tag(lds), lane);
      lds_wait<2, 3>(lds, F_QDONE, 4);
      u_fill(d0, ws, lds + LDS_CV, lds0 + LDS_CV, lane);
      u_fill(d0 + 1, ws, lds + LDS_CV + CV_CH, lds0 + LDS_CV + CV_CH, lane);
      asm volatile("s_waitcnt vmcnt(0) lgkmcnt(0)" ::: "memory");
      FLAG_ADD(F_CFILL);
      if (third) {
        lds_wait<2, 3>(lds, F_KCOP, 3);
        u_fill(dx, ws, lds + LDS_CV + 2 * CV_CH, lds0 + LDS_CV + 2 * CV_CH, lane);
        asm volatile("s_waitcnt vmcnt(0) lgkmcnt(0)" ::: "memory");
        FLAG_ADD(F_CFILL3);
      }
    } else if (sw < 3 || third) {
      __builtin_amdgcn_s_setprio(GEN_PRIO);
      gen_wave(sw < 3 ? d0 + sw - 1 : dx, Are, Aim, Bre, Bim, Cre, Cim, logdt, area, lane);
      STAMP(1, 3);
    }
    __builtin_amdgcn_s_setprio(3);
    if (sw != 0) {
      if (sw < 3 || third) {
        lds_wait<2, 3>(lds, F_QDONE, 4);
        build_copies(area, lds + LDS_CV + (sw - 1) * CV_CH, lane);
      }
      asm volatile("s_waitcnt lgkmcnt(0)" ::: "memory");
      FLAG_ADD(F_KCOP);
      if (sw < 3) FLAG_ADD(F_KC01);
    }
    STAMP(1, 6);
    {
      const int r = lane & 31, h = lane >> 5;
      const unsigned kb = LDS_CV + (3 - (r & 3)) * CV_K + 2 * (8 * h - 8 * (r >> 2));
      const unsigned ub = LDS_CV + CV_U + (r & 1) * CV_UP + 144 * (r >> 1) + 16 * h;
      const unsigned A1 = kb + 2 * 1080, A2 = kb + 2 * 56, B1 = ub, B3 = ub + 2304;
      char* scr = lds + LDS_SCR + sw * 2048;
      f32x16 c0 = {}, c1 = {};
      if (sw < 3) { lds_wait<2>(lds, F_KC01, 2); lds_wait<2>(lds, F_CFILL, 1); }
      else if (third) lds_wait<2>(lds, F_CFILL3, 1);
      const int cu = CONV_MAP(sw);
      if (cu == 2) {
        conv_steps<true, false>(lds, A1, B1, A1 + CV_CH, B1 + CV_CH, c0, c1);
        conv_out(c0, d0, 0, ws, scr, lane);
        conv_out(c1, d0 + 1, 0, ws, scr, lane);
      } else if (cu < 2 || (i & 3) == 0) {
        const unsigned so = (cu < 2 ? cu : 2) * CV_CH;
        conv_steps<true, true>(lds, A2 + so, B1 + so, A1 + so, B3 + so, c0, c1);
#pragma unroll
        for (int k = 0; k < 16; ++k) c0[k] += c1[k];
        conv_out(c0, cu < 2 ? d0 + cu : dx, 1, ws, scr, lane);
      } else if (third) {
        conv_steps<false, false>(lds, A1 + 2 * CV_CH, B1 + 2 * CV_CH, 0, 0, c0, c1);
        conv_out(c0, dx, 0, ws, scr, lane);
      }
      STAMP(1, 7);
    }
    asm volatile("s_waitcnt vmcnt(0) lgkmcnt(0)" ::: "memory");
    unsigned arrived = 0;
    if (lane == 0) arrived = __hip_atomic_fetch_add((LDSP unsigned*)((LDSP char*)lds + LDS_FLAG) + F_CDONE, 1u, __ATOMIC_RELAXED, __HIP_MEMORY_SCOPE_WORKGROUP);
    if (rfl(arrived) == 3) { const u64 tag = get_tag(lds); if (lane == 0) tag_store(ws + WS_YF + 8 * (32 * xc + i), tag); }
    lds_wait<4>(lds, F_CDONE, 4);
    {
      unsigned vo[3];
#pragma unroll
      for (int k = 0; k < 3; ++k) { const int ch = 64 * (sw + 4 * k) + lane, row = ch / 9, c = ch - 9 * row; vo[k] = row * (DM * 2) + c * 16; }
      const char* wb = (const char*)(wo + 72 * hg);
#pragma unroll 1
      for (int a = 0; a < 9; ++a) {
#pragma unroll
        for (int k = 0; k < 3; ++k)
          if (k < 2 || sw == 0) glds16s(wb + (size_t)a * 64 * (DM * 2), vo[k], lds0 + LDS_CV + 1024 * (9 * a + sw + 4 * k));
      }
      STAMP(1, 8);
      WAITV(0);
    }
    STAMP(1, 15);
    return;
  }
  __builtin_amdgcn_s_setprio(3);
  if (w < 4) {
    q_compute(wk, wv, hg, lds, w, lane, ws);
  } else if (w == 4) {
    q_loader<6>(x, wq, rb, hg, lds, lds0, lane);
  } else {
    if (w == 5) q_loader<0>(x, wq, rb, hg, lds, lds0, lane);
    else if (w == 6) q_loader<1>(x, wq, rb, hg, lds, lds0, lane);
    else q_loader<2>(x, wq, rb, hg, lds, lds0, lane);
    STAMP(1, 6);
  }
  STAMP(1, 3);
  const int r = lane & 31, h = lane >> 5;
  const int pt = w & 1, hp = (w >> 1) & 1, kh = w >> 2;

  lds_wait<LPOLL, 3>(lds, F_QDONE, 4);
  const unsigned qo = LDS_QT + (32 * pt + r) * 192 + 2 * hp * 48;
  const u32x4 qam = ldsr<u32x4>(lds, qo + 16 * h), qbm = ldsr<u32x4>(lds, qo + 48 + 16 * h);
  const unsigned qa16 = ldsr<unsigned>(lds, qo + 32), qb16 = ldsr<unsigned>(lds, qo + 48 + 32);
  auto sq = [](unsigned u) { return bflo(u) * bflo(u) + bfhi(u) * bfhi(u); };
  float na = half_sum(sq(qam[0]) + sq(qam[1]) + sq(qam[2]) + sq(qam[3]) + (h ? 0.f : sq(qa16)));
  float nb = half_sum(sq(qbm[0]) + sq(qbm[1]) + sq(qbm[2]) + sq(qbm[3]) + (h ? 0.f : sq(qb16)));
  PIN(na); PIN(nb);
  KVAddr ad;
  {
    const int vr = r <= 18 ? r : 19;
    const unsigned tb = kh * KV_HALF;
    ad.k = tb + h * 512 + r * 16; ad.kx = tb + 1024 + r * 4; ad.v = tb + 1152 + h * KV_VP + vr * 16;
  }
  PIN(ad.k); PIN(ad.kx); PIN(ad.v);
  f32x16 oa = {}, ob = {};
  PIN(oa); PIN(ob);
  lds_wait<LPOLL, 3>(lds, F_KMOK, 1);
  const float kmax2 = __uint_as_float(flag_get(lds, F_KMAX));
  const float ba = rbf(__builtin_amdgcn_sqrtf(na * kmax2) * 1.01f + 1e-6f), bb = rbf(__builtin_amdgcn_sqrtf(nb * kmax2) * 1.01f + 1e-6f);
  const bool fast = __builtin_amdgcn_ballot_w64(!(ba <= FAST_BOUND && bb <= FAST_BOUND)) == 0;
  lds_wait<LPOLL, 3>(lds, F_SEG0, 4);
  u32x4 kA = ldsr<u32x4>(lds, LDS_K0 + kh * 1152 + h * 512 + r * 16);
  u32x2 kAx = {ldsr<unsigned>(lds, LDS_K0 + kh * 1152 + 1024 + r * 4), 0x3f80u};
  asm volatile("s_waitcnt lgkmcnt(0)" ::: "memory");
  FLAG_ADD(F_QRD);
  __builtin_amdgcn_s_setprio(ATT_PRIO);
  float ma, mb;
  if (fast) {
    ma = 0.f; mb = 0.f;
    u32x2 qax = {h ? 0u : qa16, h ? 0u : (cvtpk(-ba, 0.f))}, qbx = {h ? 0u : qb16, h ? 0u : (cvtpk(-bb, 0.f))};
    u32x4 kB, vA0, vA1, vB0, vB1;
    u32x2 kBx = {0u, 0x3f80u};
    f32x16 zero16 = {}, sa, sb;
    u32x8 pa, pb = {};
    sa = MFMA16(kA, qam, zero16); sa = MFMA8(kAx, qax, sa);
    STAMP(1, 1);
    STAMP(1, 2);
    LDV(vB0, vB1, 0);
    unsigned one = 1u, dad = lds0 + LDS_FLAG + 4 * (F_DONE + w);
    PIN(one); PIN(dad);
#pragma unroll
    for (int s = 0; s < 8; ++s) {
      const unsigned off = (s % 3) * KV_SEG;
      STEP(kA, kAx, kB, kBx, vB0, vB1, vA0, vA1, off);
      STEP(kB, kBx, kA, kAx, vA0, vA1, vB0, vB1, off + KV_TILE);
      STEP(kA, kAx, kB, kBx, vB0, vB1, vA0, vA1, off + 2 * KV_TILE);
      unsigned full = 0;
      if (s < 7) full = ring_full(lds);
      STEP(kB, kBx, kA, kAx, vA0, vA1, vB0, vB1, off + 3 * KV_TILE);
      if (s < 5) asm volatile("s_mov_b64 exec, 1\n\tds_add_u32 %0, %1\n\ts_mov_b64 exec, -1" :: "v"(dad), "v"(one) : "memory");
      if (s < 7 && rfl(full) < s + 2) ring_wait(lds, s + 2);
    }
    ob = MFMA16(vB0, lo4(pb), ob);
    ob = MFMA16(vB1, hi4(pb), ob);
  } else {
    ma = -INFINITY; mb = -INFINITY;
    const u32x2 qax = {h ? 0u : qa16, 0u}, qbx = {h ? 0u : qb16, 0u};
    kAx[1] = 0u;
#pragma unroll 1
    for (int t = 0; t < 32; ++t) {
      if (t && (t & 3) == 0) ring_wait(lds, (t >> 2) + 1);
      const unsigned off = ((t >> 2) % 3) * KV_SEG + (t & 3) * KV_TILE;
      u32x4 kn, v0, v1; u32x2 knx = {0u, 0u};
      LDK(kn, knx, off); LDV(v0, v1, off);
      if ((t & 3) == 3) flag_set(lds, F_DONE + w, (t >> 2) + 1);
      slow_tile(kA, kAx, v0, v1, qam, qax, oa, ma);
      slow_tile(kA, kAx, v0, v1, qbm, qbx, ob, mb);
      kA = kn; kAx = knx;
    }
  }
  STAMP(1, 5);
  tail_part(oa, ob, ma, mb, w, rb, hg, u, dskip, ws, out, lds);
}

#if defined(__HIP_DEVICE_COMPILE__)
#pragma clang attribute pop
#endif
extern "C" void kernel_launch(void* const* d_in, const int* in_sizes, int n_in, void* d_out, int out_size, void* d_ws, size_t ws_size,
                              hipStream_t stream) {
  (void)hipFuncSetAttribute((const void*)fused_kernel, hipFuncAttributeMaxDynamicSharedMemorySize, K_LDS);
  fused_kernel<<<256, 1024, K_LDS, stream>>>((const u16*)d_in[0], (const u16*)d_in[1], (const u16*)d_in[2], (const u16*)d_in[3], (const u16*)d_in[4],
                                            (const u16*)d_in[5], (const u16*)d_in[6], (const u16*)d_in[7], (const u16*)d_in[8], (const u16*)d_in[9],
                                            (const u16*)d_in[10], (const u16*)d_in[11], (const u16*)d_in[12], (const u16*)d_in[13], (char*)d_ws,
                                            (u16*)d_out);
}
```
